```python
import torch

def _softmax(x, axis=-1):
    return torch.softmax(x, dim=axis)
def _gelu(x):
    return torch.nn.functional.gelu(x, approximate="tanh")
def _silu(x):
    return torch.nn.functional.silu(x)
def _sigmoid(x):
    return torch.sigmoid(x)
def _layernorm(x, axis=-1, eps=1e-5):
    m = x.mean(dim=axis, keepdim=True)
    v = x.var(dim=axis, keepdim=True, unbiased=False)
    return (x - m) / torch.sqrt(v + eps)
def _scatter_add(tgt, idx, src, axis=0):
    return tgt.clone().index_add_(axis, idx, src)
def _segment_sum(src, seg, n):
    out = torch.zeros((n,) + src.shape[1:], dtype=src.dtype)
    return out.index_add_(0, seg, src)
def _pad(x, pw):
    flat = tuple(v for pair in reversed(pw) for v in pair)
    return torch.nn.functional.pad(x, flat)

kernel_name = 'sequential_preLN__spectral_gfnet__geglu_ffn__prompt'

def setup_inputs():

    _w = (lambda a: (lambda t: t.to(torch.bfloat16) if t.is_floating_point() else t)(torch.as_tensor(a)))
    def _A_setup():
        B, S, D = 1, 2048, 512
        Df = D // 2 + 1
        return {
            "x": _w((lambda *s: torch.randn(*s, dtype=torch.bfloat16) * 0.05)(B, S, D)),
            "w_r": _w((lambda *s: torch.randn(*s, dtype=torch.bfloat16) * 0.4)(S, Df) * 0.02),
            "w_i": _w((lambda *s: torch.randn(*s, dtype=torch.bfloat16) * 0.4)(S, Df) * 0.02),
        }
    _A_in = _A_setup()

    def _B_setup():
        B, S, D, F = 1, 2048, 512, 1376
        return {
            "x": _w((lambda *s: torch.randn(*s, dtype=torch.bfloat16) * 0.05)(B, S, D)),
            "w_gate": _w((lambda *s: torch.randn(*s, dtype=torch.bfloat16) * 0.05)(D, F)),
            "w_up": _w((lambda *s: torch.randn(*s, dtype=torch.bfloat16) * 0.05)(D, F)),
            "w_down": _w((lambda *s: torch.randn(*s, dtype=torch.bfloat16) * 0.05)(F, D)),
        }
    _B_in = _B_setup()

    return {
        "A_x": _A_in["x"],
        "A_w_r": _A_in["w_r"],
        "A_w_i": _A_in["w_i"],
        "B_x": _B_in["x"],
        "B_w_gate": _B_in["w_gate"],
        "B_w_up": _B_in["w_up"],
        "B_w_down": _B_in["w_down"],
    }

def reference(A_x, A_w_r, A_w_i, B_x, B_w_gate, B_w_up, B_w_down):
    def _A_ref(x, w_r, w_i):
        B, S, D = x.shape
        X = (lambda x, axes=(-2,-1): torch.fft.rfft2(x.float(), dim=axes))(x, axes=(-2, -1))
        W = w_r + 1j * w_i
        X = X * W
        y = (lambda x, s, axes=(-2,-1): torch.fft.irfft2(x, s=s, dim=axes).to(torch.bfloat16))(X, (S, D), axes=(-2, -1))
        return y

    def _B_ref(x, w_gate, w_up, w_down):
        return (_gelu(x @ w_gate) * (x @ w_up)) @ w_down

    x = A_x
    h = x + _A_ref(_layernorm(x, axis=-1), A_w_r, A_w_i)
    h2 = h + _B_ref(_layernorm(h, axis=-1), B_w_gate, B_w_up, B_w_down)
    return h2
```

```cpp
#include <hip/hip_runtime.h>
#include <hip/hip_bf16.h>
#include <stdint.h>

typedef __attribute__((ext_vector_type(8))) short bf16x8;
typedef __attribute__((ext_vector_type(4))) float f32x4;
typedef unsigned short u16;

#define S_LEN 2048
#define D_DIM 512
#define F_DIM 1376
#define DF 257
#define NT16 86
#define KS2 43

__device__ const float2 g_tw[2048] = {
{OPQ,OPQ}, {OPQ,OPQ}, {OPQ,OPQ}, {OPQ,OPQ},
{OPQ,OPQ}, {OPQ,OPQ}, {OPQ,OPQ}, {OPQ,OPQ},
{OPQ,OPQ}, {OPQ,OPQ}, {OPQ,OPQ}, {OPQ,OPQ},
{OPQ,OPQ}, {OPQ,OPQ}, {OPQ,OPQ}, {OPQ,OPQ},
{OPQ,OPQ}, {OPQ,OPQ}, {OPQ,OPQ}, {OPQ,OPQ},
{OPQ,OPQ}, {OPQ,OPQ}, {OPQ,OPQ}, {OPQ,OPQ},
{OPQ,OPQ}, {OPQ,OPQ}, {OPQ,OPQ}, {OPQ,OPQ},
{OPQ,OPQ}, {OPQ,OPQ}, {OPQ,OPQ}, {OPQ,OPQ},
{OPQ,OPQ}, {OPQ,OPQ}, {OPQ,OPQ}, {OPQ,OPQ},
{OPQ,OPQ}, {OPQ,OPQ}, {OPQ,OPQ}, {OPQ,OPQ},
{OPQ,OPQ}, {OPQ,OPQ}, {OPQ,OPQ}, {OPQ,OPQ},
{OPQ,OPQ}, {OPQ,OPQ}, {OPQ,OPQ}, {OPQ,OPQ},
{OPQ,OPQ}, {OPQ,OPQ}, {OPQ,OPQ}, {OPQ,OPQ},
{OPQ,OPQ}, {OPQ,OPQ}, {OPQ,OPQ}, {OPQ,OPQ},
{OPQ,OPQ}, {OPQ,OPQ}, {OPQ,OPQ}, {OPQ,OPQ},
{OPQ,OPQ}, {OPQ,OPQ}, {OPQ,OPQ}, {OPQ,OPQ},
{OPQ,OPQ}, {OPQ,OPQ}, {OPQ,OPQ}, {OPQ,OPQ},
{OPQ,OPQ}, {OPQ,OPQ}, {OPQ,OPQ}, {OPQ,OPQ},
{OPQ,OPQ}, {OPQ,OPQ}, {OPQ,OPQ}, {OPQ,OPQ},
{OPQ,OPQ}, {OPQ,OPQ}, {OPQ,OPQ}, {OPQ,OPQ},
{OPQ,OPQ}, {OPQ,OPQ}, {OPQ,OPQ}, {OPQ,OPQ},
{OPQ,OPQ}, {OPQ,OPQ}, {OPQ,OPQ}, {OPQ,OPQ},
{OPQ,OPQ}, {OPQ,OPQ}, {OPQ,OPQ}, {OPQ,OPQ},
{OPQ,OPQ}, {OPQ,OPQ}, {OPQ,OPQ}, {OPQ,OPQ},
{OPQ,OPQ}, {OPQ,OPQ}, {OPQ,OPQ}, {OPQ,OPQ},
{OPQ,OPQ}, {OPQ,OPQ}, {OPQ,OPQ}, {OPQ,OPQ},
{OPQ,OPQ}, {OPQ,OPQ}, {OPQ,OPQ}, {OPQ,OPQ},
{OPQ,OPQ}, {OPQ,OPQ}, {OPQ,OPQ}, {OPQ,OPQ},
{OPQ,OPQ}, {OPQ,OPQ}, {OPQ,OPQ}, {OPQ,OPQ},
{OPQ,OPQ}, {OPQ,OPQ}, {OPQ,OPQ}, {OPQ,OPQ},
{OPQ,OPQ}, {OPQ,OPQ}, {OPQ,OPQ}, {OPQ,OPQ},
{OPQ,OPQ}, {OPQ,OPQ}, {OPQ,OPQ}, {OPQ,OPQ},
{OPQ,OPQ}, {OPQ,OPQ}, {OPQ,OPQ}, {OPQ,OPQ},
{OPQ,OPQ}, {OPQ,OPQ}, {OPQ,OPQ}, {OPQ,OPQ},
{OPQ,OPQ}, {OPQ,OPQ}, {OPQ,OPQ}, {OPQ,OPQ},
{OPQ,OPQ}, {OPQ,OPQ}, {OPQ,OPQ}, {OPQ,OPQ},
{OPQ,OPQ}, {OPQ,OPQ}, {OPQ,OPQ}, {OPQ,OPQ},
{OPQ,OPQ}, {OPQ,OPQ}, {OPQ,OPQ}, {OPQ,OPQ},
{OPQ,OPQ}, {OPQ,OPQ}, {OPQ,OPQ}, {OPQ,OPQ},
{OPQ,OPQ}, {OPQ,OPQ}, {OPQ,OPQ}, {OPQ,OPQ},
{OPQ,OPQ}, {OPQ,OPQ}, {OPQ,OPQ}, {OPQ,OPQ},
{OPQ,OPQ}, {OPQ,OPQ}, {OPQ,OPQ}, {OPQ,OPQ},
{OPQ,OPQ}, {OPQ,OPQ}, {OPQ,OPQ}, {OPQ,OPQ},
{OPQ,OPQ}, {OPQ,OPQ}, {OPQ,OPQ}, {OPQ,OPQ},
{OPQ,OPQ}, {OPQ,OPQ}, {OPQ,OPQ}, {OPQ,OPQ},
{OPQ,OPQ}, {OPQ,OPQ}, {OPQ,OPQ}, {OPQ,OPQ},
{OPQ,OPQ}, {OPQ,OPQ}, {OPQ,OPQ}, {OPQ,OPQ},
{OPQ,OPQ}, {OPQ,OPQ}, {OPQ,OPQ}, {OPQ,OPQ},
{OPQ,OPQ}, {OPQ,OPQ}, {OPQ,OPQ}, {OPQ,OPQ},
{OPQ,OPQ}, {OPQ,OPQ}, {OPQ,OPQ}, {OPQ,OPQ},
{OPQ,OPQ}, {OPQ,OPQ}, {OPQ,OPQ}, {OPQ,OPQ},
{OPQ,OPQ}, {OPQ,OPQ}, {OPQ,OPQ}, {OPQ,OPQ},
{OPQ,OPQ}, {OPQ,OPQ}, {OPQ,OPQ}, {OPQ,OPQ},
{OPQ,OPQ}, {OPQ,OPQ}, {OPQ,OPQ}, {OPQ,OPQ},
{OPQ,OPQ}, {OPQ,OPQ}, {OPQ,OPQ}, {OPQ,OPQ},
{OPQ,OPQ}, {OPQ,OPQ}, {OPQ,OPQ}, {OPQ,OPQ},
{OPQ,OPQ}, {OPQ,OPQ}, {OPQ,OPQ}, {OPQ,OPQ},
{OPQ,OPQ}, {OPQ,OPQ}, {OPQ,OPQ}, {OPQ,OPQ},
{OPQ,OPQ}, {OPQ,OPQ}, {OPQ,OPQ}, {OPQ,OPQ},
{OPQ,OPQ}, {OPQ,OPQ}, {OPQ,OPQ}, {OPQ,OPQ},
{OPQ,OPQ}, {OPQ,OPQ}, {OPQ,OPQ}, {OPQ,OPQ},
{OPQ,OPQ}, {OPQ,OPQ}, {OPQ,OPQ}, {OPQ,OPQ},
{OPQ,OPQ}, {OPQ,OPQ}, {OPQ,OPQ}, {OPQ,OPQ},
{OPQ,OPQ}, {OPQ,OPQ}, {OPQ,OPQ}, {OPQ,OPQ},
{OPQ,OPQ}, {OPQ,OPQ}, {OPQ,OPQ}, {OPQ,OPQ},
{OPQ,OPQ}, {OPQ,OPQ}, {OPQ,OPQ}, {OPQ,OPQ},
{OPQ,OPQ}, {OPQ,OPQ}, {OPQ,OPQ}, {OPQ,OPQ},
{OPQ,OPQ}, {OPQ,OPQ}, {OPQ,OPQ}, {OPQ,OPQ},
{OPQ,OPQ}, {OPQ,OPQ}, {OPQ,OPQ}, {OPQ,OPQ},
{OPQ,OPQ}, {OPQ,OPQ}, {OPQ,OPQ}, {OPQ,OPQ},
{OPQ,OPQ}, {OPQ,OPQ}, {OPQ,OPQ}, {OPQ,OPQ},
{OPQ,OPQ}, {OPQ,OPQ}, {OPQ,OPQ}, {OPQ,OPQ},
{OPQ,OPQ}, {OPQ,OPQ}, {OPQ,OPQ}, {OPQ,OPQ},
{OPQ,OPQ}, {OPQ,OPQ}, {OPQ,OPQ}, {OPQ,OPQ},
{OPQ,OPQ}, {OPQ,OPQ}, {OPQ,OPQ}, {OPQ,OPQ},
{OPQ,OPQ}, {OPQ,OPQ}, {OPQ,OPQ}, {OPQ,OPQ},
{OPQ,OPQ}, {OPQ,OPQ}, {OPQ,OPQ}, {OPQ,OPQ},
{OPQ,OPQ}, {OPQ,OPQ}, {OPQ,OPQ}, {OPQ,OPQ},
{OPQ,OPQ}, {OPQ,OPQ}, {OPQ,OPQ}, {OPQ,OPQ},
{OPQ,OPQ}, {OPQ,OPQ}, {OPQ,OPQ}, {OPQ,OPQ},
{OPQ,OPQ}, {OPQ,OPQ}, {OPQ,OPQ}, {OPQ,OPQ},
{OPQ,OPQ}, {OPQ,OPQ}, {OPQ,OPQ}, {OPQ,OPQ},
{OPQ,OPQ}, {OPQ,OPQ}, {OPQ,OPQ}, {OPQ,OPQ},
{OPQ,OPQ}, {OPQ,OPQ}, {OPQ,OPQ}, {OPQ,OPQ},
{OPQ,OPQ}, {OPQ,OPQ}, {OPQ,OPQ}, {OPQ,OPQ},
{OPQ,OPQ}, {OPQ,OPQ}, {OPQ,OPQ}, {OPQ,OPQ},
{OPQ,OPQ}, {OPQ,OPQ}, {OPQ,OPQ}, {OPQ,OPQ},
{OPQ,OPQ}, {OPQ,OPQ}, {OPQ,OPQ}, {OPQ,OPQ},
{OPQ,OPQ}, {OPQ,OPQ}, {OPQ,OPQ}, {OPQ,OPQ},
{OPQ,OPQ}, {OPQ,OPQ}, {OPQ,OPQ}, {OPQ,OPQ},
{OPQ,OPQ}, {OPQ,OPQ}, {OPQ,OPQ}, {OPQ,OPQ},
{OPQ,OPQ}, {OPQ,OPQ}, {OPQ,OPQ}, {OPQ,OPQ},
{OPQ,OPQ}, {OPQ,OPQ}, {OPQ,OPQ}, {OPQ,OPQ},
{OPQ,OPQ}, {OPQ,OPQ}, {OPQ,OPQ}, {OPQ,OPQ},
{OPQ,OPQ}, {OPQ,OPQ}, {OPQ,OPQ}, {OPQ,OPQ},
{OPQ,OPQ}, {OPQ,OPQ}, {OPQ,OPQ}, {OPQ,OPQ},
{OPQ,OPQ}, {OPQ,OPQ}, {OPQ,OPQ}, {OPQ,OPQ},
{OPQ,OPQ}, {OPQ,OPQ}, {OPQ,OPQ}, {OPQ,OPQ},
{OPQ,OPQ}, {OPQ,OPQ}, {OPQ,OPQ}, {OPQ,OPQ},
{OPQ,OPQ}, {OPQ,OPQ}, {OPQ,OPQ}, {OPQ,OPQ},
{OPQ,OPQ}, {OPQ,OPQ}, {OPQ,OPQ}, {OPQ,OPQ},
{OPQ,OPQ}, {OPQ,OPQ}, {OPQ,OPQ}, {OPQ,OPQ},
{OPQ,OPQ}, {OPQ,OPQ}, {OPQ,OPQ}, {OPQ,OPQ},
{OPQ,OPQ}, {OPQ,OPQ}, {OPQ,OPQ}, {OPQ,OPQ},
{OPQ,OPQ}, {OPQ,OPQ}, {OPQ,OPQ}, {OPQ,OPQ},
{OPQ,OPQ}, {OPQ,OPQ}, {OPQ,OPQ}, {OPQ,OPQ},
{OPQ,OPQ}, {OPQ,OPQ}, {OPQ,OPQ}, {OPQ,OPQ},
{OPQ,OPQ}, {OPQ,OPQ}, {OPQ,OPQ}, {OPQ,OPQ},
{OPQ,OPQ}, {OPQ,OPQ}, {OPQ,OPQ}, {OPQ,OPQ},
{OPQ,OPQ}, {OPQ,OPQ}, {OPQ,OPQ}, {OPQ,OPQ},
{OPQ,OPQ}, {OPQ,OPQ}, {OPQ,OPQ}, {OPQ,OPQ},
{OPQ,OPQ}, {OPQ,OPQ}, {OPQ,OPQ}, {OPQ,OPQ},
{OPQ,OPQ}, {OPQ,OPQ}, {OPQ,OPQ}, {OPQ,OPQ},
{OPQ,OPQ}, {OPQ,OPQ}, {OPQ,OPQ}, {OPQ,OPQ},
{OPQ,OPQ}, {OPQ,OPQ}, {OPQ,OPQ}, {OPQ,OPQ},
{OPQ,OPQ}, {OPQ,OPQ}, {OPQ,OPQ}, {OPQ,OPQ},
{OPQ,OPQ}, {OPQ,OPQ}, {OPQ,OPQ}, {OPQ,OPQ},
{OPQ,OPQ}, {OPQ,OPQ}, {OPQ,OPQ}, {OPQ,OPQ},
{OPQ,OPQ}, {OPQ,OPQ}, {OPQ,OPQ}, {OPQ,OPQ},
{OPQ,OPQ}, {OPQ,OPQ}, {OPQ,OPQ}, {OPQ,OPQ},
{OPQ,OPQ}, {OPQ,OPQ}, {OPQ,OPQ}, {OPQ,OPQ},
{OPQ,OPQ}, {OPQ,OPQ}, {OPQ,OPQ}, {OPQ,OPQ},
{OPQ,OPQ}, {OPQ,OPQ}, {OPQ,OPQ}, {OPQ,OPQ},
{OPQ,OPQ}, {OPQ,OPQ}, {OPQ,OPQ}, {OPQ,OPQ},
{OPQ,OPQ}, {OPQ,OPQ}, {OPQ,OPQ}, {OPQ,OPQ},
{OPQ,OPQ}, {OPQ,OPQ}, {OPQ,OPQ}, {OPQ,OPQ},
{OPQ,OPQ}, {OPQ,OPQ}, {OPQ,OPQ}, {OPQ,OPQ},
{OPQ,OPQ}, {OPQ,OPQ}, {OPQ,OPQ}, {OPQ,OPQ},
{OPQ,OPQ}, {OPQ,OPQ}, {OPQ,OPQ}, {OPQ,OPQ},
{OPQ,OPQ}, {OPQ,OPQ}, {OPQ,OPQ}, {OPQ,OPQ},
{OPQ,OPQ}, {OPQ,OPQ}, {OPQ,OPQ}, {OPQ,OPQ},
{OPQ,OPQ}, {OPQ,OPQ}, {OPQ,OPQ}, {OPQ,OPQ},
{OPQ,OPQ}, {OPQ,OPQ}, {OPQ,OPQ}, {OPQ,OPQ},
{OPQ,OPQ}, {OPQ,OPQ}, {OPQ,OPQ}, {OPQ,OPQ},
{OPQ,OPQ}, {OPQ,OPQ}, {OPQ,OPQ}, {OPQ,OPQ},
{OPQ,OPQ}, {OPQ,OPQ}, {OPQ,OPQ}, {OPQ,OPQ},
{OPQ,OPQ}, {OPQ,OPQ}, {OPQ,OPQ}, {OPQ,OPQ},
{OPQ,OPQ}, {OPQ,OPQ}, {OPQ,OPQ}, {OPQ,OPQ},
{OPQ,OPQ}, {OPQ,OPQ}, {OPQ,OPQ}, {OPQ,OPQ},
{OPQ,OPQ}, {OPQ,OPQ}, {OPQ,OPQ}, {OPQ,OPQ},
{OPQ,OPQ}, {OPQ,OPQ}, {OPQ,OPQ}, {OPQ,OPQ},
{OPQ,OPQ}, {OPQ,OPQ}, {OPQ,OPQ}, {OPQ,OPQ},
{OPQ,OPQ}, {OPQ,OPQ}, {OPQ,OPQ}, {OPQ,OPQ},
{OPQ,OPQ}, {OPQ,OPQ}, {OPQ,OPQ}, {OPQ,OPQ},
{OPQ,OPQ}, {OPQ,OPQ}, {OPQ,OPQ}, {OPQ,OPQ},
{OPQ,OPQ}, {OPQ,OPQ}, {OPQ,OPQ}, {OPQ,OPQ},
{OPQ,OPQ}, {OPQ,OPQ}, {OPQ,OPQ}, {OPQ,OPQ},
{OPQ,OPQ}, {OPQ,OPQ}, {OPQ,OPQ}, {OPQ,OPQ},
{OPQ,OPQ}, {OPQ,OPQ}, {OPQ,OPQ}, {OPQ,OPQ},
{OPQ,OPQ}, {OPQ,OPQ}, {OPQ,OPQ}, {OPQ,OPQ},
{OPQ,OPQ}, {OPQ,OPQ}, {OPQ,OPQ}, {OPQ,OPQ},
{OPQ,OPQ}, {OPQ,OPQ}, {OPQ,OPQ}, {OPQ,OPQ},
{OPQ,OPQ}, {OPQ,OPQ}, {OPQ,OPQ}, {OPQ,OPQ},
{OPQ,OPQ}, {OPQ,OPQ}, {OPQ,OPQ}, {OPQ,OPQ},
{OPQ,OPQ}, {OPQ,OPQ}, {OPQ,OPQ}, {OPQ,OPQ},
{OPQ,OPQ}, {OPQ,OPQ}, {OPQ,OPQ}, {OPQ,OPQ},
{OPQ,OPQ}, {OPQ,OPQ}, {OPQ,OPQ}, {OPQ,OPQ},
{OPQ,OPQ}, {OPQ,OPQ}, {OPQ,OPQ}, {OPQ,OPQ},
{OPQ,OPQ}, {OPQ,OPQ}, {OPQ,OPQ}, {OPQ,OPQ},
{OPQ,OPQ}, {OPQ,OPQ}, {OPQ,OPQ}, {OPQ,OPQ},
{OPQ,OPQ}, {OPQ,OPQ}, {OPQ,OPQ}, {OPQ,OPQ},
{OPQ,OPQ}, {OPQ,OPQ}, {OPQ,OPQ}, {OPQ,OPQ},
{OPQ,OPQ}, {OPQ,OPQ}, {OPQ,OPQ}, {OPQ,OPQ},
{OPQ,OPQ}, {OPQ,OPQ}, {OPQ,OPQ}, {OPQ,OPQ},
{OPQ,OPQ}, {OPQ,OPQ}, {OPQ,OPQ}, {OPQ,OPQ},
{OPQ,OPQ}, {OPQ,OPQ}, {OPQ,OPQ}, {OPQ,OPQ},
{OPQ,OPQ}, {OPQ,OPQ}, {OPQ,OPQ}, {OPQ,OPQ},
{OPQ,OPQ}, {OPQ,OPQ}, {OPQ,OPQ}, {OPQ,OPQ},
{OPQ,OPQ}, {OPQ,OPQ}, {OPQ,OPQ}, {OPQ,OPQ},
{OPQ,OPQ}, {OPQ,OPQ}, {OPQ,OPQ}, {OPQ,OPQ},
{OPQ,OPQ}, {OPQ,OPQ}, {OPQ,OPQ}, {OPQ,OPQ},
{OPQ,OPQ}, {OPQ,OPQ}, {OPQ,OPQ}, {OPQ,OPQ},
{OPQ,OPQ}, {OPQ,OPQ}, {OPQ,OPQ}, {OPQ,OPQ},
{OPQ,OPQ}, {OPQ,OPQ}, {OPQ,OPQ}, {OPQ,OPQ},
{OPQ,OPQ}, {OPQ,OPQ}, {OPQ,OPQ}, {OPQ,OPQ},
{OPQ,OPQ}, {OPQ,OPQ}, {OPQ,OPQ}, {OPQ,OPQ},
{OPQ,OPQ}, {OPQ,OPQ}, {OPQ,OPQ}, {OPQ,OPQ},
{OPQ,OPQ}, {OPQ,OPQ}, {OPQ,OPQ}, {OPQ,OPQ},
{OPQ,OPQ}, {OPQ,OPQ}, {OPQ,OPQ}, {OPQ,OPQ},
{OPQ,OPQ}, {OPQ,OPQ}, {OPQ,OPQ}, {OPQ,OPQ},
{OPQ,OPQ}, {OPQ,OPQ}, {OPQ,OPQ}, {OPQ,OPQ},
{OPQ,OPQ}, {OPQ,OPQ}, {OPQ,OPQ}, {OPQ,OPQ},
{OPQ,OPQ}, {OPQ,OPQ}, {OPQ,OPQ}, {OPQ,OPQ},
{OPQ,OPQ}, {OPQ,OPQ}, {OPQ,OPQ}, {OPQ,OPQ},
{OPQ,OPQ}, {OPQ,OPQ}, {OPQ,OPQ}, {OPQ,OPQ},
{OPQ,OPQ}, {OPQ,OPQ}, {OPQ,OPQ}, {OPQ,OPQ},
{OPQ,OPQ}, {OPQ,OPQ}, {OPQ,OPQ}, {OPQ,OPQ},
{OPQ,OPQ}, {OPQ,OPQ}, {OPQ,OPQ}, {OPQ,OPQ},
{OPQ,OPQ}, {OPQ,OPQ}, {OPQ,OPQ}, {OPQ,OPQ},
{OPQ,OPQ}, {OPQ,OPQ}, {OPQ,OPQ}, {OPQ,OPQ},
{OPQ,OPQ}, {OPQ,OPQ}, {OPQ,OPQ}, {OPQ,OPQ},
{OPQ,OPQ}, {OPQ,OPQ}, {OPQ,OPQ}, {OPQ,OPQ},
{OPQ,OPQ}, {OPQ,OPQ}, {OPQ,OPQ}, {OPQ,OPQ},
{OPQ,OPQ}, {OPQ,OPQ}, {OPQ,OPQ}, {OPQ,OPQ},
{OPQ,OPQ}, {OPQ,OPQ}, {OPQ,OPQ}, {OPQ,OPQ},
{OPQ,OPQ}, {OPQ,OPQ}, {OPQ,OPQ}, {OPQ,OPQ},
{OPQ,OPQ}, {OPQ,OPQ}, {OPQ,OPQ}, {OPQ,OPQ},
{OPQ,OPQ}, {OPQ,OPQ}, {OPQ,OPQ}, {OPQ,OPQ},
{OPQ,OPQ}, {OPQ,OPQ}, {OPQ,OPQ}, {OPQ,OPQ},
{OPQ,OPQ}, {OPQ,OPQ}, {OPQ,OPQ}, {OPQ,OPQ},
{OPQ,OPQ}, {OPQ,OPQ}, {OPQ,OPQ}, {OPQ,OPQ},
{OPQ,OPQ}, {OPQ,OPQ}, {OPQ,OPQ}, {OPQ,OPQ},
{OPQ,OPQ}, {OPQ,OPQ}, {OPQ,OPQ}, {OPQ,OPQ},
{OPQ,OPQ}, {OPQ,OPQ}, {OPQ,OPQ}, {OPQ,OPQ},
{OPQ,OPQ}, {OPQ,OPQ}, {OPQ,OPQ}, {OPQ,OPQ},
{OPQ,OPQ}, {OPQ,OPQ}, {OPQ,OPQ}, {OPQ,OPQ},
{OPQ,OPQ}, {OPQ,OPQ}, {OPQ,OPQ}, {OPQ,OPQ},
{OPQ,OPQ}, {OPQ,OPQ}, {OPQ,OPQ}, {OPQ,OPQ},
{OPQ,OPQ}, {OPQ,OPQ}, {OPQ,OPQ}, {OPQ,OPQ},
{OPQ,OPQ}, {OPQ,OPQ}, {OPQ,OPQ}, {OPQ,OPQ},
{OPQ,OPQ}, {OPQ,OPQ}, {OPQ,OPQ}, {OPQ,OPQ},
{OPQ,OPQ}, {OPQ,OPQ}, {OPQ,OPQ}, {OPQ,OPQ},
{OPQ,OPQ}, {OPQ,OPQ}, {OPQ,OPQ}, {OPQ,OPQ},
{OPQ,OPQ}, {OPQ,OPQ}, {OPQ,OPQ}, {OPQ,OPQ},
{OPQ,OPQ}, {OPQ,OPQ}, {OPQ,OPQ}, {OPQ,OPQ},
{OPQ,OPQ}, {OPQ,OPQ}, {OPQ,OPQ}, {OPQ,OPQ},
{OPQ,OPQ}, {OPQ,OPQ}, {OPQ,OPQ}, {OPQ,OPQ},
{OPQ,OPQ}, {OPQ,OPQ}, {OPQ,OPQ}, {OPQ,OPQ},
{OPQ,OPQ}, {OPQ,OPQ}, {OPQ,OPQ}, {OPQ,OPQ},
{OPQ,OPQ}, {OPQ,OPQ}, {OPQ,OPQ}, {OPQ,OPQ},
{OPQ,OPQ}, {OPQ,OPQ}, {OPQ,OPQ}, {OPQ,OPQ},
{OPQ,OPQ}, {OPQ,OPQ}, {OPQ,OPQ}, {OPQ,OPQ},
{OPQ,OPQ}, {OPQ,OPQ}, {OPQ,OPQ}, {OPQ,OPQ},
{OPQ,OPQ}, {OPQ,OPQ}, {OPQ,OPQ}, {OPQ,OPQ},
{OPQ,OPQ}, {OPQ,OPQ}, {OPQ,OPQ}, {OPQ,OPQ},
{OPQ,OPQ}, {OPQ,OPQ}, {OPQ,OPQ}, {OPQ,OPQ},
{OPQ,OPQ}, {OPQ,OPQ}, {OPQ,OPQ}, {OPQ,OPQ},
{OPQ,OPQ}, {OPQ,OPQ}, {OPQ,OPQ}, {OPQ,OPQ},
{OPQ,OPQ}, {OPQ,OPQ}, {OPQ,OPQ}, {OPQ,OPQ},
{OPQ,OPQ}, {OPQ,OPQ}, {OPQ,OPQ}, {OPQ,OPQ},
{OPQ,OPQ}, {OPQ,OPQ}, {OPQ,OPQ}, {OPQ,OPQ},
{OPQ,OPQ}, {OPQ,OPQ}, {OPQ,OPQ}, {OPQ,OPQ},
{OPQ,OPQ}, {OPQ,OPQ}, {OPQ,OPQ}, {OPQ,OPQ},
{OPQ,OPQ}, {OPQ,OPQ}, {OPQ,OPQ}, {OPQ,OPQ},
{OPQ,OPQ}, {OPQ,OPQ}, {OPQ,OPQ}, {OPQ,OPQ},
{OPQ,OPQ}, {OPQ,OPQ}, {OPQ,OPQ}, {OPQ,OPQ},
{OPQ,OPQ}, {OPQ,OPQ}, {OPQ,OPQ}, {OPQ,OPQ},
{OPQ,OPQ}, {OPQ,OPQ}, {OPQ,OPQ}, {OPQ,OPQ},
{OPQ,OPQ}, {OPQ,OPQ}, {OPQ,OPQ}, {OPQ,OPQ},
{OPQ,OPQ}, {OPQ,OPQ}, {OPQ,OPQ}, {OPQ,OPQ},
{OPQ,OPQ}, {OPQ,OPQ}, {OPQ,OPQ}, {OPQ,OPQ},
{OPQ,OPQ}, {OPQ,OPQ}, {OPQ,OPQ}, {OPQ,OPQ},
{OPQ,OPQ}, {OPQ,OPQ}, {OPQ,OPQ}, {OPQ,OPQ},
{OPQ,OPQ}, {OPQ,OPQ}, {OPQ,OPQ}, {OPQ,OPQ},
{OPQ,OPQ}, {OPQ,OPQ}, {OPQ,OPQ}, {OPQ,OPQ},
{OPQ,OPQ}, {OPQ,OPQ}, {OPQ,OPQ}, {OPQ,OPQ},
{OPQ,OPQ}, {OPQ,OPQ}, {OPQ,OPQ}, {OPQ,OPQ},
{OPQ,OPQ}, {OPQ,OPQ}, {OPQ,OPQ}, {OPQ,OPQ},
{OPQ,OPQ}, {OPQ,OPQ}, {OPQ,OPQ}, {OPQ,OPQ},
{OPQ,OPQ}, {OPQ,OPQ}, {OPQ,OPQ}, {OPQ,OPQ},
{OPQ,OPQ}, {OPQ,OPQ}, {OPQ,OPQ}, {OPQ,OPQ},
{OPQ,OPQ}, {OPQ,OPQ}, {OPQ,OPQ}, {OPQ,OPQ},
{OPQ,OPQ}, {OPQ,OPQ}, {OPQ,OPQ}, {OPQ,OPQ},
{OPQ,OPQ}, {OPQ,OPQ}, {OPQ,OPQ}, {OPQ,OPQ},
{OPQ,OPQ}, {OPQ,OPQ}, {OPQ,OPQ}, {OPQ,OPQ},
{OPQ,OPQ}, {OPQ,OPQ}, {OPQ,OPQ}, {OPQ,OPQ},
{OPQ,OPQ}, {OPQ,OPQ}, {OPQ,OPQ}, {OPQ,OPQ},
{OPQ,OPQ}, {OPQ,OPQ}, {OPQ,OPQ}, {OPQ,OPQ},
{OPQ,OPQ}, {OPQ,OPQ}, {OPQ,OPQ}, {OPQ,OPQ},
{OPQ,OPQ}, {OPQ,OPQ}, {OPQ,OPQ}, {OPQ,OPQ},
{OPQ,OPQ}, {OPQ,OPQ}, {OPQ,OPQ}, {OPQ,OPQ},
{OPQ,OPQ}, {OPQ,OPQ}, {OPQ,OPQ}, {OPQ,OPQ},
{OPQ,OPQ}, {OPQ,OPQ}, {OPQ,OPQ}, {OPQ,OPQ},
{OPQ,OPQ}, {OPQ,OPQ}, {OPQ,OPQ}, {OPQ,OPQ},
{OPQ,OPQ}, {OPQ,OPQ}, {OPQ,OPQ}, {OPQ,OPQ},
{OPQ,OPQ}, {OPQ,OPQ}, {OPQ,OPQ}, {OPQ,OPQ},
{OPQ,OPQ}, {OPQ,OPQ}, {OPQ,OPQ}, {OPQ,OPQ},
{OPQ,OPQ}, {OPQ,OPQ}, {OPQ,OPQ}, {OPQ,OPQ},
{OPQ,OPQ}, {OPQ,OPQ}, {OPQ,OPQ}, {OPQ,OPQ},
{OPQ,OPQ}, {OPQ,OPQ}, {OPQ,OPQ}, {OPQ,OPQ},
{OPQ,OPQ}, {OPQ,OPQ}, {OPQ,OPQ}, {OPQ,OPQ},
{OPQ,OPQ}, {OPQ,OPQ}, {OPQ,OPQ}, {OPQ,OPQ},
{OPQ,OPQ}, {OPQ,OPQ}, {OPQ,OPQ}, {OPQ,OPQ},
{OPQ,OPQ}, {OPQ,OPQ}, {OPQ,OPQ}, {OPQ,OPQ},
{OPQ,OPQ}, {OPQ,OPQ}, {OPQ,OPQ}, {OPQ,OPQ},
{OPQ,OPQ}, {OPQ,OPQ}, {OPQ,OPQ}, {OPQ,OPQ},
{OPQ,OPQ}, {OPQ,OPQ}, {OPQ,OPQ}, {OPQ,OPQ},
{OPQ,OPQ}, {OPQ,OPQ}, {OPQ,OPQ}, {OPQ,OPQ},
{OPQ,OPQ}, {OPQ,OPQ}, {OPQ,OPQ}, {OPQ,OPQ},
{OPQ,OPQ}, {OPQ,OPQ}, {OPQ,OPQ}, {OPQ,OPQ},
{OPQ,OPQ}, {OPQ,OPQ}, {OPQ,OPQ}, {OPQ,OPQ},
{OPQ,OPQ}, {OPQ,OPQ}, {OPQ,OPQ}, {OPQ,OPQ},
{OPQ,OPQ}, {OPQ,OPQ}, {OPQ,OPQ}, {OPQ,OPQ},
{OPQ,OPQ}, {OPQ,OPQ}, {OPQ,OPQ}, {OPQ,OPQ},
{OPQ,OPQ}, {OPQ,OPQ}, {OPQ,OPQ}, {OPQ,OPQ},
{OPQ,OPQ}, {OPQ,OPQ}, {OPQ,OPQ}, {OPQ,OPQ},
{OPQ,OPQ}, {OPQ,OPQ}, {OPQ,OPQ}, {OPQ,OPQ},
{OPQ,OPQ}, {OPQ,OPQ}, {OPQ,OPQ}, {OPQ,OPQ},
{OPQ,OPQ}, {OPQ,OPQ}, {OPQ,OPQ}, {OPQ,OPQ},
{OPQ,OPQ}, {OPQ,OPQ}, {OPQ,OPQ}, {OPQ,OPQ},
{OPQ,OPQ}, {OPQ,OPQ}, {OPQ,OPQ}, {OPQ,OPQ},
{OPQ,OPQ}, {OPQ,OPQ}, {OPQ,OPQ}, {OPQ,OPQ},
{OPQ,OPQ}, {OPQ,OPQ}, {OPQ,OPQ}, {OPQ,OPQ},
{OPQ,OPQ}, {OPQ,OPQ}, {OPQ,OPQ}, {OPQ,OPQ},
{OPQ,OPQ}, {OPQ,OPQ}, {OPQ,OPQ}, {OPQ,OPQ},
{OPQ,OPQ}, {OPQ,OPQ}, {OPQ,OPQ}, {OPQ,OPQ},
{OPQ,OPQ}, {OPQ,OPQ}, {OPQ,OPQ}, {OPQ,OPQ},
{OPQ,OPQ}, {OPQ,OPQ}, {OPQ,OPQ}, {OPQ,OPQ},
{OPQ,OPQ}, {OPQ,OPQ}, {OPQ,OPQ}, {OPQ,OPQ},
{OPQ,OPQ}, {OPQ,OPQ}, {OPQ,OPQ}, {OPQ,OPQ},
{OPQ,OPQ}, {OPQ,OPQ}, {OPQ,OPQ}, {OPQ,OPQ},
{OPQ,OPQ}, {OPQ,OPQ}, {OPQ,OPQ}, {OPQ,OPQ},
{OPQ,OPQ}, {OPQ,OPQ}, {OPQ,OPQ}, {OPQ,OPQ},
{OPQ,OPQ}, {OPQ,OPQ}, {OPQ,OPQ}, {OPQ,OPQ},
{OPQ,OPQ}, {OPQ,OPQ}, {OPQ,OPQ}, {OPQ,OPQ},
{OPQ,OPQ}, {OPQ,OPQ}, {OPQ,OPQ}, {OPQ,OPQ},
{OPQ,OPQ}, {OPQ,OPQ}, {OPQ,OPQ}, {OPQ,OPQ},
{OPQ,OPQ}, {OPQ,OPQ}, {OPQ,OPQ}, {OPQ,OPQ},
{OPQ,OPQ}, {OPQ,OPQ}, {OPQ,OPQ}, {OPQ,OPQ},
{OPQ,OPQ}, {OPQ,OPQ}, {OPQ,OPQ}, {OPQ,OPQ},
{OPQ,OPQ}, {OPQ,OPQ}, {OPQ,OPQ}, {OPQ,OPQ},
{OPQ,OPQ}, {OPQ,OPQ}, {OPQ,OPQ}, {OPQ,OPQ},
{OPQ,OPQ}, {OPQ,OPQ}, {OPQ,OPQ}, {OPQ,OPQ},
{OPQ,OPQ}, {OPQ,OPQ}, {OPQ,OPQ}, {OPQ,OPQ},
{OPQ,OPQ}, {OPQ,OPQ}, {OPQ,OPQ}, {OPQ,OPQ},
{OPQ,OPQ}, {OPQ,OPQ}, {OPQ,OPQ}, {OPQ,OPQ},
{OPQ,OPQ}, {OPQ,OPQ}, {OPQ,OPQ}, {OPQ,OPQ},
{OPQ,OPQ}, {OPQ,OPQ}, {OPQ,OPQ}, {OPQ,OPQ},
{OPQ,OPQ}, {OPQ,OPQ}, {OPQ,OPQ}, {OPQ,OPQ},
{OPQ,OPQ}, {OPQ,OPQ}, {OPQ,OPQ}, {OPQ,OPQ},
{OPQ,OPQ}, {OPQ,OPQ}, {OPQ,OPQ}, {OPQ,OPQ},
{OPQ,OPQ}, {OPQ,OPQ}, {OPQ,OPQ}, {OPQ,OPQ},
{OPQ,OPQ}, {OPQ,OPQ}, {OPQ,OPQ}, {OPQ,OPQ},
{OPQ,OPQ}, {OPQ,OPQ}, {OPQ,OPQ}, {OPQ,OPQ},
{OPQ,OPQ}, {OPQ,OPQ}, {OPQ,OPQ}, {OPQ,OPQ},
{OPQ,OPQ}, {OPQ,OPQ}, {OPQ,OPQ}, {OPQ,OPQ},
{OPQ,OPQ}, {OPQ,OPQ}, {OPQ,OPQ}, {OPQ,OPQ},
{OPQ,OPQ}, {OPQ,OPQ}, {OPQ,OPQ}, {OPQ,OPQ},
{OPQ,OPQ}, {OPQ,OPQ}, {OPQ,OPQ}, {OPQ,OPQ},
{OPQ,OPQ}, {OPQ,OPQ}, {OPQ,OPQ}, {OPQ,OPQ},
{OPQ,OPQ}, {OPQ,OPQ}, {OPQ,OPQ}, {OPQ,OPQ},
{OPQ,OPQ}, {OPQ,OPQ}, {OPQ,OPQ}, {OPQ,OPQ},
{OPQ,OPQ}, {OPQ,OPQ}, {OPQ,OPQ}, {OPQ,OPQ},
{OPQ,OPQ}, {OPQ,OPQ}, {OPQ,OPQ}, {OPQ,OPQ},
{OPQ,OPQ}, {OPQ,OPQ}, {OPQ,OPQ}, {OPQ,OPQ},
{OPQ,OPQ}, {OPQ,OPQ}, {OPQ,OPQ}, {OPQ,OPQ},
{OPQ,OPQ}, {OPQ,OPQ}, {OPQ,OPQ}, {OPQ,OPQ},
{OPQ,OPQ}, {OPQ,OPQ}, {OPQ,OPQ}, {OPQ,OPQ},
{OPQ,OPQ}, {OPQ,OPQ}, {OPQ,OPQ}, {OPQ,OPQ},
{OPQ,OPQ}, {OPQ,OPQ}, {OPQ,OPQ}, {OPQ,OPQ},
{OPQ,OPQ}, {OPQ,OPQ}, {OPQ,OPQ}, {OPQ,OPQ},
{OPQ,OPQ}, {OPQ,OPQ}, {OPQ,OPQ}, {OPQ,OPQ},
{OPQ,OPQ}, {OPQ,OPQ}, {OPQ,OPQ}, {OPQ,OPQ},
{OPQ,OPQ}, {OPQ,OPQ}, {OPQ,OPQ}, {OPQ,OPQ},
{OPQ,OPQ}, {OPQ,OPQ}, {OPQ,OPQ}, {OPQ,OPQ},
{OPQ,OPQ}, {OPQ,OPQ}, {OPQ,OPQ}, {OPQ,OPQ},
{OPQ,OPQ}, {OPQ,OPQ}, {OPQ,OPQ}, {OPQ,OPQ},
{OPQ,OPQ}, {OPQ,OPQ}, {OPQ,OPQ}, {OPQ,OPQ},
{OPQ,OPQ}, {OPQ,OPQ}, {OPQ,OPQ}, {OPQ,OPQ},
{OPQ,OPQ}, {OPQ,OPQ}, {OPQ,OPQ}, {OPQ,OPQ},
{OPQ,OPQ}, {OPQ,OPQ}, {OPQ,OPQ}, {OPQ,OPQ},
{OPQ,OPQ}, {OPQ,OPQ}, {OPQ,OPQ}, {OPQ,OPQ},
{OPQ,OPQ}, {OPQ,OPQ}, {OPQ,OPQ}, {OPQ,OPQ},
{OPQ,OPQ}, {OPQ,OPQ}, {OPQ,OPQ}, {OPQ,OPQ},
{OPQ,OPQ}, {OPQ,OPQ}, {OPQ,OPQ}, {OPQ,OPQ},
{OPQ,OPQ}, {OPQ,OPQ}, {OPQ,OPQ}, {OPQ,OPQ},
{OPQ,OPQ}, {OPQ,OPQ}, {OPQ,OPQ}, {OPQ,OPQ},
{OPQ,OPQ}, {OPQ,OPQ}, {OPQ,OPQ}, {OPQ,OPQ},
{OPQ,OPQ}, {OPQ,OPQ}, {OPQ,OPQ}, {OPQ,OPQ},
{OPQ,OPQ}, {OPQ,OPQ}, {OPQ,OPQ}, {OPQ,OPQ},
{OPQ,OPQ}, {OPQ,OPQ}, {OPQ,OPQ}, {OPQ,OPQ},
{OPQ,OPQ}, {OPQ,OPQ}, {OPQ,OPQ}, {OPQ,OPQ},
{OPQ,OPQ}, {OPQ,OPQ}, {OPQ,OPQ}, {OPQ,OPQ},
{OPQ,OPQ}, {OPQ,OPQ}, {OPQ,OPQ}, {OPQ,OPQ},
{OPQ,OPQ}, {OPQ,OPQ}, {OPQ,OPQ}, {OPQ,OPQ},
{OPQ,OPQ}, {OPQ,OPQ}, {OPQ,OPQ}, {OPQ,OPQ},
{OPQ,OPQ}, {OPQ,OPQ}, {OPQ,OPQ}, {OPQ,OPQ},
{OPQ,OPQ}, {OPQ,OPQ}, {OPQ,OPQ}, {OPQ,OPQ},
{OPQ,OPQ}, {OPQ,OPQ}, {OPQ,OPQ}, {OPQ,OPQ},
{OPQ,OPQ}, {OPQ,OPQ}, {OPQ,OPQ}, {OPQ,OPQ},
{OPQ,OPQ}, {OPQ,OPQ}, {OPQ,OPQ}, {OPQ,OPQ},
{OPQ,OPQ}, {OPQ,OPQ}, {OPQ,OPQ}, {OPQ,OPQ},
{OPQ,OPQ}, {OPQ,OPQ}, {OPQ,OPQ}, {OPQ,OPQ},
{OPQ,OPQ}, {OPQ,OPQ}, {OPQ,OPQ}, {OPQ,OPQ},
{OPQ,OPQ}, {OPQ,OPQ}, {OPQ,OPQ}, {OPQ,OPQ},
{OPQ,OPQ}, {OPQ,OPQ}, {OPQ,OPQ}, {OPQ,OPQ},
{OPQ,OPQ}, {OPQ,OPQ}, {OPQ,OPQ}, {OPQ,OPQ},
{OPQ,OPQ}, {OPQ,OPQ}, {OPQ,OPQ}, {OPQ,OPQ},
{OPQ,OPQ}, {OPQ,OPQ}, {OPQ,OPQ}, {OPQ,OPQ},
{OPQ,OPQ}, {OPQ,OPQ}, {OPQ,OPQ}, {OPQ,OPQ},
{OPQ,OPQ}, {OPQ,OPQ}, {OPQ,OPQ}, {OPQ,OPQ},
{OPQ,OPQ}, {OPQ,OPQ}, {OPQ,OPQ}, {OPQ,OPQ},
{OPQ,OPQ}, {OPQ,OPQ}, {OPQ,OPQ}, {OPQ,OPQ},
{OPQ,OPQ}, {OPQ,OPQ}, {OPQ,OPQ}, {OPQ,OPQ},
{OPQ,OPQ}, {OPQ,OPQ}, {OPQ,OPQ}, {OPQ,OPQ},
{OPQ,OPQ}, {OPQ,OPQ}, {OPQ,OPQ}, {OPQ,OPQ},
{OPQ,OPQ}, {OPQ,OPQ}, {OPQ,OPQ}, {OPQ,OPQ},
{OPQ,OPQ}, {OPQ,OPQ}, {OPQ,OPQ}, {OPQ,OPQ},
{OPQ,OPQ}, {OPQ,OPQ}, {OPQ,OPQ}, {OPQ,OPQ},
{OPQ,OPQ}, {OPQ,OPQ}, {OPQ,OPQ}, {OPQ,OPQ},
{OPQ,OPQ}, {OPQ,OPQ}, {OPQ,OPQ}, {OPQ,OPQ},
{OPQ,OPQ}, {OPQ,OPQ}, {OPQ,OPQ}, {OPQ,OPQ},
{OPQ,OPQ}, {OPQ,OPQ}, {OPQ,OPQ}, {OPQ,OPQ},
{OPQ,OPQ}, {OPQ,OPQ}, {OPQ,OPQ}, {OPQ,OPQ},
{OPQ,OPQ}, {OPQ,OPQ}, {OPQ,OPQ}, {OPQ,OPQ},
{OPQ,OPQ}, {OPQ,OPQ}, {OPQ,OPQ}, {OPQ,OPQ},
{OPQ,OPQ}, {OPQ,OPQ}, {OPQ,OPQ}, {OPQ,OPQ},
{OPQ,OPQ}, {OPQ,OPQ}, {OPQ,OPQ}, {OPQ,OPQ},
{OPQ,OPQ}, {OPQ,OPQ}, {OPQ,OPQ}, {OPQ,OPQ},
{OPQ,OPQ}, {OPQ,OPQ}, {OPQ,OPQ}, {OPQ,OPQ},
{OPQ,OPQ}, {OPQ,OPQ}, {OPQ,OPQ}, {OPQ,OPQ},
{OPQ,OPQ}, {OPQ,OPQ}, {OPQ,OPQ}, {OPQ,OPQ},
{OPQ,OPQ}, {OPQ,OPQ}, {OPQ,OPQ}, {OPQ,OPQ},
{OPQ,OPQ}, {OPQ,OPQ}, {OPQ,OPQ}, {OPQ,OPQ},
{OPQ,OPQ}, {OPQ,OPQ}, {OPQ,OPQ}, {OPQ,OPQ},
{OPQ,OPQ}, {OPQ,OPQ}, {OPQ,OPQ}, {OPQ,OPQ},
{OPQ,OPQ}, {OPQ,OPQ}, {OPQ,OPQ}, {OPQ,OPQ},
{OPQ,OPQ}, {OPQ,OPQ}, {OPQ,OPQ}, {OPQ,OPQ},
{OPQ,OPQ}, {OPQ,OPQ}, {OPQ,OPQ}, {OPQ,OPQ},
{OPQ,OPQ}, {OPQ,OPQ}, {OPQ,OPQ}, {OPQ,OPQ},
{OPQ,OPQ}, {OPQ,OPQ}, {OPQ,OPQ}, {OPQ,OPQ},
{OPQ,OPQ}, {OPQ,OPQ}, {OPQ,OPQ}, {OPQ,OPQ},
{OPQ,OPQ}, {OPQ,OPQ}, {OPQ,OPQ}, {OPQ,OPQ},
{OPQ,OPQ}, {OPQ,OPQ}, {OPQ,OPQ}, {OPQ,OPQ},
{OPQ,OPQ}, {OPQ,OPQ}, {OPQ,OPQ}, {OPQ,OPQ},
{OPQ,OPQ}, {OPQ,OPQ}, {OPQ,OPQ}, {OPQ,OPQ},
{OPQ,OPQ}, {OPQ,OPQ}, {OPQ,OPQ}, {OPQ,OPQ},
{OPQ,OPQ}, {OPQ,OPQ}, {OPQ,OPQ}, {OPQ,OPQ},
{OPQ,OPQ}, {OPQ,OPQ}, {OPQ,OPQ}, {OPQ,OPQ},
{OPQ,OPQ}, {OPQ,OPQ}, {OPQ,OPQ}, {OPQ,OPQ},
{OPQ,OPQ}, {OPQ,OPQ}, {OPQ,OPQ}, {OPQ,OPQ},
{OPQ,OPQ}, {OPQ,OPQ}, {OPQ,OPQ}, {OPQ,OPQ},
{OPQ,OPQ}, {OPQ,OPQ}, {OPQ,OPQ}, {OPQ,OPQ},
{OPQ,OPQ}, {OPQ,OPQ}, {OPQ,OPQ}, {OPQ,OPQ},
{OPQ,OPQ}, {OPQ,OPQ}, {OPQ,OPQ}, {OPQ,OPQ},
{OPQ,OPQ}, {OPQ,OPQ}, {OPQ,OPQ}, {OPQ,OPQ},
{OPQ,OPQ}, {OPQ,OPQ}, {OPQ,OPQ}, {OPQ,OPQ},
{OPQ,OPQ}, {OPQ,OPQ}, {OPQ,OPQ}, {OPQ,OPQ},
{OPQ,OPQ}, {OPQ,OPQ}, {OPQ,OPQ}, {OPQ,OPQ},
{OPQ,OPQ}, {OPQ,OPQ}, {OPQ,OPQ}, {OPQ,OPQ},
{OPQ,OPQ}, {OPQ,OPQ}, {OPQ,OPQ}, {OPQ,OPQ},
{OPQ,OPQ}, {OPQ,OPQ}, {OPQ,OPQ}, {OPQ,OPQ},
{OPQ,OPQ}, {OPQ,OPQ}, {OPQ,OPQ}, {OPQ,OPQ},
{OPQ,OPQ}, {OPQ,OPQ}, {OPQ,OPQ}, {OPQ,OPQ},
{OPQ,OPQ}, {OPQ,OPQ}, {OPQ,OPQ}, {OPQ,OPQ},
{OPQ,OPQ}, {OPQ,OPQ}, {OPQ,OPQ}, {OPQ,OPQ},
{OPQ,OPQ}, {OPQ,OPQ}, {OPQ,OPQ}, {OPQ,OPQ},
{OPQ,OPQ}, {OPQ,OPQ}, {OPQ,OPQ}, {OPQ,OPQ},
{OPQ,OPQ}, {OPQ,OPQ}, {OPQ,OPQ}, {OPQ,OPQ},
{OPQ,OPQ}, {OPQ,OPQ}, {OPQ,OPQ}, {OPQ,OPQ},
{OPQ,OPQ}, {OPQ,OPQ}, {OPQ,OPQ}, {OPQ,OPQ},
{OPQ,OPQ}, {OPQ,OPQ}, {OPQ,OPQ}, {OPQ,OPQ},
{OPQ,OPQ}, {OPQ,OPQ}, {OPQ,OPQ}, {OPQ,OPQ},
{OPQ,OPQ}, {OPQ,OPQ}, {OPQ,OPQ}, {OPQ,OPQ},
{OPQ,OPQ}, {OPQ,OPQ}, {OPQ,OPQ}, {OPQ,OPQ},
{OPQ,OPQ}, {OPQ,OPQ}, {OPQ,OPQ}, {OPQ,OPQ},
{OPQ,OPQ}, {OPQ,OPQ}, {OPQ,OPQ}, {OPQ,OPQ},
{OPQ,OPQ}, {OPQ,OPQ}, {OPQ,OPQ}, {OPQ,OPQ},
{OPQ,OPQ}, {OPQ,OPQ}, {OPQ,OPQ}, {OPQ,OPQ},
{OPQ,OPQ}, {OPQ,OPQ}, {OPQ,OPQ}, {OPQ,OPQ},
{OPQ,OPQ}, {OPQ,OPQ}, {OPQ,OPQ}, {OPQ,OPQ},
{OPQ,OPQ}, {OPQ,OPQ}, {OPQ,OPQ}, {OPQ,OPQ},
{OPQ,OPQ}, {OPQ,OPQ}, {OPQ,OPQ}, {OPQ,OPQ},
{OPQ,OPQ}, {OPQ,OPQ}, {OPQ,OPQ}, {OPQ,OPQ},
{OPQ,OPQ}, {OPQ,OPQ}, {OPQ,OPQ}, {OPQ,OPQ},
{OPQ,OPQ}, {OPQ,OPQ}, {OPQ,OPQ}, {OPQ,OPQ},
{OPQ,OPQ}, {OPQ,OPQ}, {OPQ,OPQ}, {OPQ,OPQ},
{OPQ,OPQ}, {OPQ,OPQ}, {OPQ,OPQ}, {OPQ,OPQ},
{OPQ,OPQ}, {OPQ,OPQ}, {OPQ,OPQ}, {OPQ,OPQ},
{OPQ,OPQ}, {OPQ,OPQ}, {OPQ,OPQ}, {OPQ,OPQ},
{OPQ,OPQ}, {OPQ,OPQ}, {OPQ,OPQ}, {OPQ,OPQ},
{OPQ,OPQ}, {OPQ,OPQ}, {OPQ,OPQ}, {OPQ,OPQ},
{OPQ,OPQ}, {OPQ,OPQ}, {OPQ,OPQ}, {OPQ,OPQ},
{OPQ,OPQ}, {OPQ,OPQ}, {OPQ,OPQ}, {OPQ,OPQ},
{OPQ,OPQ}, {OPQ,OPQ}, {OPQ,OPQ}, {OPQ,OPQ},
{OPQ,OPQ}, {OPQ,OPQ}, {OPQ,OPQ}, {OPQ,OPQ},
{OPQ,OPQ}, {OPQ,OPQ}, {OPQ,OPQ}, {OPQ,OPQ},
{OPQ,OPQ}, {OPQ,OPQ}, {OPQ,OPQ}, {OPQ,OPQ},
{OPQ,OPQ}, {OPQ,OPQ}, {OPQ,OPQ}, {OPQ,OPQ},
{OPQ,OPQ}, {OPQ,OPQ}, {OPQ,OPQ}, {OPQ,OPQ},
{OPQ,OPQ}, {OPQ,OPQ}, {OPQ,OPQ}, {OPQ,OPQ},
{OPQ,OPQ}, {OPQ,OPQ}, {OPQ,OPQ}, {OPQ,OPQ},
{OPQ,OPQ}, {OPQ,OPQ}, {OPQ,OPQ}, {OPQ,OPQ},
{OPQ,OPQ}, {OPQ,OPQ}, {OPQ,OPQ}, {OPQ,OPQ},
{OPQ,OPQ}, {OPQ,OPQ}, {OPQ,OPQ}, {OPQ,OPQ},
{OPQ,OPQ}, {OPQ,OPQ}, {OPQ,OPQ}, {OPQ,OPQ},
{OPQ,OPQ}, {OPQ,OPQ}, {OPQ,OPQ}, {OPQ,OPQ},
{OPQ,OPQ}, {OPQ,OPQ}, {OPQ,OPQ}, {OPQ,OPQ},
{OPQ,OPQ}, {OPQ,OPQ}, {OPQ,OPQ}, {OPQ,OPQ},
{OPQ,OPQ}, {OPQ,OPQ}, {OPQ,OPQ}, {OPQ,OPQ},
{OPQ,OPQ}, {OPQ,OPQ}, {OPQ,OPQ}, {OPQ,OPQ},
{OPQ,OPQ}, {OPQ,OPQ}, {OPQ,OPQ}, {OPQ,OPQ},
{OPQ,OPQ}, {OPQ,OPQ}, {OPQ,OPQ}, {OPQ,OPQ},
{OPQ,OPQ}, {OPQ,OPQ}, {OPQ,OPQ}, {OPQ,OPQ},
{OPQ,OPQ}, {OPQ,OPQ}, {OPQ,OPQ}, {OPQ,OPQ},
{OPQ,OPQ}, {OPQ,OPQ}, {OPQ,OPQ}, {OPQ,OPQ},
{OPQ,OPQ}, {OPQ,OPQ}, {OPQ,OPQ}, {OPQ,OPQ},
{OPQ,OPQ}, {OPQ,OPQ}, {OPQ,OPQ}, {OPQ,OPQ},
{OPQ,OPQ}, {OPQ,OPQ}, {OPQ,OPQ}, {OPQ,OPQ},
{OPQ,OPQ}, {OPQ,OPQ}, {OPQ,OPQ}, {OPQ,OPQ},
{OPQ,OPQ}, {OPQ,OPQ}, {OPQ,OPQ}, {OPQ,OPQ},
{OPQ,OPQ}, {OPQ,OPQ}, {OPQ,OPQ}, {OPQ,OPQ},
{OPQ,OPQ}, {OPQ,OPQ}, {OPQ,OPQ}, {OPQ,OPQ},
{OPQ,OPQ}, {OPQ,OPQ}, {OPQ,OPQ}, {OPQ,OPQ},
{OPQ,OPQ}, {OPQ,OPQ}, {OPQ,OPQ}, {OPQ,OPQ},
{OPQ,OPQ}, {OPQ,OPQ}, {OPQ,OPQ}, {OPQ,OPQ},
{OPQ,OPQ}, {OPQ,OPQ}, {OPQ,OPQ}, {OPQ,OPQ},
{OPQ,OPQ}, {OPQ,OPQ}, {OPQ,OPQ}, {OPQ,OPQ},
{OPQ,OPQ}, {OPQ,OPQ}, {OPQ,OPQ}, {OPQ,OPQ},
{OPQ,OPQ}, {OPQ,OPQ}, {OPQ,OPQ}, {OPQ,OPQ},
{OPQ,OPQ}, {OPQ,OPQ}, {OPQ,OPQ}, {OPQ,OPQ},
{OPQ,OPQ}, {OPQ,OPQ}, {OPQ,OPQ}, {OPQ,OPQ},
{OPQ,OPQ}, {OPQ,OPQ}, {OPQ,OPQ}, {OPQ,OPQ},
{OPQ,OPQ}, {OPQ,OPQ}, {OPQ,OPQ}, {OPQ,OPQ},
{OPQ,OPQ}, {OPQ,OPQ}, {OPQ,OPQ}, {OPQ,OPQ},
{OPQ,OPQ}, {OPQ,OPQ}, {OPQ,OPQ}, {OPQ,OPQ},
{OPQ,OPQ}, {OPQ,OPQ}, {OPQ,OPQ}, {OPQ,OPQ},
{OPQ,OPQ}, {OPQ,OPQ}, {OPQ,OPQ}, {OPQ,OPQ},
{OPQ,OPQ}, {OPQ,OPQ}, {OPQ,OPQ}, {OPQ,OPQ},
{OPQ,OPQ}, {OPQ,OPQ}, {OPQ,OPQ}, {OPQ,OPQ},
{OPQ,OPQ}, {OPQ,OPQ}, {OPQ,OPQ}, {OPQ,OPQ},
{OPQ,OPQ}, {OPQ,OPQ}, {OPQ,OPQ}, {OPQ,OPQ},
};

__device__ __forceinline__ float bf2f(u16 v) { return __uint_as_float(((unsigned)v) << 16); }
__device__ __forceinline__ u16 f2bf(float f) {
  __hip_bfloat16 b = __float2bfloat16(f);
  return *reinterpret_cast<u16*>(&b);
}
__device__ __forceinline__ float bfr(float f) { return bf2f(f2bf(f)); }

__device__ __forceinline__ float2 cadd(float2 a, float2 b) { return make_float2(a.x + b.x, a.y + b.y); }
__device__ __forceinline__ float2 csub(float2 a, float2 b) { return make_float2(a.x - b.x, a.y - b.y); }
__device__ __forceinline__ float2 cmul(float2 a, float2 b) {
  return make_float2(a.x * b.x - a.y * b.y, a.x * b.y + a.y * b.x);
}
template <bool INV>
__device__ __forceinline__ float2 twmul(float2 a, float2 w) {
  if (INV) return make_float2(a.x * w.x + a.y * w.y, a.y * w.x - a.x * w.y);
  else return make_float2(a.x * w.x - a.y * w.y, a.x * w.y + a.y * w.x);
}
template <bool INV>
__device__ __forceinline__ float2 mul_mi(float2 a) {
  if (INV) return make_float2(-a.y, a.x);
  else return make_float2(a.y, -a.x);
}
template <bool INV>
__device__ __forceinline__ void fft4(float2& v0, float2& v1, float2& v2, float2& v3) {
  float2 a0 = cadd(v0, v2), a1 = csub(v0, v2);
  float2 b0 = cadd(v1, v3), b1 = mul_mi<INV>(csub(v1, v3));
  v0 = cadd(a0, b0);
  v2 = csub(a0, b0);
  v1 = cadd(a1, b1);
  v3 = csub(a1, b1);
}
template <bool INV>
__device__ __forceinline__ void fft8(float2 (&v)[8]) {
  const float r = 0.70710678118654752440f;
  float2 e0 = cadd(v[0], v[4]), o0 = csub(v[0], v[4]);
  float2 e1 = cadd(v[1], v[5]), o1 = csub(v[1], v[5]);
  float2 e2 = cadd(v[2], v[6]), o2 = csub(v[2], v[6]);
  float2 e3 = cadd(v[3], v[7]), o3 = csub(v[3], v[7]);
  if (INV) {
    o1 = make_float2((o1.x - o1.y) * r, (o1.x + o1.y) * r);
    o3 = make_float2((-o3.x - o3.y) * r, (o3.x - o3.y) * r);
  } else {
    o1 = make_float2((o1.x + o1.y) * r, (o1.y - o1.x) * r);
    o3 = make_float2((o3.y - o3.x) * r, (-o3.x - o3.y) * r);
  }
  o2 = mul_mi<INV>(o2);
  fft4<INV>(e0, e1, e2, e3);
  fft4<INV>(o0, o1, o2, o3);
  v[0] = e0; v[2] = e1; v[4] = e2; v[6] = e3;
  v[1] = o0; v[3] = o1; v[5] = o2; v[7] = o3;
}
__device__ __forceinline__ int padi(int i) { return i + (i >> 4); }

__device__ __forceinline__ float wave_sum(float v) {
#pragma unroll
  for (int o = 32; o >= 1; o >>= 1) v += __shfl_xor(v, o, 64);
  return v;
}

__device__ __forceinline__ void ln_row(float (&x)[8]) {
  float s = 0.f;
#pragma unroll
  for (int m = 0; m < 8; ++m) s += x[m];
  s = wave_sum(s);
  const float mean = s * (1.0f / 512.0f);
  float q = 0.f;
#pragma unroll
  for (int m = 0; m < 8; ++m) { float d = x[m] - mean; q += d * d; }
  q = wave_sum(q);
  const float var = q * (1.0f / 512.0f);
  const float mb = bfr(mean);
  const float vb = bfr(var);
  const float den = bfr(sqrtf(bfr(vb + 1e-5f)));
#pragma unroll
  for (int m = 0; m < 8; ++m) x[m] = bfr(bfr(x[m] - mb) / den);
}

template <bool INV>
__device__ __forceinline__ void wave_fft512(float2 (&v)[8], float2* ex, int j, const float2 (&tw1)[7],
                                            const float2 (&tw2)[7]) {
  fft8<INV>(v);
#pragma unroll
  for (int m = 0; m < 8; ++m) ex[padi(j * 8 + m)] = v[m];
  __builtin_amdgcn_wave_barrier();
#pragma unroll
  for (int m = 0; m < 8; ++m) v[m] = ex[padi(j + 64 * m)];
  __builtin_amdgcn_wave_barrier();
#pragma unroll
  for (int m = 1; m < 8; ++m) v[m] = twmul<INV>(v[m], tw1[m - 1]);
  fft8<INV>(v);
#pragma unroll
  for (int m = 0; m < 8; ++m) ex[padi((j >> 3) * 64 + (j & 7) + 8 * m)] = v[m];
  __builtin_amdgcn_wave_barrier();
#pragma unroll
  for (int m = 0; m < 8; ++m) v[m] = ex[padi(j + 64 * m)];
  __builtin_amdgcn_wave_barrier();
#pragma unroll
  for (int m = 1; m < 8; ++m) v[m] = twmul<INV>(v[m], tw2[m - 1]);
  fft8<INV>(v);
}

#define EXW 544
#define TSTR 10

__global__ __launch_bounds__(256) void k1_ln_rfft(const u16* __restrict__ x, const u16* __restrict__ wr,
                                                  const u16* __restrict__ wi, float2* __restrict__ spec,
                                                  unsigned* __restrict__ wt) {
  __shared__ __attribute__((aligned(16))) float2 sm[4 * EXW + 256 * TSTR];
  const int t = threadIdx.x;
  if (blockIdx.x >= 256) {
    const int m0 = (blockIdx.x - 256) * 8;
    unsigned p[8];
#pragma unroll
    for (int r = 0; r < 8; ++r)
      p[r] = (unsigned)wr[(m0 + r) * DF + t] | ((unsigned)wi[(m0 + r) * DF + t] << 16);
    uint4* dst = reinterpret_cast<uint4*>(wt + t * S_LEN + m0);
    dst[0] = make_uint4(p[0], p[1], p[2], p[3]);
    dst[1] = make_uint4(p[4], p[5], p[6], p[7]);
    if (t < 8) wt[256 * S_LEN + m0 + t] = (unsigned)wr[(m0 + t) * DF + 256] | ((unsigned)wi[(m0 + t) * DF + 256] << 16);
    return;
  }
  const int w = t >> 6, j = t & 63;
  const int s0 = blockIdx.x * 8;
  const int ra = s0 + 2 * w;
  float2* ex = sm + w * EXW;
  float2* tile = sm + 4 * EXW;

  float xa[8], xb[8];
#pragma unroll
  for (int m = 0; m < 8; ++m) {
    xa[m] = bf2f(x[ra * D_DIM + j + 64 * m]);
    xb[m] = bf2f(x[(ra + 1) * D_DIM + j + 64 * m]);
  }
  float2 tw1[7], tw2[7];
#pragma unroll
  for (int m = 1; m < 8; ++m) {
    tw1[m - 1] = g_tw[32 * (j & 7) * m];
    tw2[m - 1] = g_tw[4 * j * m];
  }
  ln_row(xa);
  ln_row(xb);
  float2 v[8];
#pragma unroll
  for (int m = 0; m < 8; ++m) v[m] = make_float2(xa[m], xb[m]);
  wave_fft512<false>(v, ex, j, tw1, tw2);
  __builtin_amdgcn_wave_barrier();
#pragma unroll
  for (int m = 4; m < 8; ++m) ex[padi(j + 64 * m)] = v[m];
  if (j == 0) ex[padi(0)] = v[0];
  __builtin_amdgcn_wave_barrier();
#pragma unroll
  for (int m = 0; m < 4; ++m) {
    const int k = j + 64 * m;
    const float2 zc = ex[padi((512 - k) & 511)];
    float4 o;
    if (k == 0) {
      const float2 zn = ex[padi(256)];
      o = make_float4(v[0].x, zn.x, v[0].y, zn.y);
    } else {
      const float2 z = v[m];
      o.x = 0.5f * (z.x + zc.x);
      o.y = 0.5f * (z.y - zc.y);
      o.z = 0.5f * (z.y + zc.y);
      o.w = 0.5f * (zc.x - z.x);
    }
    *reinterpret_cast<float4*>(tile + k * TSTR + 2 * w) = o;
  }
  __syncthreads();
#pragma unroll
  for (int i = 0; i < 4; ++i) {
    const int idx = t + 256 * i;
    const int k = idx >> 2, part = idx & 3;
    const float4 o = *reinterpret_cast<const float4*>(tile + k * TSTR + 2 * part);
    *reinterpret_cast<float4*>(spec + k * S_LEN + s0 + 2 * part) = o;
  }
}

__device__ __forceinline__ float2 unpack_w(unsigned p) {
  return make_float2(__uint_as_float(p << 16), __uint_as_float(p & 0xffff0000u));
}

__global__ __launch_bounds__(256) void k2_colfft(float2* __restrict__ spec, const unsigned* __restrict__ wt,
                                                 const u16* __restrict__ wg, const u16* __restrict__ wu,
                                                 const u16* __restrict__ wd, uint4* __restrict__ wgu_t,
                                                 uint4* __restrict__ wd_t) {
  __shared__ __attribute__((aligned(16))) float2 ex[2048 + 128];
  const int t = threadIdx.x;
  if (blockIdx.x >= 256) {
    const int g = (blockIdx.x - 256) * 256 + t;
    const int lane = g & 63, q = lane >> 4, c = lane & 15;
    if (g < NT16 * 16 * 2 * 64) {
      const int f = g >> 6;
      const int which = f & 1, s = (f >> 1) & 15, jt = f >> 5;
      const u16* src = (which ? wu : wg) + (32 * s + 8 * q) * F_DIM + 16 * jt + c;
      unsigned p[4];
#pragma unroll
      for (int i = 0; i < 4; ++i)
        p[i] = (unsigned)src[(2 * i) * F_DIM] | ((unsigned)src[(2 * i + 1) * F_DIM] << 16);
      wgu_t[g] = make_uint4(p[0], p[1], p[2], p[3]);
    } else {
      const int g2 = g - NT16 * 16 * 2 * 64;
      if (g2 < 32 * KS2 * 64) {
        const int f = g2 >> 6;
        const int s = f % KS2, nt = f / KS2;
        const u16* src0 = wd + (32 * s + 4 * q) * D_DIM + 16 * nt + c;
        const u16* src1 = src0 + 16 * D_DIM;
        unsigned p[4];
        p[0] = (unsigned)src0[0] | ((unsigned)src0[D_DIM] << 16);
        p[1] = (unsigned)src0[2 * D_DIM] | ((unsigned)src0[3 * D_DIM] << 16);
        p[2] = (unsigned)src1[0] | ((unsigned)src1[D_DIM] << 16);
        p[3] = (unsigned)src1[2 * D_DIM] | ((unsigned)src1[3 * D_DIM] << 16);
        wd_t[g2] = make_uint4(p[0], p[1], p[2], p[3]);
      }
    }
    return;
  }
  const int k = blockIdx.x;
  float2* col = spec + k * S_LEN;
  float2 v[8];
#pragma unroll
  for (int m = 0; m < 8; ++m) v[m] = col[t + 256 * m];
  unsigned wp[8];
#pragma unroll
  for (int m = 0; m < 8; ++m) wp[m] = wt[k * S_LEN + t + 256 * m];

  fft4<false>(v[0], v[2], v[4], v[6]);
  fft4<false>(v[1], v[3], v[5], v[7]);
#pragma unroll
  for (int r = 0; r < 4; ++r) {
    ex[padi(t * 4 + r)] = v[2 * r];
    ex[padi((t + 256) * 4 + r)] = v[2 * r + 1];
  }
  __syncthreads();
#pragma unroll
  for (int m = 0; m < 8; ++m) v[m] = ex[padi(t + 256 * m)];
#pragma unroll
  for (int m = 1; m < 8; ++m) v[m] = twmul<false>(v[m], g_tw[64 * (t & 3) * m]);
  fft8<false>(v);
  __syncthreads();
#pragma unroll
  for (int m = 0; m < 8; ++m) ex[padi((t >> 2) * 32 + (t & 3) + 4 * m)] = v[m];
  __syncthreads();
#pragma unroll
  for (int m = 0; m < 8; ++m) v[m] = ex[padi(t + 256 * m)];
#pragma unroll
  for (int m = 1; m < 8; ++m) v[m] = twmul<false>(v[m], g_tw[8 * (t & 31) * m]);
  fft8<false>(v);
  __syncthreads();
#pragma unroll
  for (int m = 0; m < 8; ++m) ex[padi((t >> 5) * 256 + (t & 31) + 32 * m)] = v[m];
  __syncthreads();
#pragma unroll
  for (int m = 0; m < 8; ++m) v[m] = ex[padi(t + 256 * m)];
#pragma unroll
  for (int m = 1; m < 8; ++m) v[m] = twmul<false>(v[m], g_tw[t * m]);
  fft8<false>(v);

  if (k != 0) {
#pragma unroll
    for (int m = 0; m < 8; ++m) v[m] = cmul(v[m], unpack_w(wp[m]));
  } else {
    __syncthreads();
#pragma unroll
    for (int m = 0; m < 8; ++m) ex[padi(t + 256 * m)] = v[m];
    __syncthreads();
#pragma unroll
    for (int m = 0; m < 8; ++m) {
      const int f = t + 256 * m, fn = (2048 - f) & 2047;
      const float2 z = v[m];
      const float2 zc = ex[padi(fn)];
      const float2 x0 = make_float2(0.5f * (z.x + zc.x), 0.5f * (z.y - zc.y));
      const float2 x1 = make_float2(0.5f * (z.y + zc.y), 0.5f * (zc.x - z.x));
      const float2 w0 = unpack_w(wp[m]), w0n = unpack_w(wt[fn]);
      const float2 w1 = unpack_w(wt[256 * S_LEN + f]), w1n = unpack_w(wt[256 * S_LEN + fn]);
      const float2 g0 = make_float2(0.5f * (w0.x + w0n.x), 0.5f * (w0.y - w0n.y));
      const float2 g1 = make_float2(0.5f * (w1.x + w1n.x), 0.5f * (w1.y - w1n.y));
      const float2 a0 = cmul(x0, g0), a1 = cmul(x1, g1);
      v[m] = make_float2(a0.x - a1.y, a0.y + a1.x);
    }
  }

  fft8<true>(v);
  __syncthreads();
#pragma unroll
  for (int m = 0; m < 8; ++m) ex[padi(t * 8 + m)] = v[m];
  __syncthreads();
#pragma unroll
  for (int m = 0; m < 8; ++m) v[m] = ex[padi(t + 256 * m)];
#pragma unroll
  for (int m = 1; m < 8; ++m) v[m] = twmul<true>(v[m], g_tw[32 * (t & 7) * m]);
  fft8<true>(v);
  __syncthreads();
#pragma unroll
  for (int m = 0; m < 8; ++m) ex[padi((t >> 3) * 64 + (t & 7) + 8 * m)] = v[m];
  __syncthreads();
#pragma unroll
  for (int m = 0; m < 8; ++m) v[m] = ex[padi(t + 256 * m)];
#pragma unroll
  for (int m = 1; m < 8; ++m) v[m] = twmul<true>(v[m], g_tw[4 * (t & 63) * m]);
  fft8<true>(v);
  __syncthreads();
#pragma unroll
  for (int m = 0; m < 8; ++m) ex[padi((t >> 6) * 512 + (t & 63) + 64 * m)] = v[m];
  __syncthreads();
#pragma unroll
  for (int m = 0; m < 8; ++m) v[m] = ex[padi(t + 256 * m)];
#pragma unroll
  for (int r = 1; r < 4; ++r) {
    v[2 * r] = twmul<true>(v[2 * r], g_tw[t * r]);
    v[2 * r + 1] = twmul<true>(v[2 * r + 1], g_tw[(t + 256) * r]);
  }
  fft4<true>(v[0], v[2], v[4], v[6]);
  fft4<true>(v[1], v[3], v[5], v[7]);
#pragma unroll
  for (int m = 0; m < 8; ++m) col[t + 256 * m] = v[m];
}

__global__ __launch_bounds__(256) void k3_irfft_ln(const float2* __restrict__ spec, const u16* __restrict__ x,
                                                   u16* __restrict__ h, u16* __restrict__ xn_t) {
  __shared__ __attribute__((aligned(16))) float2 sm[4 * EXW + 256 * TSTR];
  const int t = threadIdx.x;
  const int w = t >> 6, j = t & 63;
  const int s0 = blockIdx.x * 8;
  const int ra = s0 + 2 * w;
  float2* ex = sm + w * EXW;
  float2* tile = sm + 4 * EXW;
#pragma unroll
  for (int i = 0; i < 4; ++i) {
    const int idx = t + 256 * i;
    const int k = idx >> 2, part = idx & 3;
    const float4 o = *reinterpret_cast<const float4*>(spec + k * S_LEN + s0 + 2 * part);
    *reinterpret_cast<float4*>(tile + k * TSTR + 2 * part) = o;
  }
  float xa[8], xb[8];
#pragma unroll
  for (int m = 0; m < 8; ++m) {
    xa[m] = bf2f(x[ra * D_DIM + j + 64 * m]);
    xb[m] = bf2f(x[(ra + 1) * D_DIM + j + 64 * m]);
  }
  float2 tw1[7], tw2[7];
#pragma unroll
  for (int m = 1; m < 8; ++m) {
    tw1[m - 1] = g_tw[32 * (j & 7) * m];
    tw2[m - 1] = g_tw[4 * j * m];
  }
  __syncthreads();
  float2 v[8];
#pragma unroll
  for (int m = 0; m < 8; ++m) {
    const int n = j + 64 * m;
    const int k = (m < 4) ? n : (512 - n);
    const float4 y = *reinterpret_cast<const float4*>(tile + (k & 255) * TSTR + 2 * w);
    if (n == 0) v[m] = make_float2(y.x, y.z);
    else if (n == 256) v[m] = make_float2(y.y, y.w);
    else if (m < 4) v[m] = make_float2(y.x - y.w, y.y + y.z);
    else v[m] = make_float2(y.x + y.w, y.z - y.y);
  }
  wave_fft512<true>(v, ex, j, tw1, tw2);
  const float sc = 1.0f / (2048.0f * 512.0f);
#pragma unroll
  for (int m = 0; m < 8; ++m) {
    xa[m] = bfr(xa[m] + bfr(v[m].x * sc));
    xb[m] = bfr(xb[m] + bfr(v[m].y * sc));
  }
  __syncthreads();
  u16* st_h = reinterpret_cast<u16*>(tile);
  u16* st_x = st_h + 8 * 512;
#pragma unroll
  for (int m = 0; m < 8; ++m) {
    st_h[(2 * w) * 512 + j + 64 * m] = f2bf(xa[m]);
    st_h[(2 * w + 1) * 512 + j + 64 * m] = f2bf(xb[m]);
  }
  ln_row(xa);
  ln_row(xb);
#pragma unroll
  for (int m = 0; m < 8; ++m) {
    st_x[(2 * w) * 512 + j + 64 * m] = f2bf(xa[m]);
    st_x[(2 * w + 1) * 512 + j + 64 * m] = f2bf(xb[m]);
  }
  __syncthreads();
#pragma unroll
  for (int i = 0; i < 2; ++i) {
    const int idx = t + 256 * i;
    reinterpret_cast<uint4*>(h + s0 * D_DIM)[idx] = reinterpret_cast<const uint4*>(st_h)[idx];
  }
  const int mt = s0 >> 4, rb = s0 & 15;
#pragma unroll
  for (int i = 0; i < 2; ++i) {
    const int idx = t + 256 * i;
    const int r = idx & 7, q = (idx >> 3) & 3, ks = idx >> 5;
    const uint4 val = *reinterpret_cast<const uint4*>(st_x + r * 512 + ks * 32 + q * 8);
    reinterpret_cast<uint4*>(xn_t)[(mt * 16 + ks) * 64 + q * 16 + rb + r] = val;
  }
}

__device__ __forceinline__ float gelu_tanh(float g) {
  const float y = 0.7978845608028654f * (g + 0.044715f * g * g * g);
  return g / (1.0f + __expf(-2.0f * y));
}

template <int NT>
__device__ __forceinline__ void k4_body(const uint4* __restrict__ As, const uint4* __restrict__ wgu_t,
                                        uint2* __restrict__ act_t, int mt64, int jt0, int lane) {
  f32x4 ag[NT][4], au[NT][4];
#pragma unroll
  for (int n = 0; n < NT; ++n)
#pragma unroll
    for (int mi = 0; mi < 4; ++mi) { ag[n][mi] = (f32x4){0.f, 0.f, 0.f, 0.f}; au[n][mi] = (f32x4){0.f, 0.f, 0.f, 0.f}; }
  const uint4* bp = wgu_t + (size_t)jt0 * 16 * 2 * 64 + lane;
  uint4 bg[16][NT], bu[16][NT];
#pragma unroll
  for (int s = 0; s < 16; ++s)
#pragma unroll
    for (int n = 0; n < NT; ++n) {
      bg[s][n] = bp[((n * 16 + s) * 2 + 0) * 64];
      bu[s][n] = bp[((n * 16 + s) * 2 + 1) * 64];
    }
  __syncthreads();
#pragma unroll
  for (int s = 0; s < 16; ++s) {
    uint4 a[4];
#pragma unroll
    for (int mi = 0; mi < 4; ++mi) a[mi] = As[(mi * 16 + s) * 64 + lane];
#pragma unroll
    for (int n = 0; n < NT; ++n)
#pragma unroll
      for (int mi = 0; mi < 4; ++mi) {
        ag[n][mi] = __builtin_amdgcn_mfma_f32_16x16x32_bf16(*reinterpret_cast<bf16x8*>(&bg[s][n]),
                                                            *reinterpret_cast<bf16x8*>(&a[mi]), ag[n][mi], 0, 0, 0);
        au[n][mi] = __builtin_amdgcn_mfma_f32_16x16x32_bf16(*reinterpret_cast<bf16x8*>(&bu[s][n]),
                                                            *reinterpret_cast<bf16x8*>(&a[mi]), au[n][mi], 0, 0, 0);
      }
  }
#pragma unroll
  for (int n = 0; n < NT; ++n) {
    const int jt = jt0 + n;
#pragma unroll
    for (int mi = 0; mi < 4; ++mi) {
      u16 p[4];
#pragma unroll
      for (int r = 0; r < 4; ++r) {
        const float g = bfr(ag[n][mi][r]);
        const float u = bfr(au[n][mi][r]);
        const float a = bfr(gelu_tanh(g));
        p[r] = f2bf(a * u);
      }
      const int mt16 = mt64 * 4 + mi;
      uint2 o = make_uint2((unsigned)p[0] | ((unsigned)p[1] << 16), (unsigned)p[2] | ((unsigned)p[3] << 16));
      act_t[((mt16 * KS2 + (jt >> 1)) * 64 + lane) * 2 + (jt & 1)] = o;
    }
  }
}

__global__ __launch_bounds__(256, 1) void k4_gemm1(const uint4* __restrict__ xn_t, const uint4* __restrict__ wgu_t,
                                                   uint2* __restrict__ act_t) {
  __shared__ __attribute__((aligned(16))) uint4 As[4 * 16 * 64];
  const int t = threadIdx.x, w = __builtin_amdgcn_readfirstlane(t >> 6), lane = t & 63;
  const int bid = blockIdx.x;
  const int xcd = bid & 7, idx = bid >> 3;
  const int mt64 = (xcd & 1) * 16 + (idx & 15);
  const int ng = (xcd >> 1) * 2 + (idx >> 4);
  const uint4* src = xn_t + (size_t)mt64 * 4 * 16 * 64;
#pragma unroll
  for (int i = 0; i < 16; ++i)
    __builtin_amdgcn_global_load_lds((const unsigned*)(src + i * 256 + t), (unsigned*)(As + i * 256 + w * 64), 16, 0, 0);
  int start, cnt;
  if (ng < 6) { start = 11 * ng; cnt = 11; } else { start = 66 + 10 * (ng - 6); cnt = 10; }
  int jt0, nt;
  if (cnt == 11) { jt0 = start + 3 * w; nt = (w < 3) ? 3 : 2; }
  else { jt0 = (w < 2) ? (start + 3 * w) : (start + 6 + 2 * (w - 2)); nt = (w < 2) ? 3 : 2; }
  if (nt == 3) k4_body<3>(As, wgu_t, act_t, mt64, jt0, lane);
  else k4_body<2>(As, wgu_t, act_t, mt64, jt0, lane);
}

#define K5_WAVES 8
#define PSTR 68
__global__ __launch_bounds__(512, 2) void k5_gemm2(const uint4* __restrict__ act_t, const uint4* __restrict__ wd_t,
                                                   const u16* __restrict__ h, u16* __restrict__ out) {
  __shared__ __attribute__((aligned(16))) float part[K5_WAVES * 64 * PSTR];
  const int t = threadIdx.x, w = __builtin_amdgcn_readfirstlane(t >> 6), lane = t & 63;
  const int bid = blockIdx.x;
  const int xcd = bid & 7, idx = bid >> 3;
  const int mt64 = (xcd & 3) * 8 + (idx & 7);
  const int nt64 = (xcd >> 2) * 4 + (idx >> 3);
  const int ks0 = (w < 3) ? 6 * w : 18 + 5 * (w - 3);
  const int nks = (w < 3) ? 6 : 5;
  f32x4 acc[4][4];
#pragma unroll
  for (int a = 0; a < 4; ++a)
#pragma unroll
    for (int b = 0; b < 4; ++b) acc[a][b] = (f32x4){0.f, 0.f, 0.f, 0.f};
  const uint4* ap = act_t + ((size_t)(mt64 * 4) * KS2 + ks0) * 64 + lane;
  const uint4* bp = wd_t + ((size_t)(nt64 * 4) * KS2 + ks0) * 64 + lane;
  uint4 af[6][4], bf[6][4];
#pragma unroll
  for (int s = 0; s < 6; ++s) {
    if (s < nks) {
#pragma unroll
      for (int i = 0; i < 4; ++i) {
        af[s][i] = ap[(i * KS2 + s) * 64];
        bf[s][i] = bp[(i * KS2 + s) * 64];
      }
    }
  }
  const int er = t >> 3, ec = (t & 7) * 8;
  const uint4 hv = *reinterpret_cast<const uint4*>(h + (size_t)(mt64 * 64 + er) * D_DIM + nt64 * 64 + ec);
#pragma unroll
  for (int s = 0; s < 6; ++s) {
    if (s < nks) {
#pragma unroll
      for (int mi = 0; mi < 4; ++mi)
#pragma unroll
        for (int ni = 0; ni < 4; ++ni)
          acc[mi][ni] = __builtin_amdgcn_mfma_f32_16x16x32_bf16(*reinterpret_cast<bf16x8*>(&bf[s][ni]),
                                                                *reinterpret_cast<bf16x8*>(&af[s][mi]), acc[mi][ni], 0, 0, 0);
    }
  }
  float* pw = part + w * 64 * PSTR;
#pragma unroll
  for (int mi = 0; mi < 4; ++mi)
#pragma unroll
    for (int ni = 0; ni < 4; ++ni)
      *reinterpret_cast<f32x4*>(pw + (16 * mi + (lane & 15)) * PSTR + 16 * ni + 4 * (lane >> 4)) = acc[mi][ni];
  __syncthreads();
  f32x4 s0 = {0.f, 0.f, 0.f, 0.f}, s1 = {0.f, 0.f, 0.f, 0.f};
#pragma unroll
  for (int ww = 0; ww < K5_WAVES; ++ww) {
    s0 += *reinterpret_cast<const f32x4*>(part + ww * 64 * PSTR + er * PSTR + ec);
    s1 += *reinterpret_cast<const f32x4*>(part + ww * 64 * PSTR + er * PSTR + ec + 4);
  }
  const unsigned hw[4] = {hv.x, hv.y, hv.z, hv.w};
  unsigned ow[4];
#pragma unroll
  for (int i = 0; i < 4; ++i) {
    const float d0 = bfr(i < 2 ? s0[2 * i] : s1[2 * i - 4]);
    const float d1 = bfr(i < 2 ? s0[2 * i + 1] : s1[2 * i - 3]);
    const float h0 = __uint_as_float(hw[i] << 16), h1 = __uint_as_float(hw[i] & 0xffff0000u);
    ow[i] = (unsigned)f2bf(h0 + d0) | ((unsigned)f2bf(h1 + d1) << 16);
  }
  *reinterpret_cast<uint4*>(out + (size_t)(mt64 * 64 + er) * D_DIM + nt64 * 64 + ec) = make_uint4(ow[0], ow[1], ow[2], ow[3]);
}

extern "C" void kernel_launch(void* const* d_in, const int* in_sizes, int n_in, void* d_out, int out_size,
                              void* d_ws, size_t ws_size, hipStream_t stream) {
  const u16* x = (const u16*)d_in[0];
  const u16* wr = (const u16*)d_in[1];
  const u16* wi = (const u16*)d_in[2];
  const u16* wg = (const u16*)d_in[4];
  const u16* wu = (const u16*)d_in[5];
  const u16* wd = (const u16*)d_in[6];
  u16* out = (u16*)d_out;
  char* ws = (char*)d_ws;
  size_t off = 0;
  float2* spec = (float2*)(ws + off); off += (size_t)256 * S_LEN * 8;
  unsigned* wt = (unsigned*)(ws + off); off += (size_t)260 * S_LEN * 4;
  u16* h = (u16*)(ws + off); off += (size_t)S_LEN * D_DIM * 2;
  u16* xn_t = (u16*)(ws + off); off += (size_t)S_LEN * D_DIM * 2;
  uint4* wgu_t = (uint4*)(ws + off); off += (size_t)NT16 * 16 * 2 * 1024;
  uint4* wd_t = (uint4*)(ws + off); off += (size_t)32 * KS2 * 1024;
  uint4* act_t = (uint4*)(ws + off); off += (size_t)128 * KS2 * 1024;

  k1_ln_rfft<<<512, 256, 0, stream>>>(x, wr, wi, spec, wt);
  const int side = (NT16 * 16 * 2 * 64 + 32 * KS2 * 64 + 255) / 256;
  k2_colfft<<<256 + side, 256, 0, stream>>>(spec, wt, wg, wu, wd, wgu_t, wd_t);
  k3_irfft_ln<<<256, 256, 0, stream>>>(spec, x, h, xn_t);
  k4_gemm1<<<256, 256, 0, stream>>>((const uint4*)xn_t, wgu_t, (uint2*)act_t);
  k5_gemm2<<<256, 512, 0, stream>>>(act_t, wd_t, h, out);
}
```

```cpp
#include <hip/hip_runtime.h>
#include <hip/hip_bf16.h>
#include <stdint.h>

typedef __attribute__((ext_vector_type(8))) short bf16x8;
typedef __attribute__((ext_vector_type(4))) float f32x4;
typedef unsigned short u16;
#define DIAG_PARAM
#define DIAG_ARG
#define STAMP(k, s)
#define STAMPW(k, s)

#define S_LEN 2048
#define D_DIM 512
#define F_DIM 1376
#define DF 257
#define NT16 86
#define KS2 43

__device__ const float2 g_tw[2048] = {
{OPQ,OPQ}, {OPQ,OPQ}, {OPQ,OPQ}, {OPQ,OPQ},
{OPQ,OPQ}, {OPQ,OPQ}, {OPQ,OPQ}, {OPQ,OPQ},
{OPQ,OPQ}, {OPQ,OPQ}, {OPQ,OPQ}, {OPQ,OPQ},
{OPQ,OPQ}, {OPQ,OPQ}, {OPQ,OPQ}, {OPQ,OPQ},
{OPQ,OPQ}, {OPQ,OPQ}, {OPQ,OPQ}, {OPQ,OPQ},
{OPQ,OPQ}, {OPQ,OPQ}, {OPQ,OPQ}, {OPQ,OPQ},
{OPQ,OPQ}, {OPQ,OPQ}, {OPQ,OPQ}, {OPQ,OPQ},
{OPQ,OPQ}, {OPQ,OPQ}, {OPQ,OPQ}, {OPQ,OPQ},
{OPQ,OPQ}, {OPQ,OPQ}, {OPQ,OPQ}, {OPQ,OPQ},
{OPQ,OPQ}, {OPQ,OPQ}, {OPQ,OPQ}, {OPQ,OPQ},
{OPQ,OPQ}, {OPQ,OPQ}, {OPQ,OPQ}, {OPQ,OPQ},
{OPQ,OPQ}, {OPQ,OPQ}, {OPQ,OPQ}, {OPQ,OPQ},
{OPQ,OPQ}, {OPQ,OPQ}, {OPQ,OPQ}, {OPQ,OPQ},
{OPQ,OPQ}, {OPQ,OPQ}, {OPQ,OPQ}, {OPQ,OPQ},
{OPQ,OPQ}, {OPQ,OPQ}, {OPQ,OPQ}, {OPQ,OPQ},
{OPQ,OPQ}, {OPQ,OPQ}, {OPQ,OPQ}, {OPQ,OPQ},
{OPQ,OPQ}, {OPQ,OPQ}, {OPQ,OPQ}, {OPQ,OPQ},
{OPQ,OPQ}, {OPQ,OPQ}, {OPQ,OPQ}, {OPQ,OPQ},
{OPQ,OPQ}, {OPQ,OPQ}, {OPQ,OPQ}, {OPQ,OPQ},
{OPQ,OPQ}, {OPQ,OPQ}, {OPQ,OPQ}, {OPQ,OPQ},
{OPQ,OPQ}, {OPQ,OPQ}, {OPQ,OPQ}, {OPQ,OPQ},
{OPQ,OPQ}, {OPQ,OPQ}, {OPQ,OPQ}, {OPQ,OPQ},
{OPQ,OPQ}, {OPQ,OPQ}, {OPQ,OPQ}, {OPQ,OPQ},
{OPQ,OPQ}, {OPQ,OPQ}, {OPQ,OPQ}, {OPQ,OPQ},
{OPQ,OPQ}, {OPQ,OPQ}, {OPQ,OPQ}, {OPQ,OPQ},
{OPQ,OPQ}, {OPQ,OPQ}, {OPQ,OPQ}, {OPQ,OPQ},
{OPQ,OPQ}, {OPQ,OPQ}, {OPQ,OPQ}, {OPQ,OPQ},
{OPQ,OPQ}, {OPQ,OPQ}, {OPQ,OPQ}, {OPQ,OPQ},
{OPQ,OPQ}, {OPQ,OPQ}, {OPQ,OPQ}, {OPQ,OPQ},
{OPQ,OPQ}, {OPQ,OPQ}, {OPQ,OPQ}, {OPQ,OPQ},
{OPQ,OPQ}, {OPQ,OPQ}, {OPQ,OPQ}, {OPQ,OPQ},
{OPQ,OPQ}, {OPQ,OPQ}, {OPQ,OPQ}, {OPQ,OPQ},
{OPQ,OPQ}, {OPQ,OPQ}, {OPQ,OPQ}, {OPQ,OPQ},
{OPQ,OPQ}, {OPQ,OPQ}, {OPQ,OPQ}, {OPQ,OPQ},
{OPQ,OPQ}, {OPQ,OPQ}, {OPQ,OPQ}, {OPQ,OPQ},
{OPQ,OPQ}, {OPQ,OPQ}, {OPQ,OPQ}, {OPQ,OPQ},
{OPQ,OPQ}, {OPQ,OPQ}, {OPQ,OPQ}, {OPQ,OPQ},
{OPQ,OPQ}, {OPQ,OPQ}, {OPQ,OPQ}, {OPQ,OPQ},
{OPQ,OPQ}, {OPQ,OPQ}, {OPQ,OPQ}, {OPQ,OPQ},
{OPQ,OPQ}, {OPQ,OPQ}, {OPQ,OPQ}, {OPQ,OPQ},
{OPQ,OPQ}, {OPQ,OPQ}, {OPQ,OPQ}, {OPQ,OPQ},
{OPQ,OPQ}, {OPQ,OPQ}, {OPQ,OPQ}, {OPQ,OPQ},
{OPQ,OPQ}, {OPQ,OPQ}, {OPQ,OPQ}, {OPQ,OPQ},
{OPQ,OPQ}, {OPQ,OPQ}, {OPQ,OPQ}, {OPQ,OPQ},
{OPQ,OPQ}, {OPQ,OPQ}, {OPQ,OPQ}, {OPQ,OPQ},
{OPQ,OPQ}, {OPQ,OPQ}, {OPQ,OPQ}, {OPQ,OPQ},
{OPQ,OPQ}, {OPQ,OPQ}, {OPQ,OPQ}, {OPQ,OPQ},
{OPQ,OPQ}, {OPQ,OPQ}, {OPQ,OPQ}, {OPQ,OPQ},
{OPQ,OPQ}, {OPQ,OPQ}, {OPQ,OPQ}, {OPQ,OPQ},
{OPQ,OPQ}, {OPQ,OPQ}, {OPQ,OPQ}, {OPQ,OPQ},
{OPQ,OPQ}, {OPQ,OPQ}, {OPQ,OPQ}, {OPQ,OPQ},
{OPQ,OPQ}, {OPQ,OPQ}, {OPQ,OPQ}, {OPQ,OPQ},
{OPQ,OPQ}, {OPQ,OPQ}, {OPQ,OPQ}, {OPQ,OPQ},
{OPQ,OPQ}, {OPQ,OPQ}, {OPQ,OPQ}, {OPQ,OPQ},
{OPQ,OPQ}, {OPQ,OPQ}, {OPQ,OPQ}, {OPQ,OPQ},
{OPQ,OPQ}, {OPQ,OPQ}, {OPQ,OPQ}, {OPQ,OPQ},
{OPQ,OPQ}, {OPQ,OPQ}, {OPQ,OPQ}, {OPQ,OPQ},
{OPQ,OPQ}, {OPQ,OPQ}, {OPQ,OPQ}, {OPQ,OPQ},
{OPQ,OPQ}, {OPQ,OPQ}, {OPQ,OPQ}, {OPQ,OPQ},
{OPQ,OPQ}, {OPQ,OPQ}, {OPQ,OPQ}, {OPQ,OPQ},
{OPQ,OPQ}, {OPQ,OPQ}, {OPQ,OPQ}, {OPQ,OPQ},
{OPQ,OPQ}, {OPQ,OPQ}, {OPQ,OPQ}, {OPQ,OPQ},
{OPQ,OPQ}, {OPQ,OPQ}, {OPQ,OPQ}, {OPQ,OPQ},
{OPQ,OPQ}, {OPQ,OPQ}, {OPQ,OPQ}, {OPQ,OPQ},
{OPQ,OPQ}, {OPQ,OPQ}, {OPQ,OPQ}, {OPQ,OPQ},
{OPQ,OPQ}, {OPQ,OPQ}, {OPQ,OPQ}, {OPQ,OPQ},
{OPQ,OPQ}, {OPQ,OPQ}, {OPQ,OPQ}, {OPQ,OPQ},
{OPQ,OPQ}, {OPQ,OPQ}, {OPQ,OPQ}, {OPQ,OPQ},
{OPQ,OPQ}, {OPQ,OPQ}, {OPQ,OPQ}, {OPQ,OPQ},
{OPQ,OPQ}, {OPQ,OPQ}, {OPQ,OPQ}, {OPQ,OPQ},
{OPQ,OPQ}, {OPQ,OPQ}, {OPQ,OPQ}, {OPQ,OPQ},
{OPQ,OPQ}, {OPQ,OPQ}, {OPQ,OPQ}, {OPQ,OPQ},
{OPQ,OPQ}, {OPQ,OPQ}, {OPQ,OPQ}, {OPQ,OPQ},
{OPQ,OPQ}, {OPQ,OPQ}, {OPQ,OPQ}, {OPQ,OPQ},
{OPQ,OPQ}, {OPQ,OPQ}, {OPQ,OPQ}, {OPQ,OPQ},
{OPQ,OPQ}, {OPQ,OPQ}, {OPQ,OPQ}, {OPQ,OPQ},
{OPQ,OPQ}, {OPQ,OPQ}, {OPQ,OPQ}, {OPQ,OPQ},
{OPQ,OPQ}, {OPQ,OPQ}, {OPQ,OPQ}, {OPQ,OPQ},
{OPQ,OPQ}, {OPQ,OPQ}, {OPQ,OPQ}, {OPQ,OPQ},
{OPQ,OPQ}, {OPQ,OPQ}, {OPQ,OPQ}, {OPQ,OPQ},
{OPQ,OPQ}, {OPQ,OPQ}, {OPQ,OPQ}, {OPQ,OPQ},
{OPQ,OPQ}, {OPQ,OPQ}, {OPQ,OPQ}, {OPQ,OPQ},
{OPQ,OPQ}, {OPQ,OPQ}, {OPQ,OPQ}, {OPQ,OPQ},
{OPQ,OPQ}, {OPQ,OPQ}, {OPQ,OPQ}, {OPQ,OPQ},
{OPQ,OPQ}, {OPQ,OPQ}, {OPQ,OPQ}, {OPQ,OPQ},
{OPQ,OPQ}, {OPQ,OPQ}, {OPQ,OPQ}, {OPQ,OPQ},
{OPQ,OPQ}, {OPQ,OPQ}, {OPQ,OPQ}, {OPQ,OPQ},
{OPQ,OPQ}, {OPQ,OPQ}, {OPQ,OPQ}, {OPQ,OPQ},
{OPQ,OPQ}, {OPQ,OPQ}, {OPQ,OPQ}, {OPQ,OPQ},
{OPQ,OPQ}, {OPQ,OPQ}, {OPQ,OPQ}, {OPQ,OPQ},
{OPQ,OPQ}, {OPQ,OPQ}, {OPQ,OPQ}, {OPQ,OPQ},
{OPQ,OPQ}, {OPQ,OPQ}, {OPQ,OPQ}, {OPQ,OPQ},
{OPQ,OPQ}, {OPQ,OPQ}, {OPQ,OPQ}, {OPQ,OPQ},
{OPQ,OPQ}, {OPQ,OPQ}, {OPQ,OPQ}, {OPQ,OPQ},
{OPQ,OPQ}, {OPQ,OPQ}, {OPQ,OPQ}, {OPQ,OPQ},
{OPQ,OPQ}, {OPQ,OPQ}, {OPQ,OPQ}, {OPQ,OPQ},
{OPQ,OPQ}, {OPQ,OPQ}, {OPQ,OPQ}, {OPQ,OPQ},
{OPQ,OPQ}, {OPQ,OPQ}, {OPQ,OPQ}, {OPQ,OPQ},
{OPQ,OPQ}, {OPQ,OPQ}, {OPQ,OPQ}, {OPQ,OPQ},
{OPQ,OPQ}, {OPQ,OPQ}, {OPQ,OPQ}, {OPQ,OPQ},
{OPQ,OPQ}, {OPQ,OPQ}, {OPQ,OPQ}, {OPQ,OPQ},
{OPQ,OPQ}, {OPQ,OPQ}, {OPQ,OPQ}, {OPQ,OPQ},
{OPQ,OPQ}, {OPQ,OPQ}, {OPQ,OPQ}, {OPQ,OPQ},
{OPQ,OPQ}, {OPQ,OPQ}, {OPQ,OPQ}, {OPQ,OPQ},
{OPQ,OPQ}, {OPQ,OPQ}, {OPQ,OPQ}, {OPQ,OPQ},
{OPQ,OPQ}, {OPQ,OPQ}, {OPQ,OPQ}, {OPQ,OPQ},
{OPQ,OPQ}, {OPQ,OPQ}, {OPQ,OPQ}, {OPQ,OPQ},
{OPQ,OPQ}, {OPQ,OPQ}, {OPQ,OPQ}, {OPQ,OPQ},
{OPQ,OPQ}, {OPQ,OPQ}, {OPQ,OPQ}, {OPQ,OPQ},
{OPQ,OPQ}, {OPQ,OPQ}, {OPQ,OPQ}, {OPQ,OPQ},
{OPQ,OPQ}, {OPQ,OPQ}, {OPQ,OPQ}, {OPQ,OPQ},
{OPQ,OPQ}, {OPQ,OPQ}, {OPQ,OPQ}, {OPQ,OPQ},
{OPQ,OPQ}, {OPQ,OPQ}, {OPQ,OPQ}, {OPQ,OPQ},
{OPQ,OPQ}, {OPQ,OPQ}, {OPQ,OPQ}, {OPQ,OPQ},
{OPQ,OPQ}, {OPQ,OPQ}, {OPQ,OPQ}, {OPQ,OPQ},
{OPQ,OPQ}, {OPQ,OPQ}, {OPQ,OPQ}, {OPQ,OPQ},
{OPQ,OPQ}, {OPQ,OPQ}, {OPQ,OPQ}, {OPQ,OPQ},
{OPQ,OPQ}, {OPQ,OPQ}, {OPQ,OPQ}, {OPQ,OPQ},
{OPQ,OPQ}, {OPQ,OPQ}, {OPQ,OPQ}, {OPQ,OPQ},
{OPQ,OPQ}, {OPQ,OPQ}, {OPQ,OPQ}, {OPQ,OPQ},
{OPQ,OPQ}, {OPQ,OPQ}, {OPQ,OPQ}, {OPQ,OPQ},
{OPQ,OPQ}, {OPQ,OPQ}, {OPQ,OPQ}, {OPQ,OPQ},
{OPQ,OPQ}, {OPQ,OPQ}, {OPQ,OPQ}, {OPQ,OPQ},
{OPQ,OPQ}, {OPQ,OPQ}, {OPQ,OPQ}, {OPQ,OPQ},
{OPQ,OPQ}, {OPQ,OPQ}, {OPQ,OPQ}, {OPQ,OPQ},
{OPQ,OPQ}, {OPQ,OPQ}, {OPQ,OPQ}, {OPQ,OPQ},
{OPQ,OPQ}, {OPQ,OPQ}, {OPQ,OPQ}, {OPQ,OPQ},
{OPQ,OPQ}, {OPQ,OPQ}, {OPQ,OPQ}, {OPQ,OPQ},
{OPQ,OPQ}, {OPQ,OPQ}, {OPQ,OPQ}, {OPQ,OPQ},
{OPQ,OPQ}, {OPQ,OPQ}, {OPQ,OPQ}, {OPQ,OPQ},
{OPQ,OPQ}, {OPQ,OPQ}, {OPQ,OPQ}, {OPQ,OPQ},
{OPQ,OPQ}, {OPQ,OPQ}, {OPQ,OPQ}, {OPQ,OPQ},
{OPQ,OPQ}, {OPQ,OPQ}, {OPQ,OPQ}, {OPQ,OPQ},
{OPQ,OPQ}, {OPQ,OPQ}, {OPQ,OPQ}, {OPQ,OPQ},
{OPQ,OPQ}, {OPQ,OPQ}, {OPQ,OPQ}, {OPQ,OPQ},
{OPQ,OPQ}, {OPQ,OPQ}, {OPQ,OPQ}, {OPQ,OPQ},
{OPQ,OPQ}, {OPQ,OPQ}, {OPQ,OPQ}, {OPQ,OPQ},
{OPQ,OPQ}, {OPQ,OPQ}, {OPQ,OPQ}, {OPQ,OPQ},
{OPQ,OPQ}, {OPQ,OPQ}, {OPQ,OPQ}, {OPQ,OPQ},
{OPQ,OPQ}, {OPQ,OPQ}, {OPQ,OPQ}, {OPQ,OPQ},
{OPQ,OPQ}, {OPQ,OPQ}, {OPQ,OPQ}, {OPQ,OPQ},
{OPQ,OPQ}, {OPQ,OPQ}, {OPQ,OPQ}, {OPQ,OPQ},
{OPQ,OPQ}, {OPQ,OPQ}, {OPQ,OPQ}, {OPQ,OPQ},
{OPQ,OPQ}, {OPQ,OPQ}, {OPQ,OPQ}, {OPQ,OPQ},
{OPQ,OPQ}, {OPQ,OPQ}, {OPQ,OPQ}, {OPQ,OPQ},
{OPQ,OPQ}, {OPQ,OPQ}, {OPQ,OPQ}, {OPQ,OPQ},
{OPQ,OPQ}, {OPQ,OPQ}, {OPQ,OPQ}, {OPQ,OPQ},
{OPQ,OPQ}, {OPQ,OPQ}, {OPQ,OPQ}, {OPQ,OPQ},
{OPQ,OPQ}, {OPQ,OPQ}, {OPQ,OPQ}, {OPQ,OPQ},
{OPQ,OPQ}, {OPQ,OPQ}, {OPQ,OPQ}, {OPQ,OPQ},
{OPQ,OPQ}, {OPQ,OPQ}, {OPQ,OPQ}, {OPQ,OPQ},
{OPQ,OPQ}, {OPQ,OPQ}, {OPQ,OPQ}, {OPQ,OPQ},
{OPQ,OPQ}, {OPQ,OPQ}, {OPQ,OPQ}, {OPQ,OPQ},
{OPQ,OPQ}, {OPQ,OPQ}, {OPQ,OPQ}, {OPQ,OPQ},
{OPQ,OPQ}, {OPQ,OPQ}, {OPQ,OPQ}, {OPQ,OPQ},
{OPQ,OPQ}, {OPQ,OPQ}, {OPQ,OPQ}, {OPQ,OPQ},
{OPQ,OPQ}, {OPQ,OPQ}, {OPQ,OPQ}, {OPQ,OPQ},
{OPQ,OPQ}, {OPQ,OPQ}, {OPQ,OPQ}, {OPQ,OPQ},
{OPQ,OPQ}, {OPQ,OPQ}, {OPQ,OPQ}, {OPQ,OPQ},
{OPQ,OPQ}, {OPQ,OPQ}, {OPQ,OPQ}, {OPQ,OPQ},
{OPQ,OPQ}, {OPQ,OPQ}, {OPQ,OPQ}, {OPQ,OPQ},
{OPQ,OPQ}, {OPQ,OPQ}, {OPQ,OPQ}, {OPQ,OPQ},
{OPQ,OPQ}, {OPQ,OPQ}, {OPQ,OPQ}, {OPQ,OPQ},
{OPQ,OPQ}, {OPQ,OPQ}, {OPQ,OPQ}, {OPQ,OPQ},
{OPQ,OPQ}, {OPQ,OPQ}, {OPQ,OPQ}, {OPQ,OPQ},
{OPQ,OPQ}, {OPQ,OPQ}, {OPQ,OPQ}, {OPQ,OPQ},
{OPQ,OPQ}, {OPQ,OPQ}, {OPQ,OPQ}, {OPQ,OPQ},
{OPQ,OPQ}, {OPQ,OPQ}, {OPQ,OPQ}, {OPQ,OPQ},
{OPQ,OPQ}, {OPQ,OPQ}, {OPQ,OPQ}, {OPQ,OPQ},
{OPQ,OPQ}, {OPQ,OPQ}, {OPQ,OPQ}, {OPQ,OPQ},
{OPQ,OPQ}, {OPQ,OPQ}, {OPQ,OPQ}, {OPQ,OPQ},
{OPQ,OPQ}, {OPQ,OPQ}, {OPQ,OPQ}, {OPQ,OPQ},
{OPQ,OPQ}, {OPQ,OPQ}, {OPQ,OPQ}, {OPQ,OPQ},
{OPQ,OPQ}, {OPQ,OPQ}, {OPQ,OPQ}, {OPQ,OPQ},
{OPQ,OPQ}, {OPQ,OPQ}, {OPQ,OPQ}, {OPQ,OPQ},
{OPQ,OPQ}, {OPQ,OPQ}, {OPQ,OPQ}, {OPQ,OPQ},
{OPQ,OPQ}, {OPQ,OPQ}, {OPQ,OPQ}, {OPQ,OPQ},
{OPQ,OPQ}, {OPQ,OPQ}, {OPQ,OPQ}, {OPQ,OPQ},
{OPQ,OPQ}, {OPQ,OPQ}, {OPQ,OPQ}, {OPQ,OPQ},
{OPQ,OPQ}, {OPQ,OPQ}, {OPQ,OPQ}, {OPQ,OPQ},
{OPQ,OPQ}, {OPQ,OPQ}, {OPQ,OPQ}, {OPQ,OPQ},
{OPQ,OPQ}, {OPQ,OPQ}, {OPQ,OPQ}, {OPQ,OPQ},
{OPQ,OPQ}, {OPQ,OPQ}, {OPQ,OPQ}, {OPQ,OPQ},
{OPQ,OPQ}, {OPQ,OPQ}, {OPQ,OPQ}, {OPQ,OPQ},
{OPQ,OPQ}, {OPQ,OPQ}, {OPQ,OPQ}, {OPQ,OPQ},
{OPQ,OPQ}, {OPQ,OPQ}, {OPQ,OPQ}, {OPQ,OPQ},
{OPQ,OPQ}, {OPQ,OPQ}, {OPQ,OPQ}, {OPQ,OPQ},
{OPQ,OPQ}, {OPQ,OPQ}, {OPQ,OPQ}, {OPQ,OPQ},
{OPQ,OPQ}, {OPQ,OPQ}, {OPQ,OPQ}, {OPQ,OPQ},
{OPQ,OPQ}, {OPQ,OPQ}, {OPQ,OPQ}, {OPQ,OPQ},
{OPQ,OPQ}, {OPQ,OPQ}, {OPQ,OPQ}, {OPQ,OPQ},
{OPQ,OPQ}, {OPQ,OPQ}, {OPQ,OPQ}, {OPQ,OPQ},
{OPQ,OPQ}, {OPQ,OPQ}, {OPQ,OPQ}, {OPQ,OPQ},
{OPQ,OPQ}, {OPQ,OPQ}, {OPQ,OPQ}, {OPQ,OPQ},
{OPQ,OPQ}, {OPQ,OPQ}, {OPQ,OPQ}, {OPQ,OPQ},
{OPQ,OPQ}, {OPQ,OPQ}, {OPQ,OPQ}, {OPQ,OPQ},
{OPQ,OPQ}, {OPQ,OPQ}, {OPQ,OPQ}, {OPQ,OPQ},
{OPQ,OPQ}, {OPQ,OPQ}, {OPQ,OPQ}, {OPQ,OPQ},
{OPQ,OPQ}, {OPQ,OPQ}, {OPQ,OPQ}, {OPQ,OPQ},
{OPQ,OPQ}, {OPQ,OPQ}, {OPQ,OPQ}, {OPQ,OPQ},
{OPQ,OPQ}, {OPQ,OPQ}, {OPQ,OPQ}, {OPQ,OPQ},
{OPQ,OPQ}, {OPQ,OPQ}, {OPQ,OPQ}, {OPQ,OPQ},
{OPQ,OPQ}, {OPQ,OPQ}, {OPQ,OPQ}, {OPQ,OPQ},
{OPQ,OPQ}, {OPQ,OPQ}, {OPQ,OPQ}, {OPQ,OPQ},
{OPQ,OPQ}, {OPQ,OPQ}, {OPQ,OPQ}, {OPQ,OPQ},
{OPQ,OPQ}, {OPQ,OPQ}, {OPQ,OPQ}, {OPQ,OPQ},
{OPQ,OPQ}, {OPQ,OPQ}, {OPQ,OPQ}, {OPQ,OPQ},
{OPQ,OPQ}, {OPQ,OPQ}, {OPQ,OPQ}, {OPQ,OPQ},
{OPQ,OPQ}, {OPQ,OPQ}, {OPQ,OPQ}, {OPQ,OPQ},
{OPQ,OPQ}, {OPQ,OPQ}, {OPQ,OPQ}, {OPQ,OPQ},
{OPQ,OPQ}, {OPQ,OPQ}, {OPQ,OPQ}, {OPQ,OPQ},
{OPQ,OPQ}, {OPQ,OPQ}, {OPQ,OPQ}, {OPQ,OPQ},
{OPQ,OPQ}, {OPQ,OPQ}, {OPQ,OPQ}, {OPQ,OPQ},
{OPQ,OPQ}, {OPQ,OPQ}, {OPQ,OPQ}, {OPQ,OPQ},
{OPQ,OPQ}, {OPQ,OPQ}, {OPQ,OPQ}, {OPQ,OPQ},
{OPQ,OPQ}, {OPQ,OPQ}, {OPQ,OPQ}, {OPQ,OPQ},
{OPQ,OPQ}, {OPQ,OPQ}, {OPQ,OPQ}, {OPQ,OPQ},
{OPQ,OPQ}, {OPQ,OPQ}, {OPQ,OPQ}, {OPQ,OPQ},
{OPQ,OPQ}, {OPQ,OPQ}, {OPQ,OPQ}, {OPQ,OPQ},
{OPQ,OPQ}, {OPQ,OPQ}, {OPQ,OPQ}, {OPQ,OPQ},
{OPQ,OPQ}, {OPQ,OPQ}, {OPQ,OPQ}, {OPQ,OPQ},
{OPQ,OPQ}, {OPQ,OPQ}, {OPQ,OPQ}, {OPQ,OPQ},
{OPQ,OPQ}, {OPQ,OPQ}, {OPQ,OPQ}, {OPQ,OPQ},
{OPQ,OPQ}, {OPQ,OPQ}, {OPQ,OPQ}, {OPQ,OPQ},
{OPQ,OPQ}, {OPQ,OPQ}, {OPQ,OPQ}, {OPQ,OPQ},
{OPQ,OPQ}, {OPQ,OPQ}, {OPQ,OPQ}, {OPQ,OPQ},
{OPQ,OPQ}, {OPQ,OPQ}, {OPQ,OPQ}, {OPQ,OPQ},
{OPQ,OPQ}, {OPQ,OPQ}, {OPQ,OPQ}, {OPQ,OPQ},
{OPQ,OPQ}, {OPQ,OPQ}, {OPQ,OPQ}, {OPQ,OPQ},
{OPQ,OPQ}, {OPQ,OPQ}, {OPQ,OPQ}, {OPQ,OPQ},
{OPQ,OPQ}, {OPQ,OPQ}, {OPQ,OPQ}, {OPQ,OPQ},
{OPQ,OPQ}, {OPQ,OPQ}, {OPQ,OPQ}, {OPQ,OPQ},
{OPQ,OPQ}, {OPQ,OPQ}, {OPQ,OPQ}, {OPQ,OPQ},
{OPQ,OPQ}, {OPQ,OPQ}, {OPQ,OPQ}, {OPQ,OPQ},
{OPQ,OPQ}, {OPQ,OPQ}, {OPQ,OPQ}, {OPQ,OPQ},
{OPQ,OPQ}, {OPQ,OPQ}, {OPQ,OPQ}, {OPQ,OPQ},
{OPQ,OPQ}, {OPQ,OPQ}, {OPQ,OPQ}, {OPQ,OPQ},
{OPQ,OPQ}, {OPQ,OPQ}, {OPQ,OPQ}, {OPQ,OPQ},
{OPQ,OPQ}, {OPQ,OPQ}, {OPQ,OPQ}, {OPQ,OPQ},
{OPQ,OPQ}, {OPQ,OPQ}, {OPQ,OPQ}, {OPQ,OPQ},
{OPQ,OPQ}, {OPQ,OPQ}, {OPQ,OPQ}, {OPQ,OPQ},
{OPQ,OPQ}, {OPQ,OPQ}, {OPQ,OPQ}, {OPQ,OPQ},
{OPQ,OPQ}, {OPQ,OPQ}, {OPQ,OPQ}, {OPQ,OPQ},
{OPQ,OPQ}, {OPQ,OPQ}, {OPQ,OPQ}, {OPQ,OPQ},
{OPQ,OPQ}, {OPQ,OPQ}, {OPQ,OPQ}, {OPQ,OPQ},
{OPQ,OPQ}, {OPQ,OPQ}, {OPQ,OPQ}, {OPQ,OPQ},
{OPQ,OPQ}, {OPQ,OPQ}, {OPQ,OPQ}, {OPQ,OPQ},
{OPQ,OPQ}, {OPQ,OPQ}, {OPQ,OPQ}, {OPQ,OPQ},
{OPQ,OPQ}, {OPQ,OPQ}, {OPQ,OPQ}, {OPQ,OPQ},
{OPQ,OPQ}, {OPQ,OPQ}, {OPQ,OPQ}, {OPQ,OPQ},
{OPQ,OPQ}, {OPQ,OPQ}, {OPQ,OPQ}, {OPQ,OPQ},
{OPQ,OPQ}, {OPQ,OPQ}, {OPQ,OPQ}, {OPQ,OPQ},
{OPQ,OPQ}, {OPQ,OPQ}, {OPQ,OPQ}, {OPQ,OPQ},
{OPQ,OPQ}, {OPQ,OPQ}, {OPQ,OPQ}, {OPQ,OPQ},
{OPQ,OPQ}, {OPQ,OPQ}, {OPQ,OPQ}, {OPQ,OPQ},
{OPQ,OPQ}, {OPQ,OPQ}, {OPQ,OPQ}, {OPQ,OPQ},
{OPQ,OPQ}, {OPQ,OPQ}, {OPQ,OPQ}, {OPQ,OPQ},
{OPQ,OPQ}, {OPQ,OPQ}, {OPQ,OPQ}, {OPQ,OPQ},
{OPQ,OPQ}, {OPQ,OPQ}, {OPQ,OPQ}, {OPQ,OPQ},
{OPQ,OPQ}, {OPQ,OPQ}, {OPQ,OPQ}, {OPQ,OPQ},
{OPQ,OPQ}, {OPQ,OPQ}, {OPQ,OPQ}, {OPQ,OPQ},
{OPQ,OPQ}, {OPQ,OPQ}, {OPQ,OPQ}, {OPQ,OPQ},
{OPQ,OPQ}, {OPQ,OPQ}, {OPQ,OPQ}, {OPQ,OPQ},
{OPQ,OPQ}, {OPQ,OPQ}, {OPQ,OPQ}, {OPQ,OPQ},
{OPQ,OPQ}, {OPQ,OPQ}, {OPQ,OPQ}, {OPQ,OPQ},
{OPQ,OPQ}, {OPQ,OPQ}, {OPQ,OPQ}, {OPQ,OPQ},
{OPQ,OPQ}, {OPQ,OPQ}, {OPQ,OPQ}, {OPQ,OPQ},
{OPQ,OPQ}, {OPQ,OPQ}, {OPQ,OPQ}, {OPQ,OPQ},
{OPQ,OPQ}, {OPQ,OPQ}, {OPQ,OPQ}, {OPQ,OPQ},
{OPQ,OPQ}, {OPQ,OPQ}, {OPQ,OPQ}, {OPQ,OPQ},
{OPQ,OPQ}, {OPQ,OPQ}, {OPQ,OPQ}, {OPQ,OPQ},
{OPQ,OPQ}, {OPQ,OPQ}, {OPQ,OPQ}, {OPQ,OPQ},
{OPQ,OPQ}, {OPQ,OPQ}, {OPQ,OPQ}, {OPQ,OPQ},
{OPQ,OPQ}, {OPQ,OPQ}, {OPQ,OPQ}, {OPQ,OPQ},
{OPQ,OPQ}, {OPQ,OPQ}, {OPQ,OPQ}, {OPQ,OPQ},
{OPQ,OPQ}, {OPQ,OPQ}, {OPQ,OPQ}, {OPQ,OPQ},
{OPQ,OPQ}, {OPQ,OPQ}, {OPQ,OPQ}, {OPQ,OPQ},
{OPQ,OPQ}, {OPQ,OPQ}, {OPQ,OPQ}, {OPQ,OPQ},
{OPQ,OPQ}, {OPQ,OPQ}, {OPQ,OPQ}, {OPQ,OPQ},
{OPQ,OPQ}, {OPQ,OPQ}, {OPQ,OPQ}, {OPQ,OPQ},
{OPQ,OPQ}, {OPQ,OPQ}, {OPQ,OPQ}, {OPQ,OPQ},
{OPQ,OPQ}, {OPQ,OPQ}, {OPQ,OPQ}, {OPQ,OPQ},
{OPQ,OPQ}, {OPQ,OPQ}, {OPQ,OPQ}, {OPQ,OPQ},
{OPQ,OPQ}, {OPQ,OPQ}, {OPQ,OPQ}, {OPQ,OPQ},
{OPQ,OPQ}, {OPQ,OPQ}, {OPQ,OPQ}, {OPQ,OPQ},
{OPQ,OPQ}, {OPQ,OPQ}, {OPQ,OPQ}, {OPQ,OPQ},
{OPQ,OPQ}, {OPQ,OPQ}, {OPQ,OPQ}, {OPQ,OPQ},
{OPQ,OPQ}, {OPQ,OPQ}, {OPQ,OPQ}, {OPQ,OPQ},
{OPQ,OPQ}, {OPQ,OPQ}, {OPQ,OPQ}, {OPQ,OPQ},
{OPQ,OPQ}, {OPQ,OPQ}, {OPQ,OPQ}, {OPQ,OPQ},
{OPQ,OPQ}, {OPQ,OPQ}, {OPQ,OPQ}, {OPQ,OPQ},
{OPQ,OPQ}, {OPQ,OPQ}, {OPQ,OPQ}, {OPQ,OPQ},
{OPQ,OPQ}, {OPQ,OPQ}, {OPQ,OPQ}, {OPQ,OPQ},
{OPQ,OPQ}, {OPQ,OPQ}, {OPQ,OPQ}, {OPQ,OPQ},
{OPQ,OPQ}, {OPQ,OPQ}, {OPQ,OPQ}, {OPQ,OPQ},
{OPQ,OPQ}, {OPQ,OPQ}, {OPQ,OPQ}, {OPQ,OPQ},
{OPQ,OPQ}, {OPQ,OPQ}, {OPQ,OPQ}, {OPQ,OPQ},
{OPQ,OPQ}, {OPQ,OPQ}, {OPQ,OPQ}, {OPQ,OPQ},
{OPQ,OPQ}, {OPQ,OPQ}, {OPQ,OPQ}, {OPQ,OPQ},
{OPQ,OPQ}, {OPQ,OPQ}, {OPQ,OPQ}, {OPQ,OPQ},
{OPQ,OPQ}, {OPQ,OPQ}, {OPQ,OPQ}, {OPQ,OPQ},
{OPQ,OPQ}, {OPQ,OPQ}, {OPQ,OPQ}, {OPQ,OPQ},
{OPQ,OPQ}, {OPQ,OPQ}, {OPQ,OPQ}, {OPQ,OPQ},
{OPQ,OPQ}, {OPQ,OPQ}, {OPQ,OPQ}, {OPQ,OPQ},
{OPQ,OPQ}, {OPQ,OPQ}, {OPQ,OPQ}, {OPQ,OPQ},
{OPQ,OPQ}, {OPQ,OPQ}, {OPQ,OPQ}, {OPQ,OPQ},
{OPQ,OPQ}, {OPQ,OPQ}, {OPQ,OPQ}, {OPQ,OPQ},
{OPQ,OPQ}, {OPQ,OPQ}, {OPQ,OPQ}, {OPQ,OPQ},
{OPQ,OPQ}, {OPQ,OPQ}, {OPQ,OPQ}, {OPQ,OPQ},
{OPQ,OPQ}, {OPQ,OPQ}, {OPQ,OPQ}, {OPQ,OPQ},
{OPQ,OPQ}, {OPQ,OPQ}, {OPQ,OPQ}, {OPQ,OPQ},
{OPQ,OPQ}, {OPQ,OPQ}, {OPQ,OPQ}, {OPQ,OPQ},
{OPQ,OPQ}, {OPQ,OPQ}, {OPQ,OPQ}, {OPQ,OPQ},
{OPQ,OPQ}, {OPQ,OPQ}, {OPQ,OPQ}, {OPQ,OPQ},
{OPQ,OPQ}, {OPQ,OPQ}, {OPQ,OPQ}, {OPQ,OPQ},
{OPQ,OPQ}, {OPQ,OPQ}, {OPQ,OPQ}, {OPQ,OPQ},
{OPQ,OPQ}, {OPQ,OPQ}, {OPQ,OPQ}, {OPQ,OPQ},
{OPQ,OPQ}, {OPQ,OPQ}, {OPQ,OPQ}, {OPQ,OPQ},
{OPQ,OPQ}, {OPQ,OPQ}, {OPQ,OPQ}, {OPQ,OPQ},
{OPQ,OPQ}, {OPQ,OPQ}, {OPQ,OPQ}, {OPQ,OPQ},
{OPQ,OPQ}, {OPQ,OPQ}, {OPQ,OPQ}, {OPQ,OPQ},
{OPQ,OPQ}, {OPQ,OPQ}, {OPQ,OPQ}, {OPQ,OPQ},
{OPQ,OPQ}, {OPQ,OPQ}, {OPQ,OPQ}, {OPQ,OPQ},
{OPQ,OPQ}, {OPQ,OPQ}, {OPQ,OPQ}, {OPQ,OPQ},
{OPQ,OPQ}, {OPQ,OPQ}, {OPQ,OPQ}, {OPQ,OPQ},
{OPQ,OPQ}, {OPQ,OPQ}, {OPQ,OPQ}, {OPQ,OPQ},
{OPQ,OPQ}, {OPQ,OPQ}, {OPQ,OPQ}, {OPQ,OPQ},
{OPQ,OPQ}, {OPQ,OPQ}, {OPQ,OPQ}, {OPQ,OPQ},
{OPQ,OPQ}, {OPQ,OPQ}, {OPQ,OPQ}, {OPQ,OPQ},
{OPQ,OPQ}, {OPQ,OPQ}, {OPQ,OPQ}, {OPQ,OPQ},
{OPQ,OPQ}, {OPQ,OPQ}, {OPQ,OPQ}, {OPQ,OPQ},
{OPQ,OPQ}, {OPQ,OPQ}, {OPQ,OPQ}, {OPQ,OPQ},
{OPQ,OPQ}, {OPQ,OPQ}, {OPQ,OPQ}, {OPQ,OPQ},
{OPQ,OPQ}, {OPQ,OPQ}, {OPQ,OPQ}, {OPQ,OPQ},
{OPQ,OPQ}, {OPQ,OPQ}, {OPQ,OPQ}, {OPQ,OPQ},
{OPQ,OPQ}, {OPQ,OPQ}, {OPQ,OPQ}, {OPQ,OPQ},
{OPQ,OPQ}, {OPQ,OPQ}, {OPQ,OPQ}, {OPQ,OPQ},
{OPQ,OPQ}, {OPQ,OPQ}, {OPQ,OPQ}, {OPQ,OPQ},
{OPQ,OPQ}, {OPQ,OPQ}, {OPQ,OPQ}, {OPQ,OPQ},
{OPQ,OPQ}, {OPQ,OPQ}, {OPQ,OPQ}, {OPQ,OPQ},
{OPQ,OPQ}, {OPQ,OPQ}, {OPQ,OPQ}, {OPQ,OPQ},
{OPQ,OPQ}, {OPQ,OPQ}, {OPQ,OPQ}, {OPQ,OPQ},
{OPQ,OPQ}, {OPQ,OPQ}, {OPQ,OPQ}, {OPQ,OPQ},
{OPQ,OPQ}, {OPQ,OPQ}, {OPQ,OPQ}, {OPQ,OPQ},
{OPQ,OPQ}, {OPQ,OPQ}, {OPQ,OPQ}, {OPQ,OPQ},
{OPQ,OPQ}, {OPQ,OPQ}, {OPQ,OPQ}, {OPQ,OPQ},
{OPQ,OPQ}, {OPQ,OPQ}, {OPQ,OPQ}, {OPQ,OPQ},
{OPQ,OPQ}, {OPQ,OPQ}, {OPQ,OPQ}, {OPQ,OPQ},
{OPQ,OPQ}, {OPQ,OPQ}, {OPQ,OPQ}, {OPQ,OPQ},
{OPQ,OPQ}, {OPQ,OPQ}, {OPQ,OPQ}, {OPQ,OPQ},
{OPQ,OPQ}, {OPQ,OPQ}, {OPQ,OPQ}, {OPQ,OPQ},
{OPQ,OPQ}, {OPQ,OPQ}, {OPQ,OPQ}, {OPQ,OPQ},
{OPQ,OPQ}, {OPQ,OPQ}, {OPQ,OPQ}, {OPQ,OPQ},
{OPQ,OPQ}, {OPQ,OPQ}, {OPQ,OPQ}, {OPQ,OPQ},
{OPQ,OPQ}, {OPQ,OPQ}, {OPQ,OPQ}, {OPQ,OPQ},
{OPQ,OPQ}, {OPQ,OPQ}, {OPQ,OPQ}, {OPQ,OPQ},
{OPQ,OPQ}, {OPQ,OPQ}, {OPQ,OPQ}, {OPQ,OPQ},
{OPQ,OPQ}, {OPQ,OPQ}, {OPQ,OPQ}, {OPQ,OPQ},
{OPQ,OPQ}, {OPQ,OPQ}, {OPQ,OPQ}, {OPQ,OPQ},
{OPQ,OPQ}, {OPQ,OPQ}, {OPQ,OPQ}, {OPQ,OPQ},
{OPQ,OPQ}, {OPQ,OPQ}, {OPQ,OPQ}, {OPQ,OPQ},
{OPQ,OPQ}, {OPQ,OPQ}, {OPQ,OPQ}, {OPQ,OPQ},
{OPQ,OPQ}, {OPQ,OPQ}, {OPQ,OPQ}, {OPQ,OPQ},
{OPQ,OPQ}, {OPQ,OPQ}, {OPQ,OPQ}, {OPQ,OPQ},
{OPQ,OPQ}, {OPQ,OPQ}, {OPQ,OPQ}, {OPQ,OPQ},
{OPQ,OPQ}, {OPQ,OPQ}, {OPQ,OPQ}, {OPQ,OPQ},
{OPQ,OPQ}, {OPQ,OPQ}, {OPQ,OPQ}, {OPQ,OPQ},
{OPQ,OPQ}, {OPQ,OPQ}, {OPQ,OPQ}, {OPQ,OPQ},
{OPQ,OPQ}, {OPQ,OPQ}, {OPQ,OPQ}, {OPQ,OPQ},
{OPQ,OPQ}, {OPQ,OPQ}, {OPQ,OPQ}, {OPQ,OPQ},
{OPQ,OPQ}, {OPQ,OPQ}, {OPQ,OPQ}, {OPQ,OPQ},
{OPQ,OPQ}, {OPQ,OPQ}, {OPQ,OPQ}, {OPQ,OPQ},
{OPQ,OPQ}, {OPQ,OPQ}, {OPQ,OPQ}, {OPQ,OPQ},
{OPQ,OPQ}, {OPQ,OPQ}, {OPQ,OPQ}, {OPQ,OPQ},
{OPQ,OPQ}, {OPQ,OPQ}, {OPQ,OPQ}, {OPQ,OPQ},
{OPQ,OPQ}, {OPQ,OPQ}, {OPQ,OPQ}, {OPQ,OPQ},
{OPQ,OPQ}, {OPQ,OPQ}, {OPQ,OPQ}, {OPQ,OPQ},
{OPQ,OPQ}, {OPQ,OPQ}, {OPQ,OPQ}, {OPQ,OPQ},
{OPQ,OPQ}, {OPQ,OPQ}, {OPQ,OPQ}, {OPQ,OPQ},
{OPQ,OPQ}, {OPQ,OPQ}, {OPQ,OPQ}, {OPQ,OPQ},
{OPQ,OPQ}, {OPQ,OPQ}, {OPQ,OPQ}, {OPQ,OPQ},
{OPQ,OPQ}, {OPQ,OPQ}, {OPQ,OPQ}, {OPQ,OPQ},
{OPQ,OPQ}, {OPQ,OPQ}, {OPQ,OPQ}, {OPQ,OPQ},
{OPQ,OPQ}, {OPQ,OPQ}, {OPQ,OPQ}, {OPQ,OPQ},
{OPQ,OPQ}, {OPQ,OPQ}, {OPQ,OPQ}, {OPQ,OPQ},
{OPQ,OPQ}, {OPQ,OPQ}, {OPQ,OPQ}, {OPQ,OPQ},
{OPQ,OPQ}, {OPQ,OPQ}, {OPQ,OPQ}, {OPQ,OPQ},
{OPQ,OPQ}, {OPQ,OPQ}, {OPQ,OPQ}, {OPQ,OPQ},
{OPQ,OPQ}, {OPQ,OPQ}, {OPQ,OPQ}, {OPQ,OPQ},
{OPQ,OPQ}, {OPQ,OPQ}, {OPQ,OPQ}, {OPQ,OPQ},
{OPQ,OPQ}, {OPQ,OPQ}, {OPQ,OPQ}, {OPQ,OPQ},
{OPQ,OPQ}, {OPQ,OPQ}, {OPQ,OPQ}, {OPQ,OPQ},
{OPQ,OPQ}, {OPQ,OPQ}, {OPQ,OPQ}, {OPQ,OPQ},
{OPQ,OPQ}, {OPQ,OPQ}, {OPQ,OPQ}, {OPQ,OPQ},
{OPQ,OPQ}, {OPQ,OPQ}, {OPQ,OPQ}, {OPQ,OPQ},
{OPQ,OPQ}, {OPQ,OPQ}, {OPQ,OPQ}, {OPQ,OPQ},
{OPQ,OPQ}, {OPQ,OPQ}, {OPQ,OPQ}, {OPQ,OPQ},
{OPQ,OPQ}, {OPQ,OPQ}, {OPQ,OPQ}, {OPQ,OPQ},
{OPQ,OPQ}, {OPQ,OPQ}, {OPQ,OPQ}, {OPQ,OPQ},
{OPQ,OPQ}, {OPQ,OPQ}, {OPQ,OPQ}, {OPQ,OPQ},
{OPQ,OPQ}, {OPQ,OPQ}, {OPQ,OPQ}, {OPQ,OPQ},
{OPQ,OPQ}, {OPQ,OPQ}, {OPQ,OPQ}, {OPQ,OPQ},
{OPQ,OPQ}, {OPQ,OPQ}, {OPQ,OPQ}, {OPQ,OPQ},
{OPQ,OPQ}, {OPQ,OPQ}, {OPQ,OPQ}, {OPQ,OPQ},
{OPQ,OPQ}, {OPQ,OPQ}, {OPQ,OPQ}, {OPQ,OPQ},
{OPQ,OPQ}, {OPQ,OPQ}, {OPQ,OPQ}, {OPQ,OPQ},
{OPQ,OPQ}, {OPQ,OPQ}, {OPQ,OPQ}, {OPQ,OPQ},
{OPQ,OPQ}, {OPQ,OPQ}, {OPQ,OPQ}, {OPQ,OPQ},
{OPQ,OPQ}, {OPQ,OPQ}, {OPQ,OPQ}, {OPQ,OPQ},
{OPQ,OPQ}, {OPQ,OPQ}, {OPQ,OPQ}, {OPQ,OPQ},
{OPQ,OPQ}, {OPQ,OPQ}, {OPQ,OPQ}, {OPQ,OPQ},
{OPQ,OPQ}, {OPQ,OPQ}, {OPQ,OPQ}, {OPQ,OPQ},
{OPQ,OPQ}, {OPQ,OPQ}, {OPQ,OPQ}, {OPQ,OPQ},
{OPQ,OPQ}, {OPQ,OPQ}, {OPQ,OPQ}, {OPQ,OPQ},
{OPQ,OPQ}, {OPQ,OPQ}, {OPQ,OPQ}, {OPQ,OPQ},
{OPQ,OPQ}, {OPQ,OPQ}, {OPQ,OPQ}, {OPQ,OPQ},
{OPQ,OPQ}, {OPQ,OPQ}, {OPQ,OPQ}, {OPQ,OPQ},
{OPQ,OPQ}, {OPQ,OPQ}, {OPQ,OPQ}, {OPQ,OPQ},
{OPQ,OPQ}, {OPQ,OPQ}, {OPQ,OPQ}, {OPQ,OPQ},
{OPQ,OPQ}, {OPQ,OPQ}, {OPQ,OPQ}, {OPQ,OPQ},
{OPQ,OPQ}, {OPQ,OPQ}, {OPQ,OPQ}, {OPQ,OPQ},
{OPQ,OPQ}, {OPQ,OPQ}, {OPQ,OPQ}, {OPQ,OPQ},
{OPQ,OPQ}, {OPQ,OPQ}, {OPQ,OPQ}, {OPQ,OPQ},
{OPQ,OPQ}, {OPQ,OPQ}, {OPQ,OPQ}, {OPQ,OPQ},
{OPQ,OPQ}, {OPQ,OPQ}, {OPQ,OPQ}, {OPQ,OPQ},
{OPQ,OPQ}, {OPQ,OPQ}, {OPQ,OPQ}, {OPQ,OPQ},
{OPQ,OPQ}, {OPQ,OPQ}, {OPQ,OPQ}, {OPQ,OPQ},
{OPQ,OPQ}, {OPQ,OPQ}, {OPQ,OPQ}, {OPQ,OPQ},
{OPQ,OPQ}, {OPQ,OPQ}, {OPQ,OPQ}, {OPQ,OPQ},
{OPQ,OPQ}, {OPQ,OPQ}, {OPQ,OPQ}, {OPQ,OPQ},
{OPQ,OPQ}, {OPQ,OPQ}, {OPQ,OPQ}, {OPQ,OPQ},
{OPQ,OPQ}, {OPQ,OPQ}, {OPQ,OPQ}, {OPQ,OPQ},
{OPQ,OPQ}, {OPQ,OPQ}, {OPQ,OPQ}, {OPQ,OPQ},
{OPQ,OPQ}, {OPQ,OPQ}, {OPQ,OPQ}, {OPQ,OPQ},
{OPQ,OPQ}, {OPQ,OPQ}, {OPQ,OPQ}, {OPQ,OPQ},
{OPQ,OPQ}, {OPQ,OPQ}, {OPQ,OPQ}, {OPQ,OPQ},
{OPQ,OPQ}, {OPQ,OPQ}, {OPQ,OPQ}, {OPQ,OPQ},
{OPQ,OPQ}, {OPQ,OPQ}, {OPQ,OPQ}, {OPQ,OPQ},
{OPQ,OPQ}, {OPQ,OPQ}, {OPQ,OPQ}, {OPQ,OPQ},
{OPQ,OPQ}, {OPQ,OPQ}, {OPQ,OPQ}, {OPQ,OPQ},
{OPQ,OPQ}, {OPQ,OPQ}, {OPQ,OPQ}, {OPQ,OPQ},
{OPQ,OPQ}, {OPQ,OPQ}, {OPQ,OPQ}, {OPQ,OPQ},
{OPQ,OPQ}, {OPQ,OPQ}, {OPQ,OPQ}, {OPQ,OPQ},
{OPQ,OPQ}, {OPQ,OPQ}, {OPQ,OPQ}, {OPQ,OPQ},
{OPQ,OPQ}, {OPQ,OPQ}, {OPQ,OPQ}, {OPQ,OPQ},
{OPQ,OPQ}, {OPQ,OPQ}, {OPQ,OPQ}, {OPQ,OPQ},
{OPQ,OPQ}, {OPQ,OPQ}, {OPQ,OPQ}, {OPQ,OPQ},
{OPQ,OPQ}, {OPQ,OPQ}, {OPQ,OPQ}, {OPQ,OPQ},
{OPQ,OPQ}, {OPQ,OPQ}, {OPQ,OPQ}, {OPQ,OPQ},
{OPQ,OPQ}, {OPQ,OPQ}, {OPQ,OPQ}, {OPQ,OPQ},
{OPQ,OPQ}, {OPQ,OPQ}, {OPQ,OPQ}, {OPQ,OPQ},
{OPQ,OPQ}, {OPQ,OPQ}, {OPQ,OPQ}, {OPQ,OPQ},
{OPQ,OPQ}, {OPQ,OPQ}, {OPQ,OPQ}, {OPQ,OPQ},
{OPQ,OPQ}, {OPQ,OPQ}, {OPQ,OPQ}, {OPQ,OPQ},
{OPQ,OPQ}, {OPQ,OPQ}, {OPQ,OPQ}, {OPQ,OPQ},
{OPQ,OPQ}, {OPQ,OPQ}, {OPQ,OPQ}, {OPQ,OPQ},
{OPQ,OPQ}, {OPQ,OPQ}, {OPQ,OPQ}, {OPQ,OPQ},
{OPQ,OPQ}, {OPQ,OPQ}, {OPQ,OPQ}, {OPQ,OPQ},
{OPQ,OPQ}, {OPQ,OPQ}, {OPQ,OPQ}, {OPQ,OPQ},
{OPQ,OPQ}, {OPQ,OPQ}, {OPQ,OPQ}, {OPQ,OPQ},
{OPQ,OPQ}, {OPQ,OPQ}, {OPQ,OPQ}, {OPQ,OPQ},
{OPQ,OPQ}, {OPQ,OPQ}, {OPQ,OPQ}, {OPQ,OPQ},
{OPQ,OPQ}, {OPQ,OPQ}, {OPQ,OPQ}, {OPQ,OPQ},
{OPQ,OPQ}, {OPQ,OPQ}, {OPQ,OPQ}, {OPQ,OPQ},
{OPQ,OPQ}, {OPQ,OPQ}, {OPQ,OPQ}, {OPQ,OPQ},
{OPQ,OPQ}, {OPQ,OPQ}, {OPQ,OPQ}, {OPQ,OPQ},
{OPQ,OPQ}, {OPQ,OPQ}, {OPQ,OPQ}, {OPQ,OPQ},
{OPQ,OPQ}, {OPQ,OPQ}, {OPQ,OPQ}, {OPQ,OPQ},
{OPQ,OPQ}, {OPQ,OPQ}, {OPQ,OPQ}, {OPQ,OPQ},
{OPQ,OPQ}, {OPQ,OPQ}, {OPQ,OPQ}, {OPQ,OPQ},
{OPQ,OPQ}, {OPQ,OPQ}, {OPQ,OPQ}, {OPQ,OPQ},
{OPQ,OPQ}, {OPQ,OPQ}, {OPQ,OPQ}, {OPQ,OPQ},
{OPQ,OPQ}, {OPQ,OPQ}, {OPQ,OPQ}, {OPQ,OPQ},
{OPQ,OPQ}, {OPQ,OPQ}, {OPQ,OPQ}, {OPQ,OPQ},
{OPQ,OPQ}, {OPQ,OPQ}, {OPQ,OPQ}, {OPQ,OPQ},
{OPQ,OPQ}, {OPQ,OPQ}, {OPQ,OPQ}, {OPQ,OPQ},
{OPQ,OPQ}, {OPQ,OPQ}, {OPQ,OPQ}, {OPQ,OPQ},
{OPQ,OPQ}, {OPQ,OPQ}, {OPQ,OPQ}, {OPQ,OPQ},
{OPQ,OPQ}, {OPQ,OPQ}, {OPQ,OPQ}, {OPQ,OPQ},
{OPQ,OPQ}, {OPQ,OPQ}, {OPQ,OPQ}, {OPQ,OPQ},
{OPQ,OPQ}, {OPQ,OPQ}, {OPQ,OPQ}, {OPQ,OPQ},
{OPQ,OPQ}, {OPQ,OPQ}, {OPQ,OPQ}, {OPQ,OPQ},
{OPQ,OPQ}, {OPQ,OPQ}, {OPQ,OPQ}, {OPQ,OPQ},
{OPQ,OPQ}, {OPQ,OPQ}, {OPQ,OPQ}, {OPQ,OPQ},
{OPQ,OPQ}, {OPQ,OPQ}, {OPQ,OPQ}, {OPQ,OPQ},
{OPQ,OPQ}, {OPQ,OPQ}, {OPQ,OPQ}, {OPQ,OPQ},
{OPQ,OPQ}, {OPQ,OPQ}, {OPQ,OPQ}, {OPQ,OPQ},
{OPQ,OPQ}, {OPQ,OPQ}, {OPQ,OPQ}, {OPQ,OPQ},
{OPQ,OPQ}, {OPQ,OPQ}, {OPQ,OPQ}, {OPQ,OPQ},
{OPQ,OPQ}, {OPQ,OPQ}, {OPQ,OPQ}, {OPQ,OPQ},
{OPQ,OPQ}, {OPQ,OPQ}, {OPQ,OPQ}, {OPQ,OPQ},
{OPQ,OPQ}, {OPQ,OPQ}, {OPQ,OPQ}, {OPQ,OPQ},
{OPQ,OPQ}, {OPQ,OPQ}, {OPQ,OPQ}, {OPQ,OPQ},
{OPQ,OPQ}, {OPQ,OPQ}, {OPQ,OPQ}, {OPQ,OPQ},
{OPQ,OPQ}, {OPQ,OPQ}, {OPQ,OPQ}, {OPQ,OPQ},
{OPQ,OPQ}, {OPQ,OPQ}, {OPQ,OPQ}, {OPQ,OPQ},
{OPQ,OPQ}, {OPQ,OPQ}, {OPQ,OPQ}, {OPQ,OPQ},
{OPQ,OPQ}, {OPQ,OPQ}, {OPQ,OPQ}, {OPQ,OPQ},
{OPQ,OPQ}, {OPQ,OPQ}, {OPQ,OPQ}, {OPQ,OPQ},
{OPQ,OPQ}, {OPQ,OPQ}, {OPQ,OPQ}, {OPQ,OPQ},
{OPQ,OPQ}, {OPQ,OPQ}, {OPQ,OPQ}, {OPQ,OPQ},
{OPQ,OPQ}, {OPQ,OPQ}, {OPQ,OPQ}, {OPQ,OPQ},
{OPQ,OPQ}, {OPQ,OPQ}, {OPQ,OPQ}, {OPQ,OPQ},
{OPQ,OPQ}, {OPQ,OPQ}, {OPQ,OPQ}, {OPQ,OPQ},
{OPQ,OPQ}, {OPQ,OPQ}, {OPQ,OPQ}, {OPQ,OPQ},
{OPQ,OPQ}, {OPQ,OPQ}, {OPQ,OPQ}, {OPQ,OPQ},
{OPQ,OPQ}, {OPQ,OPQ}, {OPQ,OPQ}, {OPQ,OPQ},
{OPQ,OPQ}, {OPQ,OPQ}, {OPQ,OPQ}, {OPQ,OPQ},
{OPQ,OPQ}, {OPQ,OPQ}, {OPQ,OPQ}, {OPQ,OPQ},
{OPQ,OPQ}, {OPQ,OPQ}, {OPQ,OPQ}, {OPQ,OPQ},
{OPQ,OPQ}, {OPQ,OPQ}, {OPQ,OPQ}, {OPQ,OPQ},
{OPQ,OPQ}, {OPQ,OPQ}, {OPQ,OPQ}, {OPQ,OPQ},
};

__device__ __forceinline__ float bf2f(u16 v) { return __uint_as_float(((unsigned)v) << 16); }
__device__ __forceinline__ u16 f2bf(float f) {
  __hip_bfloat16 b = __float2bfloat16(f);
  return *reinterpret_cast<u16*>(&b);
}
__device__ __forceinline__ float bfr(float f) { return bf2f(f2bf(f)); }
__device__ __forceinline__ int row_of_pos(int p) { return 4 * (p & 511) + (p >> 9); }

__device__ __forceinline__ float2 cadd(float2 a, float2 b) { return make_float2(a.x + b.x, a.y + b.y); }
__device__ __forceinline__ float2 csub(float2 a, float2 b) { return make_float2(a.x - b.x, a.y - b.y); }
__device__ __forceinline__ float2 cmul(float2 a, float2 b) {
  return make_float2(a.x * b.x - a.y * b.y, a.x * b.y + a.y * b.x);
}
template <bool INV>
__device__ __forceinline__ float2 twmul(float2 a, float2 w) {
  if (INV) return make_float2(a.x * w.x + a.y * w.y, a.y * w.x - a.x * w.y);
  else return make_float2(a.x * w.x - a.y * w.y, a.x * w.y + a.y * w.x);
}
template <bool INV>
__device__ __forceinline__ float2 mul_mi(float2 a) {
  if (INV) return make_float2(-a.y, a.x);
  else return make_float2(a.y, -a.x);
}
template <bool INV>
__device__ __forceinline__ void fft4(float2& v0, float2& v1, float2& v2, float2& v3) {
  float2 a0 = cadd(v0, v2), a1 = csub(v0, v2);
  float2 b0 = cadd(v1, v3), b1 = mul_mi<INV>(csub(v1, v3));
  v0 = cadd(a0, b0);
  v2 = csub(a0, b0);
  v1 = cadd(a1, b1);
  v3 = csub(a1, b1);
}
template <bool INV>
__device__ __forceinline__ void fft8(float2 (&v)[8]) {
  const float r = 0.70710678118654752440f;
  float2 e0 = cadd(v[0], v[4]), o0 = csub(v[0], v[4]);
  float2 e1 = cadd(v[1], v[5]), o1 = csub(v[1], v[5]);
  float2 e2 = cadd(v[2], v[6]), o2 = csub(v[2], v[6]);
  float2 e3 = cadd(v[3], v[7]), o3 = csub(v[3], v[7]);
  if (INV) {
    o1 = make_float2((o1.x - o1.y) * r, (o1.x + o1.y) * r);
    o3 = make_float2((-o3.x - o3.y) * r, (o3.x - o3.y) * r);
  } else {
    o1 = make_float2((o1.x + o1.y) * r, (o1.y - o1.x) * r);
    o3 = make_float2((o3.y - o3.x) * r, (-o3.x - o3.y) * r);
  }
  o2 = mul_mi<INV>(o2);
  fft4<INV>(e0, e1, e2, e3);
  fft4<INV>(o0, o1, o2, o3);
  v[0] = e0; v[2] = e1; v[4] = e2; v[6] = e3;
  v[1] = o0; v[3] = o1; v[5] = o2; v[7] = o3;
}
__device__ __forceinline__ int padi(int i) { return i + (i >> 4); }

template <int CTRL>
__device__ __forceinline__ float dpp_mov(float v) {
  return __int_as_float(__builtin_amdgcn_update_dpp(0, __float_as_int(v), CTRL, 0xF, 0xF, true));
}
__device__ __forceinline__ float wave_sum(float v) {
  v += dpp_mov<0xB1>(v);
  v += dpp_mov<0x4E>(v);
  v += dpp_mov<0x141>(v);
  v += dpp_mov<0x140>(v);
  const float a = __int_as_float(__builtin_amdgcn_readlane(__float_as_int(v), 0));
  const float b = __int_as_float(__builtin_amdgcn_readlane(__float_as_int(v), 16));
  const float c = __int_as_float(__builtin_amdgcn_readlane(__float_as_int(v), 32));
  const float d = __int_as_float(__builtin_amdgcn_readlane(__float_as_int(v), 48));
  return (a + b) + (c + d);
}

__device__ __forceinline__ void ln_rows2(float (&xa)[8], float (&xb)[8]) {
  float sa = 0.f, sb = 0.f;
#pragma unroll
  for (int m = 0; m < 8; ++m) { sa += xa[m]; sb += xb[m]; }
  sa = wave_sum(sa);
  sb = wave_sum(sb);
  const float ma = sa * (1.0f / 512.0f), mbn = sb * (1.0f / 512.0f);
  float qa = 0.f, qb = 0.f;
#pragma unroll
  for (int m = 0; m < 8; ++m) {
    float da = xa[m] - ma, db = xb[m] - mbn;
    qa += da * da; qb += db * db;
  }
  qa = wave_sum(qa);
  qb = wave_sum(qb);
  const float mab = bfr(ma), mbb = bfr(mbn);
  const float dena = bfr(sqrtf(bfr(bfr(qa * (1.0f / 512.0f)) + 1e-5f)));
  const float denb = bfr(sqrtf(bfr(bfr(qb * (1.0f / 512.0f)) + 1e-5f)));
  const float ra = __builtin_amdgcn_rcpf(dena), rb = __builtin_amdgcn_rcpf(denb);
#pragma unroll
  for (int m = 0; m < 8; ++m) {
    const float na = bfr(xa[m] - mab), nb = bfr(xb[m] - mbb);
    float q0 = na * ra, q1 = nb * rb;
    q0 = fmaf(fmaf(-q0, dena, na), ra, q0);
    q1 = fmaf(fmaf(-q1, denb, nb), rb, q1);
    xa[m] = bfr(q0);
    xb[m] = bfr(q1);
  }
}

template <bool INV>
__device__ __forceinline__ void wave_fft512(float2 (&v)[8], float2* ex, int j, const float2 (&tw1)[7],
                                            const float2 (&tw2)[7]) {
  fft8<INV>(v);
#pragma unroll
  for (int m = 0; m < 8; ++m) ex[padi(j * 8 + m)] = v[m];
  __builtin_amdgcn_wave_barrier();
#pragma unroll
  for (int m = 0; m < 8; ++m) v[m] = ex[padi(j + 64 * m)];
  __builtin_amdgcn_wave_barrier();
#pragma unroll
  for (int m = 1; m < 8; ++m) v[m] = twmul<INV>(v[m], tw1[m - 1]);
  fft8<INV>(v);
#pragma unroll
  for (int m = 0; m < 8; ++m) ex[padi((j >> 3) * 64 + (j & 7) + 8 * m)] = v[m];
  __builtin_amdgcn_wave_barrier();
#pragma unroll
  for (int m = 0; m < 8; ++m) v[m] = ex[padi(j + 64 * m)];
  __builtin_amdgcn_wave_barrier();
#pragma unroll
  for (int m = 1; m < 8; ++m) v[m] = twmul<INV>(v[m], tw2[m - 1]);
  fft8<INV>(v);
}

#define EXW 544
#define TSTR 10

__global__ __launch_bounds__(256) void k1_ln_rfft(const u16* __restrict__ x, const u16* __restrict__ wr,
                                                  const u16* __restrict__ wi, float2* __restrict__ spec,
                                                  unsigned* __restrict__ wt DIAG_PARAM) {
  __shared__ __attribute__((aligned(16))) float2 sm[4 * EXW + 256 * TSTR];
  const int t = threadIdx.x;
  STAMP(0, 0);
  if (blockIdx.x >= 256) {
    const int m0 = (blockIdx.x - 256) * 8;
    unsigned p[8];
#pragma unroll
    for (int r = 0; r < 8; ++r)
      p[r] = (unsigned)wr[(m0 + r) * DF + t] | ((unsigned)wi[(m0 + r) * DF + t] << 16);
    uint4* dst = reinterpret_cast<uint4*>(wt + t * S_LEN + m0);
    dst[0] = make_uint4(p[0], p[1], p[2], p[3]);
    dst[1] = make_uint4(p[4], p[5], p[6], p[7]);
    if (t < 8) wt[256 * S_LEN + m0 + t] = (unsigned)wr[(m0 + t) * DF + 256] | ((unsigned)wi[(m0 + t) * DF + 256] << 16);
    STAMP(0, 8);
    return;
  }
  const int w = t >> 6, j = t & 63;
  const int p0 = blockIdx.x * 8;
  const int ra = row_of_pos(p0 + 2 * w), rb = row_of_pos(p0 + 2 * w + 1);
  float2* ex = sm + w * EXW;
  float2* tile = sm + 4 * EXW;

  float xa[8], xb[8];
#pragma unroll
  for (int m = 0; m < 8; ++m) {
    xa[m] = bf2f(x[ra * D_DIM + j + 64 * m]);
    xb[m] = bf2f(x[rb * D_DIM + j + 64 * m]);
  }
  float2 tw1[7], tw2[7];
#pragma unroll
  for (int m = 1; m < 8; ++m) {
    tw1[m - 1] = g_tw[32 * (j & 7) * m];
    tw2[m - 1] = g_tw[4 * j * m];
  }
  STAMPW(0, 1);
  ln_rows2(xa, xb);
  float2 v[8];
#pragma unroll
  for (int m = 0; m < 8; ++m) v[m] = make_float2(xa[m], xb[m]);
  STAMP(0, 2);
  wave_fft512<false>(v, ex, j, tw1, tw2);
  STAMP(0, 3);
  __builtin_amdgcn_wave_barrier();
#pragma unroll
  for (int m = 4; m < 8; ++m) ex[padi(j + 64 * m)] = v[m];
  if (j == 0) ex[padi(0)] = v[0];
  __builtin_amdgcn_wave_barrier();
#pragma unroll
  for (int m = 0; m < 4; ++m) {
    const int k = j + 64 * m;
    const float2 zc = ex[padi((512 - k) & 511)];
    float4 o;
    if (k == 0) {
      const float2 zn = ex[padi(256)];
      o = make_float4(v[0].x, zn.x, v[0].y, zn.y);
    } else {
      const float2 z = v[m];
      o.x = 0.5f * (z.x + zc.x);
      o.y = 0.5f * (z.y - zc.y);
      o.z = 0.5f * (z.y + zc.y);
      o.w = 0.5f * (zc.x - z.x);
    }
    *reinterpret_cast<float4*>(tile + k * TSTR + 2 * w) = o;
  }
  __syncthreads();
#pragma unroll
  for (int i = 0; i < 4; ++i) {
    const int idx = t + 256 * i;
    const int k = idx >> 2, part = idx & 3;
    const float4 o = *reinterpret_cast<const float4*>(tile + k * TSTR + 2 * part);
    *reinterpret_cast<float4*>(spec + k * S_LEN + p0 + 2 * part) = o;
  }
  STAMP(0, 4);
  STAMPW(0, 5);
}

__device__ __forceinline__ float2 unpack_w(unsigned p) {
  return make_float2(__uint_as_float(p << 16), __uint_as_float(p & 0xffff0000u));
}

__global__ __launch_bounds__(256) void k2_colfft(float2* __restrict__ spec, const unsigned* __restrict__ wt,
                                                 const u16* __restrict__ wg, const u16* __restrict__ wu,
                                                 const u16* __restrict__ wd, uint4* __restrict__ wgu_t,
                                                 uint4* __restrict__ wd_t DIAG_PARAM) {
  __shared__ __attribute__((aligned(16))) float2 sm[4 * EXW + 3 * 2048];
  const int t = threadIdx.x;
  STAMP(1, 0);
  if (blockIdx.x >= 256) {
    const int g = (blockIdx.x - 256) * 256 + t;
    const int lane = g & 63, q = lane >> 4, c = lane & 15;
    if (g < NT16 * 16 * 2 * 64) {
      const int f = g >> 6;
      const int which = f & 1, s = (f >> 1) & 15, jt = f >> 5;
      const u16* src = (which ? wu : wg) + (32 * s + 8 * q) * F_DIM + 16 * jt + c;
      unsigned p[4];
#pragma unroll
      for (int i = 0; i < 4; ++i)
        p[i] = (unsigned)src[(2 * i) * F_DIM] | ((unsigned)src[(2 * i + 1) * F_DIM] << 16);
      wgu_t[g] = make_uint4(p[0], p[1], p[2], p[3]);
    } else {
      const int g2 = g - NT16 * 16 * 2 * 64;
      if (g2 < 32 * KS2 * 64) {
        const int f = g2 >> 6;
        const int s = f % KS2, nt = f / KS2;
        const u16* src0 = wd + (32 * s + 4 * q) * D_DIM + 16 * nt + c;
        const u16* src1 = src0 + 16 * D_DIM;
        unsigned p[4];
        p[0] = (unsigned)src0[0] | ((unsigned)src0[D_DIM] << 16);
        p[1] = (unsigned)src0[2 * D_DIM] | ((unsigned)src0[3 * D_DIM] << 16);
        p[2] = (unsigned)src1[0] | ((unsigned)src1[D_DIM] << 16);
        p[3] = (unsigned)src1[2 * D_DIM] | ((unsigned)src1[3 * D_DIM] << 16);
        wd_t[g2] = make_uint4(p[0], p[1], p[2], p[3]);
      }
    }
    STAMP(1, 8);
    return;
  }
  const int k = blockIdx.x;
  const int w = t >> 6, j = t & 63;
  float2* ex = sm + w * EXW;
  float2* xb1 = sm + 4 * EXW;
  float2* xb2 = xb1 + 2048;
  float2* zb = xb2 + 2048;
  float2* col = spec + k * S_LEN;
  float2 v[8];
#pragma unroll
  for (int m = 0; m < 8; ++m) v[m] = col[w * 512 + j + 64 * m];
  const int k0 = j + 128 * w;
  unsigned wp[8];
#pragma unroll
  for (int q = 0; q < 4; ++q) {
    wp[q] = wt[k * S_LEN + k0 + 512 * q];
    wp[4 + q] = wt[k * S_LEN + k0 + 64 + 512 * q];
  }
  float2 tw1[7], tw2[7], twa[3], twb[3];
#pragma unroll
  for (int m = 1; m < 8; ++m) {
    tw1[m - 1] = g_tw[32 * (j & 7) * m];
    tw2[m - 1] = g_tw[4 * j * m];
  }
#pragma unroll
  for (int ww = 1; ww < 4; ++ww) {
    twa[ww - 1] = g_tw[k0 * ww];
    twb[ww - 1] = g_tw[(k0 + 64) * ww];
  }
  STAMPW(1, 1);
  wave_fft512<false>(v, ex, j, tw1, tw2);
#pragma unroll
  for (int m = 0; m < 8; ++m) xb1[w * 512 + m * 64 + j] = v[m];
  __syncthreads();
  float2 a[4], b[4];
#pragma unroll
  for (int ww = 0; ww < 4; ++ww) {
    a[ww] = xb1[ww * 512 + (2 * w) * 64 + j];
    b[ww] = xb1[ww * 512 + (2 * w + 1) * 64 + j];
  }
#pragma unroll
  for (int ww = 1; ww < 4; ++ww) {
    a[ww] = twmul<false>(a[ww], twa[ww - 1]);
    b[ww] = twmul<false>(b[ww], twb[ww - 1]);
  }
  fft4<false>(a[0], a[1], a[2], a[3]);
  fft4<false>(b[0], b[1], b[2], b[3]);
  STAMP(1, 2);
  if (k != 0) {
#pragma unroll
    for (int q = 0; q < 4; ++q) {
      a[q] = cmul(a[q], unpack_w(wp[q]));
      b[q] = cmul(b[q], unpack_w(wp[4 + q]));
    }
  } else {
#pragma unroll
    for (int q = 0; q < 4; ++q) {
      zb[k0 + 512 * q] = a[q];
      zb[k0 + 64 + 512 * q] = b[q];
    }
    __syncthreads();
#pragma unroll
    for (int i = 0; i < 8; ++i) {
      const int f = k0 + (i >> 2) * 64 + 512 * (i & 3), fn = (2048 - f) & 2047;
      const float2 z = (i < 4) ? a[i & 3] : b[i & 3];
      const float2 zc = zb[fn];
      const float2 x0 = make_float2(0.5f * (z.x + zc.x), 0.5f * (z.y - zc.y));
      const float2 x1 = make_float2(0.5f * (z.y + zc.y), 0.5f * (zc.x - z.x));
      const float2 w0 = unpack_w(wp[i]), w0n = unpack_w(wt[fn]);
      const float2 w1 = unpack_w(wt[256 * S_LEN + f]), w1n = unpack_w(wt[256 * S_LEN + fn]);
      const float2 g0 = make_float2(0.5f * (w0.x + w0n.x), 0.5f * (w0.y - w0n.y));
      const float2 g1 = make_float2(0.5f * (w1.x + w1n.x), 0.5f * (w1.y - w1n.y));
      const float2 a0 = cmul(x0, g0), a1 = cmul(x1, g1);
      const float2 r = make_float2(a0.x - a1.y, a0.y + a1.x);
      if (i < 4) a[i & 3] = r; else b[i & 3] = r;
    }
  }
  STAMP(1, 3);
  fft4<true>(a[0], a[1], a[2], a[3]);
  fft4<true>(b[0], b[1], b[2], b[3]);
#pragma unroll
  for (int ww = 1; ww < 4; ++ww) {
    a[ww] = twmul<true>(a[ww], twa[ww - 1]);
    b[ww] = twmul<true>(b[ww], twb[ww - 1]);
  }
#pragma unroll
  for (int ww = 0; ww < 4; ++ww) {
    xb2[ww * 512 + (2 * w) * 64 + j] = a[ww];
    xb2[ww * 512 + (2 * w + 1) * 64 + j] = b[ww];
  }
  __syncthreads();
#pragma unroll
  for (int m = 0; m < 8; ++m) v[m] = xb2[w * 512 + m * 64 + j];
  wave_fft512<true>(v, ex, j, tw1, tw2);
#pragma unroll
  for (int m = 0; m < 8; ++m) col[w * 512 + j + 64 * m] = v[m];
  STAMP(1, 4);
  STAMPW(1, 5);
}

__global__ __launch_bounds__(256) void k3_irfft_ln(const float2* __restrict__ spec, const u16* __restrict__ x,
                                                   u16* __restrict__ h, u16* __restrict__ xn_t DIAG_PARAM) {
  __shared__ __attribute__((aligned(16))) float2 sm[4 * EXW + 256 * TSTR];
  const int t = threadIdx.x;
  STAMP(2, 0);
  const int w = t >> 6, j = t & 63;
  const int p0 = blockIdx.x * 8;
  const int ra = row_of_pos(p0 + 2 * w), rb = row_of_pos(p0 + 2 * w + 1);
  float2* ex = sm + w * EXW;
  float2* tile = sm + 4 * EXW;
  float4 ld[4];
#pragma unroll
  for (int i = 0; i < 4; ++i) {
    const int idx = t + 256 * i;
    const int k = idx >> 2, part = idx & 3;
    ld[i] = *reinterpret_cast<const float4*>(spec + k * S_LEN + p0 + 2 * part);
  }
  float xa[8], xb[8];
#pragma unroll
  for (int m = 0; m < 8; ++m) {
    xa[m] = bf2f(x[ra * D_DIM + j + 64 * m]);
    xb[m] = bf2f(x[rb * D_DIM + j + 64 * m]);
  }
  float2 tw1[7], tw2[7];
#pragma unroll
  for (int m = 1; m < 8; ++m) {
    tw1[m - 1] = g_tw[32 * (j & 7) * m];
    tw2[m - 1] = g_tw[4 * j * m];
  }
#pragma unroll
  for (int i = 0; i < 4; ++i) {
    const int idx = t + 256 * i;
    const int k = idx >> 2, part = idx & 3;
    *reinterpret_cast<float4*>(tile + k * TSTR + 2 * part) = ld[i];
  }
  __syncthreads();
  STAMP(2, 1);
  float2 v[8];
#pragma unroll
  for (int m = 0; m < 8; ++m) {
    const int n = j + 64 * m;
    const int k = (m < 4) ? n : (512 - n);
    const float4 y = *reinterpret_cast<const float4*>(tile + (k & 255) * TSTR + 2 * w);
    if (n == 0) v[m] = make_float2(y.x, y.z);
    else if (n == 256) v[m] = make_float2(y.y, y.w);
    else if (m < 4) v[m] = make_float2(y.x - y.w, y.y + y.z);
    else v[m] = make_float2(y.x + y.w, y.z - y.y);
  }
  wave_fft512<true>(v, ex, j, tw1, tw2);
  STAMP(2, 2);
  const float sc = 1.0f / (2048.0f * 512.0f);
#pragma unroll
  for (int m = 0; m < 8; ++m) {
    xa[m] = bfr(xa[m] + bfr(v[m].x * sc));
    xb[m] = bfr(xb[m] + bfr(v[m].y * sc));
  }
  u16* st_h = reinterpret_cast<u16*>(ex);
  u16* st_x = st_h + 2 * 512;
  __builtin_amdgcn_wave_barrier();
#pragma unroll
  for (int m = 0; m < 8; ++m) {
    st_h[j + 64 * m] = f2bf(xa[m]);
    st_h[512 + j + 64 * m] = f2bf(xb[m]);
  }
  ln_rows2(xa, xb);
#pragma unroll
  for (int m = 0; m < 8; ++m) {
    st_x[j + 64 * m] = f2bf(xa[m]);
    st_x[512 + j + 64 * m] = f2bf(xb[m]);
  }
  __builtin_amdgcn_wave_barrier();
  STAMP(2, 3);
  {
    const int pr = p0 + 2 * w;
    uint4* dst = reinterpret_cast<uint4*>(h + pr * D_DIM);
    dst[j] = reinterpret_cast<const uint4*>(st_h)[j];
    dst[64 + j] = reinterpret_cast<const uint4*>(st_h)[64 + j];
    const int mt = pr >> 4, r16 = pr & 15;
    uint4* xd = reinterpret_cast<uint4*>(xn_t) + (mt * 16 + (j >> 2)) * 64 + (j & 3) * 16 + r16;
    xd[0] = reinterpret_cast<const uint4*>(st_x)[j];
    xd[1] = reinterpret_cast<const uint4*>(st_x)[64 + j];
  }
  STAMP(2, 4);
  STAMPW(2, 5);
}

__device__ __forceinline__ unsigned geglu2(float g0, float g1, float u0, float u1) {
  g0 = bfr(g0); g1 = bfr(g1); u0 = bfr(u0); u1 = bfr(u1);
  const float c1 = -2.0f * 0.7978845608028654f * 1.4426950408889634f;
  const float c2 = c1 * 0.044715f;
  const float z0 = g0 * fmaf(g0 * g0, c2, c1), z1 = g1 * fmaf(g1 * g1, c2, c1);
  const float e0 = __builtin_amdgcn_exp2f(z0), e1 = __builtin_amdgcn_exp2f(z1);
  const float a0 = bfr(g0 * __builtin_amdgcn_rcpf(1.0f + e0));
  const float a1 = bfr(g1 * __builtin_amdgcn_rcpf(1.0f + e1));
  return (unsigned)f2bf(a0 * u0) | ((unsigned)f2bf(a1 * u1) << 16);
}

template <int NT>
__device__ __forceinline__ void k4_body(uint4* __restrict__ As, const uint4* __restrict__ xn_src,
                                        const uint4* __restrict__ wgu_t, uint2* __restrict__ act_t, int mt64,
                                        int jt0, int t, int lane DIAG_PARAM) {
  f32x4 ag[NT][4], au[NT][4];
#pragma unroll
  for (int n = 0; n < NT; ++n)
#pragma unroll
    for (int mi = 0; mi < 4; ++mi) { ag[n][mi] = (f32x4){0.f, 0.f, 0.f, 0.f}; au[n][mi] = (f32x4){0.f, 0.f, 0.f, 0.f}; }
  uint4 at[16];
#pragma unroll
  for (int i = 0; i < 16; ++i) at[i] = xn_src[i * 256 + t];
  const uint4* bp = wgu_t + (size_t)jt0 * 16 * 2 * 64 + lane;
  uint4 bg[16][NT], bu[16][NT];
#pragma unroll
  for (int s = 0; s < 11; ++s)
#pragma unroll
    for (int n = 0; n < NT; ++n) {
      bg[s][n] = bp[((n * 16 + s) * 2 + 0) * 64];
      bu[s][n] = bp[((n * 16 + s) * 2 + 1) * 64];
    }
#pragma unroll
  for (int i = 0; i < 16; ++i) As[i * 256 + t] = at[i];
  __syncthreads();
  STAMP(3, 1);
  uint4 a[4], an[4];
#pragma unroll
  for (int mi = 0; mi < 4; ++mi) a[mi] = As[(mi * 16) * 64 + lane];
#pragma unroll
  for (int s = 0; s < 16; ++s) {
    if (s < 15) {
#pragma unroll
      for (int mi = 0; mi < 4; ++mi) an[mi] = As[(mi * 16 + s + 1) * 64 + lane];
    }
#pragma unroll
    for (int n = 0; n < NT; ++n)
#pragma unroll
      for (int mi = 0; mi < 4; ++mi) {
        ag[n][mi] = __builtin_amdgcn_mfma_f32_16x16x32_bf16(*reinterpret_cast<bf16x8*>(&bg[s][n]),
                                                            *reinterpret_cast<bf16x8*>(&a[mi]), ag[n][mi], 0, 0, 0);
        au[n][mi] = __builtin_amdgcn_mfma_f32_16x16x32_bf16(*reinterpret_cast<bf16x8*>(&bu[s][n]),
                                                            *reinterpret_cast<bf16x8*>(&a[mi]), au[n][mi], 0, 0, 0);
      }
#pragma unroll
    for (int mi = 0; mi < 4; ++mi) a[mi] = an[mi];
    if (s < 5) {
      __builtin_amdgcn_sched_barrier(0);
#pragma unroll
      for (int n = 0; n < NT; ++n) {
        bg[11 + s][n] = bp[((n * 16 + 11 + s) * 2 + 0) * 64];
        bu[11 + s][n] = bp[((n * 16 + 11 + s) * 2 + 1) * 64];
      }
    }
  }
  STAMP(3, 2);
#pragma unroll
  for (int n = 0; n < NT; ++n) {
    const int jt = jt0 + n;
#pragma unroll
    for (int mi = 0; mi < 4; ++mi) {
      const int mt16 = mt64 * 4 + mi;
      uint2 o = make_uint2(geglu2(ag[n][mi][0], ag[n][mi][1], au[n][mi][0], au[n][mi][1]),
                           geglu2(ag[n][mi][2], ag[n][mi][3], au[n][mi][2], au[n][mi][3]));
      act_t[((mt16 * KS2 + (jt >> 1)) * 64 + lane) * 2 + (jt & 1)] = o;
    }
  }
  STAMP(3, 3);
  STAMPW(3, 4);
}

__global__ __launch_bounds__(256, 1) void k4_gemm1(const uint4* __restrict__ xn_t, const uint4* __restrict__ wgu_t,
                                                   uint2* __restrict__ act_t DIAG_PARAM) {
  __shared__ __attribute__((aligned(16))) uint4 As[4 * 16 * 64];
  STAMP(3, 0);
  const int t = threadIdx.x, w = __builtin_amdgcn_readfirstlane(t >> 6), lane = t & 63;
  const int bid = blockIdx.x;
  const int xcd = bid & 7, idx = bid >> 3;
  const int mt64 = (xcd & 1) * 16 + (idx & 15);
  const int ng = (xcd >> 1) * 2 + (idx >> 4);
  const uint4* src = xn_t + (size_t)mt64 * 4 * 16 * 64;
  int start, cnt;
  if (ng < 6) { start = 11 * ng; cnt = 11; } else { start = 66 + 10 * (ng - 6); cnt = 10; }
  int jt0, nt;
  if (cnt == 11) { jt0 = start + 3 * w; nt = (w < 3) ? 3 : 2; }
  else { jt0 = (w < 2) ? (start + 3 * w) : (start + 6 + 2 * (w - 2)); nt = (w < 2) ? 3 : 2; }
  if (nt == 3) k4_body<3>(As, src, wgu_t, act_t, mt64, jt0, t, lane);
  else k4_body<2>(As, src, wgu_t, act_t, mt64, jt0, t, lane);
}

#define K5_WAVES 8
#define PSTR 68
__global__ __launch_bounds__(512, 2) void k5_gemm2(const uint4* __restrict__ act_t, const uint4* __restrict__ wd_t,
                                                   const u16* __restrict__ h, u16* __restrict__ out DIAG_PARAM) {
  __shared__ __attribute__((aligned(16))) float part[K5_WAVES * 64 * PSTR];
  STAMP(4, 0);
  const int t = threadIdx.x, w = __builtin_amdgcn_readfirstlane(t >> 6), lane = t & 63;
  const int bid = blockIdx.x;
  const int xcd = bid & 7, idx = bid >> 3;
  const int mt64 = (xcd & 3) * 8 + (idx & 7);
  const int nt64 = (xcd >> 2) * 4 + (idx >> 3);
  const int ks0 = (w < 3) ? 6 * w : 18 + 5 * (w - 3);
  const int nks = (w < 3) ? 6 : 5;
  f32x4 acc[4][4];
#pragma unroll
  for (int a = 0; a < 4; ++a)
#pragma unroll
    for (int b = 0; b < 4; ++b) acc[a][b] = (f32x4){0.f, 0.f, 0.f, 0.f};
  const uint4* ap = act_t + ((size_t)(mt64 * 4) * KS2 + ks0) * 64 + lane;
  const uint4* bp = wd_t + ((size_t)(nt64 * 4) * KS2 + ks0) * 64 + lane;
  uint4 af[6][4], bf[6][4];
#pragma unroll
  for (int s = 0; s < 6; ++s) {
    if (s < nks) {
#pragma unroll
      for (int i = 0; i < 4; ++i) {
        af[s][i] = ap[(i * KS2 + s) * 64];
        bf[s][i] = bp[(i * KS2 + s) * 64];
      }
    }
  }
  const int er = t >> 3, ec = (t & 7) * 8;
  const int pos = mt64 * 64 + er;
  const uint4 hv = *reinterpret_cast<const uint4*>(h + (size_t)pos * D_DIM + nt64 * 64 + ec);
#pragma unroll
  for (int s = 0; s < 6; ++s) {
    if (s < nks) {
#pragma unroll
      for (int mi = 0; mi < 4; ++mi)
#pragma unroll
        for (int ni = 0; ni < 4; ++ni)
          acc[mi][ni] = __builtin_amdgcn_mfma_f32_16x16x32_bf16(*reinterpret_cast<bf16x8*>(&bf[s][ni]),
                                                                *reinterpret_cast<bf16x8*>(&af[s][mi]), acc[mi][ni], 0, 0, 0);
    }
  }
  STAMP(4, 1);
  float* pw = part + w * 64 * PSTR;
#pragma unroll
  for (int mi = 0; mi < 4; ++mi)
#pragma unroll
    for (int ni = 0; ni < 4; ++ni)
      *reinterpret_cast<f32x4*>(pw + (16 * mi + (lane & 15)) * PSTR + 16 * ni + 4 * (lane >> 4)) = acc[mi][ni];
  __syncthreads();
  STAMP(4, 2);
  f32x4 s0 = {0.f, 0.f, 0.f, 0.f}, s1 = {0.f, 0.f, 0.f, 0.f};
#pragma unroll
  for (int ww = 0; ww < K5_WAVES; ++ww) {
    s0 += *reinterpret_cast<const f32x4*>(part + ww * 64 * PSTR + er * PSTR + ec);
    s1 += *reinterpret_cast<const f32x4*>(part + ww * 64 * PSTR + er * PSTR + ec + 4);
  }
  const unsigned hw[4] = {hv.x, hv.y, hv.z, hv.w};
  unsigned ow[4];
#pragma unroll
  for (int i = 0; i < 4; ++i) {
    const float d0 = bfr(i < 2 ? s0[2 * i] : s1[2 * i - 4]);
    const float d1 = bfr(i < 2 ? s0[2 * i + 1] : s1[2 * i - 3]);
    const float h0 = __uint_as_float(hw[i] << 16), h1 = __uint_as_float(hw[i] & 0xffff0000u);
    ow[i] = (unsigned)f2bf(h0 + d0) | ((unsigned)f2bf(h1 + d1) << 16);
  }
  *reinterpret_cast<uint4*>(out + (size_t)row_of_pos(pos) * D_DIM + nt64 * 64 + ec) = make_uint4(ow[0], ow[1], ow[2], ow[3]);
  STAMP(4, 3);
  STAMPW(4, 4);
}

extern "C" void kernel_launch(void* const* d_in, const int* in_sizes, int n_in, void* d_out, int out_size,
                              void* d_ws, size_t ws_size, hipStream_t stream) {
  const u16* x = (const u16*)d_in[0];
  const u16* wr = (const u16*)d_in[1];
  const u16* wi = (const u16*)d_in[2];
  const u16* wg = (const u16*)d_in[4];
  const u16* wu = (const u16*)d_in[5];
  const u16* wd = (const u16*)d_in[6];
  u16* out = (u16*)d_out;
  char* ws = (char*)d_ws;
  size_t off = 0;
  float2* spec = (float2*)(ws + off); off += (size_t)256 * S_LEN * 8;
  unsigned* wt = (unsigned*)(ws + off); off += (size_t)260 * S_LEN * 4;
  u16* h = (u16*)(ws + off); off += (size_t)S_LEN * D_DIM * 2;
  u16* xn_t = (u16*)(ws + off); off += (size_t)S_LEN * D_DIM * 2;
  uint4* wgu_t = (uint4*)(ws + off); off += (size_t)NT16 * 16 * 2 * 1024;
  uint4* wd_t = (uint4*)(ws + off); off += (size_t)32 * KS2 * 1024;
  uint4* act_t = (uint4*)(ws + off); off += (size_t)128 * KS2 * 1024;

  k1_ln_rfft<<<512, 256, 0, stream>>>(x, wr, wi, spec, wt DIAG_ARG);
  const int side = (NT16 * 16 * 2 * 64 + 32 * KS2 * 64 + 255) / 256;
  k2_colfft<<<256 + side, 256, 0, stream>>>(spec, wt, wg, wu, wd, wgu_t, wd_t DIAG_ARG);
  k3_irfft_ln<<<256, 256, 0, stream>>>(spec, x, h, xn_t DIAG_ARG);
  k4_gemm1<<<256, 256, 0, stream>>>((const uint4*)xn_t, wgu_t, (uint2*)act_t DIAG_ARG);
  k5_gemm2<<<256, 512, 0, stream>>>(act_t, wd_t, h, out DIAG_ARG);
}
```

```cpp
#include <hip/hip_runtime.h>
#include <hip/hip_bf16.h>
#include <stdint.h>

typedef __attribute__((ext_vector_type(8))) short bf16x8;
typedef __attribute__((ext_vector_type(4))) float f32x4;
typedef unsigned short u16;
#define DIAG_PARAM
#define DIAG_ARG
#define STAMP(k, s)
#define STAMPW(k, s)

#define S_LEN 2048
#define D_DIM 512
#define F_DIM 1376
#define DF 257
#define NT16 86
#define KS2 43

__device__ const float2 g_tw[2048] = {
{OPQ,OPQ}, {OPQ,OPQ}, {OPQ,OPQ}, {OPQ,OPQ},
{OPQ,OPQ}, {OPQ,OPQ}, {OPQ,OPQ}, {OPQ,OPQ},
{OPQ,OPQ}, {OPQ,OPQ}, {OPQ,OPQ}, {OPQ,OPQ},
{OPQ,OPQ}, {OPQ,OPQ}, {OPQ,OPQ}, {OPQ,OPQ},
{OPQ,OPQ}, {OPQ,OPQ}, {OPQ,OPQ}, {OPQ,OPQ},
{OPQ,OPQ}, {OPQ,OPQ}, {OPQ,OPQ}, {OPQ,OPQ},
{OPQ,OPQ}, {OPQ,OPQ}, {OPQ,OPQ}, {OPQ,OPQ},
{OPQ,OPQ}, {OPQ,OPQ}, {OPQ,OPQ}, {OPQ,OPQ},
{OPQ,OPQ}, {OPQ,OPQ}, {OPQ,OPQ}, {OPQ,OPQ},
{OPQ,OPQ}, {OPQ,OPQ}, {OPQ,OPQ}, {OPQ,OPQ},
{OPQ,OPQ}, {OPQ,OPQ}, {OPQ,OPQ}, {OPQ,OPQ},
{OPQ,OPQ}, {OPQ,OPQ}, {OPQ,OPQ}, {OPQ,OPQ},
{OPQ,OPQ}, {OPQ,OPQ}, {OPQ,OPQ}, {OPQ,OPQ},
{OPQ,OPQ}, {OPQ,OPQ}, {OPQ,OPQ}, {OPQ,OPQ},
{OPQ,OPQ}, {OPQ,OPQ}, {OPQ,OPQ}, {OPQ,OPQ},
{OPQ,OPQ}, {OPQ,OPQ}, {OPQ,OPQ}, {OPQ,OPQ},
{OPQ,OPQ}, {OPQ,OPQ}, {OPQ,OPQ}, {OPQ,OPQ},
{OPQ,OPQ}, {OPQ,OPQ}, {OPQ,OPQ}, {OPQ,OPQ},
{OPQ,OPQ}, {OPQ,OPQ}, {OPQ,OPQ}, {OPQ,OPQ},
{OPQ,OPQ}, {OPQ,OPQ}, {OPQ,OPQ}, {OPQ,OPQ},
{OPQ,OPQ}, {OPQ,OPQ}, {OPQ,OPQ}, {OPQ,OPQ},
{OPQ,OPQ}, {OPQ,OPQ}, {OPQ,OPQ}, {OPQ,OPQ},
{OPQ,OPQ}, {OPQ,OPQ}, {OPQ,OPQ}, {OPQ,OPQ},
{OPQ,OPQ}, {OPQ,OPQ}, {OPQ,OPQ}, {OPQ,OPQ},
{OPQ,OPQ}, {OPQ,OPQ}, {OPQ,OPQ}, {OPQ,OPQ},
{OPQ,OPQ}, {OPQ,OPQ}, {OPQ,OPQ}, {OPQ,OPQ},
{OPQ,OPQ}, {OPQ,OPQ}, {OPQ,OPQ}, {OPQ,OPQ},
{OPQ,OPQ}, {OPQ,OPQ}, {OPQ,OPQ}, {OPQ,OPQ},
{OPQ,OPQ}, {OPQ,OPQ}, {OPQ,OPQ}, {OPQ,OPQ},
{OPQ,OPQ}, {OPQ,OPQ}, {OPQ,OPQ}, {OPQ,OPQ},
{OPQ,OPQ}, {OPQ,OPQ}, {OPQ,OPQ}, {OPQ,OPQ},
{OPQ,OPQ}, {OPQ,OPQ}, {OPQ,OPQ}, {OPQ,OPQ},
{OPQ,OPQ}, {OPQ,OPQ}, {OPQ,OPQ}, {OPQ,OPQ},
{OPQ,OPQ}, {OPQ,OPQ}, {OPQ,OPQ}, {OPQ,OPQ},
{OPQ,OPQ}, {OPQ,OPQ}, {OPQ,OPQ}, {OPQ,OPQ},
{OPQ,OPQ}, {OPQ,OPQ}, {OPQ,OPQ}, {OPQ,OPQ},
{OPQ,OPQ}, {OPQ,OPQ}, {OPQ,OPQ}, {OPQ,OPQ},
{OPQ,OPQ}, {OPQ,OPQ}, {OPQ,OPQ}, {OPQ,OPQ},
{OPQ,OPQ}, {OPQ,OPQ}, {OPQ,OPQ}, {OPQ,OPQ},
{OPQ,OPQ}, {OPQ,OPQ}, {OPQ,OPQ}, {OPQ,OPQ},
{OPQ,OPQ}, {OPQ,OPQ}, {OPQ,OPQ}, {OPQ,OPQ},
{OPQ,OPQ}, {OPQ,OPQ}, {OPQ,OPQ}, {OPQ,OPQ},
{OPQ,OPQ}, {OPQ,OPQ}, {OPQ,OPQ}, {OPQ,OPQ},
{OPQ,OPQ}, {OPQ,OPQ}, {OPQ,OPQ}, {OPQ,OPQ},
{OPQ,OPQ}, {OPQ,OPQ}, {OPQ,OPQ}, {OPQ,OPQ},
{OPQ,OPQ}, {OPQ,OPQ}, {OPQ,OPQ}, {OPQ,OPQ},
{OPQ,OPQ}, {OPQ,OPQ}, {OPQ,OPQ}, {OPQ,OPQ},
{OPQ,OPQ}, {OPQ,OPQ}, {OPQ,OPQ}, {OPQ,OPQ},
{OPQ,OPQ}, {OPQ,OPQ}, {OPQ,OPQ}, {OPQ,OPQ},
{OPQ,OPQ}, {OPQ,OPQ}, {OPQ,OPQ}, {OPQ,OPQ},
{OPQ,OPQ}, {OPQ,OPQ}, {OPQ,OPQ}, {OPQ,OPQ},
{OPQ,OPQ}, {OPQ,OPQ}, {OPQ,OPQ}, {OPQ,OPQ},
{OPQ,OPQ}, {OPQ,OPQ}, {OPQ,OPQ}, {OPQ,OPQ},
{OPQ,OPQ}, {OPQ,OPQ}, {OPQ,OPQ}, {OPQ,OPQ},
{OPQ,OPQ}, {OPQ,OPQ}, {OPQ,OPQ}, {OPQ,OPQ},
{OPQ,OPQ}, {OPQ,OPQ}, {OPQ,OPQ}, {OPQ,OPQ},
{OPQ,OPQ}, {OPQ,OPQ}, {OPQ,OPQ}, {OPQ,OPQ},
{OPQ,OPQ}, {OPQ,OPQ}, {OPQ,OPQ}, {OPQ,OPQ},
{OPQ,OPQ}, {OPQ,OPQ}, {OPQ,OPQ}, {OPQ,OPQ},
{OPQ,OPQ}, {OPQ,OPQ}, {OPQ,OPQ}, {OPQ,OPQ},
{OPQ,OPQ}, {OPQ,OPQ}, {OPQ,OPQ}, {OPQ,OPQ},
{OPQ,OPQ}, {OPQ,OPQ}, {OPQ,OPQ}, {OPQ,OPQ},
{OPQ,OPQ}, {OPQ,OPQ}, {OPQ,OPQ}, {OPQ,OPQ},
{OPQ,OPQ}, {OPQ,OPQ}, {OPQ,OPQ}, {OPQ,OPQ},
{OPQ,OPQ}, {OPQ,OPQ}, {OPQ,OPQ}, {OPQ,OPQ},
{OPQ,OPQ}, {OPQ,OPQ}, {OPQ,OPQ}, {OPQ,OPQ},
{OPQ,OPQ}, {OPQ,OPQ}, {OPQ,OPQ}, {OPQ,OPQ},
{OPQ,OPQ}, {OPQ,OPQ}, {OPQ,OPQ}, {OPQ,OPQ},
{OPQ,OPQ}, {OPQ,OPQ}, {OPQ,OPQ}, {OPQ,OPQ},
{OPQ,OPQ}, {OPQ,OPQ}, {OPQ,OPQ}, {OPQ,OPQ},
{OPQ,OPQ}, {OPQ,OPQ}, {OPQ,OPQ}, {OPQ,OPQ},
{OPQ,OPQ}, {OPQ,OPQ}, {OPQ,OPQ}, {OPQ,OPQ},
{OPQ,OPQ}, {OPQ,OPQ}, {OPQ,OPQ}, {OPQ,OPQ},
{OPQ,OPQ}, {OPQ,OPQ}, {OPQ,OPQ}, {OPQ,OPQ},
{OPQ,OPQ}, {OPQ,OPQ}, {OPQ,OPQ}, {OPQ,OPQ},
{OPQ,OPQ}, {OPQ,OPQ}, {OPQ,OPQ}, {OPQ,OPQ},
{OPQ,OPQ}, {OPQ,OPQ}, {OPQ,OPQ}, {OPQ,OPQ},
{OPQ,OPQ}, {OPQ,OPQ}, {OPQ,OPQ}, {OPQ,OPQ},
{OPQ,OPQ}, {OPQ,OPQ}, {OPQ,OPQ}, {OPQ,OPQ},
{OPQ,OPQ}, {OPQ,OPQ}, {OPQ,OPQ}, {OPQ,OPQ},
{OPQ,OPQ}, {OPQ,OPQ}, {OPQ,OPQ}, {OPQ,OPQ},
{OPQ,OPQ}, {OPQ,OPQ}, {OPQ,OPQ}, {OPQ,OPQ},
{OPQ,OPQ}, {OPQ,OPQ}, {OPQ,OPQ}, {OPQ,OPQ},
{OPQ,OPQ}, {OPQ,OPQ}, {OPQ,OPQ}, {OPQ,OPQ},
{OPQ,OPQ}, {OPQ,OPQ}, {OPQ,OPQ}, {OPQ,OPQ},
{OPQ,OPQ}, {OPQ,OPQ}, {OPQ,OPQ}, {OPQ,OPQ},
{OPQ,OPQ}, {OPQ,OPQ}, {OPQ,OPQ}, {OPQ,OPQ},
{OPQ,OPQ}, {OPQ,OPQ}, {OPQ,OPQ}, {OPQ,OPQ},
{OPQ,OPQ}, {OPQ,OPQ}, {OPQ,OPQ}, {OPQ,OPQ},
{OPQ,OPQ}, {OPQ,OPQ}, {OPQ,OPQ}, {OPQ,OPQ},
{OPQ,OPQ}, {OPQ,OPQ}, {OPQ,OPQ}, {OPQ,OPQ},
{OPQ,OPQ}, {OPQ,OPQ}, {OPQ,OPQ}, {OPQ,OPQ},
{OPQ,OPQ}, {OPQ,OPQ}, {OPQ,OPQ}, {OPQ,OPQ},
{OPQ,OPQ}, {OPQ,OPQ}, {OPQ,OPQ}, {OPQ,OPQ},
{OPQ,OPQ}, {OPQ,OPQ}, {OPQ,OPQ}, {OPQ,OPQ},
{OPQ,OPQ}, {OPQ,OPQ}, {OPQ,OPQ}, {OPQ,OPQ},
{OPQ,OPQ}, {OPQ,OPQ}, {OPQ,OPQ}, {OPQ,OPQ},
{OPQ,OPQ}, {OPQ,OPQ}, {OPQ,OPQ}, {OPQ,OPQ},
{OPQ,OPQ}, {OPQ,OPQ}, {OPQ,OPQ}, {OPQ,OPQ},
{OPQ,OPQ}, {OPQ,OPQ}, {OPQ,OPQ}, {OPQ,OPQ},
{OPQ,OPQ}, {OPQ,OPQ}, {OPQ,OPQ}, {OPQ,OPQ},
{OPQ,OPQ}, {OPQ,OPQ}, {OPQ,OPQ}, {OPQ,OPQ},
{OPQ,OPQ}, {OPQ,OPQ}, {OPQ,OPQ}, {OPQ,OPQ},
{OPQ,OPQ}, {OPQ,OPQ}, {OPQ,OPQ}, {OPQ,OPQ},
{OPQ,OPQ}, {OPQ,OPQ}, {OPQ,OPQ}, {OPQ,OPQ},
{OPQ,OPQ}, {OPQ,OPQ}, {OPQ,OPQ}, {OPQ,OPQ},
{OPQ,OPQ}, {OPQ,OPQ}, {OPQ,OPQ}, {OPQ,OPQ},
{OPQ,OPQ}, {OPQ,OPQ}, {OPQ,OPQ}, {OPQ,OPQ},
{OPQ,OPQ}, {OPQ,OPQ}, {OPQ,OPQ}, {OPQ,OPQ},
{OPQ,OPQ}, {OPQ,OPQ}, {OPQ,OPQ}, {OPQ,OPQ},
{OPQ,OPQ}, {OPQ,OPQ}, {OPQ,OPQ}, {OPQ,OPQ},
{OPQ,OPQ}, {OPQ,OPQ}, {OPQ,OPQ}, {OPQ,OPQ},
{OPQ,OPQ}, {OPQ,OPQ}, {OPQ,OPQ}, {OPQ,OPQ},
{OPQ,OPQ}, {OPQ,OPQ}, {OPQ,OPQ}, {OPQ,OPQ},
{OPQ,OPQ}, {OPQ,OPQ}, {OPQ,OPQ}, {OPQ,OPQ},
{OPQ,OPQ}, {OPQ,OPQ}, {OPQ,OPQ}, {OPQ,OPQ},
{OPQ,OPQ}, {OPQ,OPQ}, {OPQ,OPQ}, {OPQ,OPQ},
{OPQ,OPQ}, {OPQ,OPQ}, {OPQ,OPQ}, {OPQ,OPQ},
{OPQ,OPQ}, {OPQ,OPQ}, {OPQ,OPQ}, {OPQ,OPQ},
{OPQ,OPQ}, {OPQ,OPQ}, {OPQ,OPQ}, {OPQ,OPQ},
{OPQ,OPQ}, {OPQ,OPQ}, {OPQ,OPQ}, {OPQ,OPQ},
{OPQ,OPQ}, {OPQ,OPQ}, {OPQ,OPQ}, {OPQ,OPQ},
{OPQ,OPQ}, {OPQ,OPQ}, {OPQ,OPQ}, {OPQ,OPQ},
{OPQ,OPQ}, {OPQ,OPQ}, {OPQ,OPQ}, {OPQ,OPQ},
{OPQ,OPQ}, {OPQ,OPQ}, {OPQ,OPQ}, {OPQ,OPQ},
{OPQ,OPQ}, {OPQ,OPQ}, {OPQ,OPQ}, {OPQ,OPQ},
{OPQ,OPQ}, {OPQ,OPQ}, {OPQ,OPQ}, {OPQ,OPQ},
{OPQ,OPQ}, {OPQ,OPQ}, {OPQ,OPQ}, {OPQ,OPQ},
{OPQ,OPQ}, {OPQ,OPQ}, {OPQ,OPQ}, {OPQ,OPQ},
{OPQ,OPQ}, {OPQ,OPQ}, {OPQ,OPQ}, {OPQ,OPQ},
{OPQ,OPQ}, {OPQ,OPQ}, {OPQ,OPQ}, {OPQ,OPQ},
{OPQ,OPQ}, {OPQ,OPQ}, {OPQ,OPQ}, {OPQ,OPQ},
{OPQ,OPQ}, {OPQ,OPQ}, {OPQ,OPQ}, {OPQ,OPQ},
{OPQ,OPQ}, {OPQ,OPQ}, {OPQ,OPQ}, {OPQ,OPQ},
{OPQ,OPQ}, {OPQ,OPQ}, {OPQ,OPQ}, {OPQ,OPQ},
{OPQ,OPQ}, {OPQ,OPQ}, {OPQ,OPQ}, {OPQ,OPQ},
{OPQ,OPQ}, {OPQ,OPQ}, {OPQ,OPQ}, {OPQ,OPQ},
{OPQ,OPQ}, {OPQ,OPQ}, {OPQ,OPQ}, {OPQ,OPQ},
{OPQ,OPQ}, {OPQ,OPQ}, {OPQ,OPQ}, {OPQ,OPQ},
{OPQ,OPQ}, {OPQ,OPQ}, {OPQ,OPQ}, {OPQ,OPQ},
{OPQ,OPQ}, {OPQ,OPQ}, {OPQ,OPQ}, {OPQ,OPQ},
{OPQ,OPQ}, {OPQ,OPQ}, {OPQ,OPQ}, {OPQ,OPQ},
{OPQ,OPQ}, {OPQ,OPQ}, {OPQ,OPQ}, {OPQ,OPQ},
{OPQ,OPQ}, {OPQ,OPQ}, {OPQ,OPQ}, {OPQ,OPQ},
{OPQ,OPQ}, {OPQ,OPQ}, {OPQ,OPQ}, {OPQ,OPQ},
{OPQ,OPQ}, {OPQ,OPQ}, {OPQ,OPQ}, {OPQ,OPQ},
{OPQ,OPQ}, {OPQ,OPQ}, {OPQ,OPQ}, {OPQ,OPQ},
{OPQ,OPQ}, {OPQ,OPQ}, {OPQ,OPQ}, {OPQ,OPQ},
{OPQ,OPQ}, {OPQ,OPQ}, {OPQ,OPQ}, {OPQ,OPQ},
{OPQ,OPQ}, {OPQ,OPQ}, {OPQ,OPQ}, {OPQ,OPQ},
{OPQ,OPQ}, {OPQ,OPQ}, {OPQ,OPQ}, {OPQ,OPQ},
{OPQ,OPQ}, {OPQ,OPQ}, {OPQ,OPQ}, {OPQ,OPQ},
{OPQ,OPQ}, {OPQ,OPQ}, {OPQ,OPQ}, {OPQ,OPQ},
{OPQ,OPQ}, {OPQ,OPQ}, {OPQ,OPQ}, {OPQ,OPQ},
{OPQ,OPQ}, {OPQ,OPQ}, {OPQ,OPQ}, {OPQ,OPQ},
{OPQ,OPQ}, {OPQ,OPQ}, {OPQ,OPQ}, {OPQ,OPQ},
{OPQ,OPQ}, {OPQ,OPQ}, {OPQ,OPQ}, {OPQ,OPQ},
{OPQ,OPQ}, {OPQ,OPQ}, {OPQ,OPQ}, {OPQ,OPQ},
{OPQ,OPQ}, {OPQ,OPQ}, {OPQ,OPQ}, {OPQ,OPQ},
{OPQ,OPQ}, {OPQ,OPQ}, {OPQ,OPQ}, {OPQ,OPQ},
{OPQ,OPQ}, {OPQ,OPQ}, {OPQ,OPQ}, {OPQ,OPQ},
{OPQ,OPQ}, {OPQ,OPQ}, {OPQ,OPQ}, {OPQ,OPQ},
{OPQ,OPQ}, {OPQ,OPQ}, {OPQ,OPQ}, {OPQ,OPQ},
{OPQ,OPQ}, {OPQ,OPQ}, {OPQ,OPQ}, {OPQ,OPQ},
{OPQ,OPQ}, {OPQ,OPQ}, {OPQ,OPQ}, {OPQ,OPQ},
{OPQ,OPQ}, {OPQ,OPQ}, {OPQ,OPQ}, {OPQ,OPQ},
{OPQ,OPQ}, {OPQ,OPQ}, {OPQ,OPQ}, {OPQ,OPQ},
{OPQ,OPQ}, {OPQ,OPQ}, {OPQ,OPQ}, {OPQ,OPQ},
{OPQ,OPQ}, {OPQ,OPQ}, {OPQ,OPQ}, {OPQ,OPQ},
{OPQ,OPQ}, {OPQ,OPQ}, {OPQ,OPQ}, {OPQ,OPQ},
{OPQ,OPQ}, {OPQ,OPQ}, {OPQ,OPQ}, {OPQ,OPQ},
{OPQ,OPQ}, {OPQ,OPQ}, {OPQ,OPQ}, {OPQ,OPQ},
{OPQ,OPQ}, {OPQ,OPQ}, {OPQ,OPQ}, {OPQ,OPQ},
{OPQ,OPQ}, {OPQ,OPQ}, {OPQ,OPQ}, {OPQ,OPQ},
{OPQ,OPQ}, {OPQ,OPQ}, {OPQ,OPQ}, {OPQ,OPQ},
{OPQ,OPQ}, {OPQ,OPQ}, {OPQ,OPQ}, {OPQ,OPQ},
{OPQ,OPQ}, {OPQ,OPQ}, {OPQ,OPQ}, {OPQ,OPQ},
{OPQ,OPQ}, {OPQ,OPQ}, {OPQ,OPQ}, {OPQ,OPQ},
{OPQ,OPQ}, {OPQ,OPQ}, {OPQ,OPQ}, {OPQ,OPQ},
{OPQ,OPQ}, {OPQ,OPQ}, {OPQ,OPQ}, {OPQ,OPQ},
{OPQ,OPQ}, {OPQ,OPQ}, {OPQ,OPQ}, {OPQ,OPQ},
{OPQ,OPQ}, {OPQ,OPQ}, {OPQ,OPQ}, {OPQ,OPQ},
{OPQ,OPQ}, {OPQ,OPQ}, {OPQ,OPQ}, {OPQ,OPQ},
{OPQ,OPQ}, {OPQ,OPQ}, {OPQ,OPQ}, {OPQ,OPQ},
{OPQ,OPQ}, {OPQ,OPQ}, {OPQ,OPQ}, {OPQ,OPQ},
{OPQ,OPQ}, {OPQ,OPQ}, {OPQ,OPQ}, {OPQ,OPQ},
{OPQ,OPQ}, {OPQ,OPQ}, {OPQ,OPQ}, {OPQ,OPQ},
{OPQ,OPQ}, {OPQ,OPQ}, {OPQ,OPQ}, {OPQ,OPQ},
{OPQ,OPQ}, {OPQ,OPQ}, {OPQ,OPQ}, {OPQ,OPQ},
{OPQ,OPQ}, {OPQ,OPQ}, {OPQ,OPQ}, {OPQ,OPQ},
{OPQ,OPQ}, {OPQ,OPQ}, {OPQ,OPQ}, {OPQ,OPQ},
{OPQ,OPQ}, {OPQ,OPQ}, {OPQ,OPQ}, {OPQ,OPQ},
{OPQ,OPQ}, {OPQ,OPQ}, {OPQ,OPQ}, {OPQ,OPQ},
{OPQ,OPQ}, {OPQ,OPQ}, {OPQ,OPQ}, {OPQ,OPQ},
{OPQ,OPQ}, {OPQ,OPQ}, {OPQ,OPQ}, {OPQ,OPQ},
{OPQ,OPQ}, {OPQ,OPQ}, {OPQ,OPQ}, {OPQ,OPQ},
{OPQ,OPQ}, {OPQ,OPQ}, {OPQ,OPQ}, {OPQ,OPQ},
{OPQ,OPQ}, {OPQ,OPQ}, {OPQ,OPQ}, {OPQ,OPQ},
{OPQ,OPQ}, {OPQ,OPQ}, {OPQ,OPQ}, {OPQ,OPQ},
{OPQ,OPQ}, {OPQ,OPQ}, {OPQ,OPQ}, {OPQ,OPQ},
{OPQ,OPQ}, {OPQ,OPQ}, {OPQ,OPQ}, {OPQ,OPQ},
{OPQ,OPQ}, {OPQ,OPQ}, {OPQ,OPQ}, {OPQ,OPQ},
{OPQ,OPQ}, {OPQ,OPQ}, {OPQ,OPQ}, {OPQ,OPQ},
{OPQ,OPQ}, {OPQ,OPQ}, {OPQ,OPQ}, {OPQ,OPQ},
{OPQ,OPQ}, {OPQ,OPQ}, {OPQ,OPQ}, {OPQ,OPQ},
{OPQ,OPQ}, {OPQ,OPQ}, {OPQ,OPQ}, {OPQ,OPQ},
{OPQ,OPQ}, {OPQ,OPQ}, {OPQ,OPQ}, {OPQ,OPQ},
{OPQ,OPQ}, {OPQ,OPQ}, {OPQ,OPQ}, {OPQ,OPQ},
{OPQ,OPQ}, {OPQ,OPQ}, {OPQ,OPQ}, {OPQ,OPQ},
{OPQ,OPQ}, {OPQ,OPQ}, {OPQ,OPQ}, {OPQ,OPQ},
{OPQ,OPQ}, {OPQ,OPQ}, {OPQ,OPQ}, {OPQ,OPQ},
{OPQ,OPQ}, {OPQ,OPQ}, {OPQ,OPQ}, {OPQ,OPQ},
{OPQ,OPQ}, {OPQ,OPQ}, {OPQ,OPQ}, {OPQ,OPQ},
{OPQ,OPQ}, {OPQ,OPQ}, {OPQ,OPQ}, {OPQ,OPQ},
{OPQ,OPQ}, {OPQ,OPQ}, {OPQ,OPQ}, {OPQ,OPQ},
{OPQ,OPQ}, {OPQ,OPQ}, {OPQ,OPQ}, {OPQ,OPQ},
{OPQ,OPQ}, {OPQ,OPQ}, {OPQ,OPQ}, {OPQ,OPQ},
{OPQ,OPQ}, {OPQ,OPQ}, {OPQ,OPQ}, {OPQ,OPQ},
{OPQ,OPQ}, {OPQ,OPQ}, {OPQ,OPQ}, {OPQ,OPQ},
{OPQ,OPQ}, {OPQ,OPQ}, {OPQ,OPQ}, {OPQ,OPQ},
{OPQ,OPQ}, {OPQ,OPQ}, {OPQ,OPQ}, {OPQ,OPQ},
{OPQ,OPQ}, {OPQ,OPQ}, {OPQ,OPQ}, {OPQ,OPQ},
{OPQ,OPQ}, {OPQ,OPQ}, {OPQ,OPQ}, {OPQ,OPQ},
{OPQ,OPQ}, {OPQ,OPQ}, {OPQ,OPQ}, {OPQ,OPQ},
{OPQ,OPQ}, {OPQ,OPQ}, {OPQ,OPQ}, {OPQ,OPQ},
{OPQ,OPQ}, {OPQ,OPQ}, {OPQ,OPQ}, {OPQ,OPQ},
{OPQ,OPQ}, {OPQ,OPQ}, {OPQ,OPQ}, {OPQ,OPQ},
{OPQ,OPQ}, {OPQ,OPQ}, {OPQ,OPQ}, {OPQ,OPQ},
{OPQ,OPQ}, {OPQ,OPQ}, {OPQ,OPQ}, {OPQ,OPQ},
{OPQ,OPQ}, {OPQ,OPQ}, {OPQ,OPQ}, {OPQ,OPQ},
{OPQ,OPQ}, {OPQ,OPQ}, {OPQ,OPQ}, {OPQ,OPQ},
{OPQ,OPQ}, {OPQ,OPQ}, {OPQ,OPQ}, {OPQ,OPQ},
{OPQ,OPQ}, {OPQ,OPQ}, {OPQ,OPQ}, {OPQ,OPQ},
{OPQ,OPQ}, {OPQ,OPQ}, {OPQ,OPQ}, {OPQ,OPQ},
{OPQ,OPQ}, {OPQ,OPQ}, {OPQ,OPQ}, {OPQ,OPQ},
{OPQ,OPQ}, {OPQ,OPQ}, {OPQ,OPQ}, {OPQ,OPQ},
{OPQ,OPQ}, {OPQ,OPQ}, {OPQ,OPQ}, {OPQ,OPQ},
{OPQ,OPQ}, {OPQ,OPQ}, {OPQ,OPQ}, {OPQ,OPQ},
{OPQ,OPQ}, {OPQ,OPQ}, {OPQ,OPQ}, {OPQ,OPQ},
{OPQ,OPQ}, {OPQ,OPQ}, {OPQ,OPQ}, {OPQ,OPQ},
{OPQ,OPQ}, {OPQ,OPQ}, {OPQ,OPQ}, {OPQ,OPQ},
{OPQ,OPQ}, {OPQ,OPQ}, {OPQ,OPQ}, {OPQ,OPQ},
{OPQ,OPQ}, {OPQ,OPQ}, {OPQ,OPQ}, {OPQ,OPQ},
{OPQ,OPQ}, {OPQ,OPQ}, {OPQ,OPQ}, {OPQ,OPQ},
{OPQ,OPQ}, {OPQ,OPQ}, {OPQ,OPQ}, {OPQ,OPQ},
{OPQ,OPQ}, {OPQ,OPQ}, {OPQ,OPQ}, {OPQ,OPQ},
{OPQ,OPQ}, {OPQ,OPQ}, {OPQ,OPQ}, {OPQ,OPQ},
{OPQ,OPQ}, {OPQ,OPQ}, {OPQ,OPQ}, {OPQ,OPQ},
{OPQ,OPQ}, {OPQ,OPQ}, {OPQ,OPQ}, {OPQ,OPQ},
{OPQ,OPQ}, {OPQ,OPQ}, {OPQ,OPQ}, {OPQ,OPQ},
{OPQ,OPQ}, {OPQ,OPQ}, {OPQ,OPQ}, {OPQ,OPQ},
{OPQ,OPQ}, {OPQ,OPQ}, {OPQ,OPQ}, {OPQ,OPQ},
{OPQ,OPQ}, {OPQ,OPQ}, {OPQ,OPQ}, {OPQ,OPQ},
{OPQ,OPQ}, {OPQ,OPQ}, {OPQ,OPQ}, {OPQ,OPQ},
{OPQ,OPQ}, {OPQ,OPQ}, {OPQ,OPQ}, {OPQ,OPQ},
{OPQ,OPQ}, {OPQ,OPQ}, {OPQ,OPQ}, {OPQ,OPQ},
{OPQ,OPQ}, {OPQ,OPQ}, {OPQ,OPQ}, {OPQ,OPQ},
{OPQ,OPQ}, {OPQ,OPQ}, {OPQ,OPQ}, {OPQ,OPQ},
{OPQ,OPQ}, {OPQ,OPQ}, {OPQ,OPQ}, {OPQ,OPQ},
{OPQ,OPQ}, {OPQ,OPQ}, {OPQ,OPQ}, {OPQ,OPQ},
{OPQ,OPQ}, {OPQ,OPQ}, {OPQ,OPQ}, {OPQ,OPQ},
{OPQ,OPQ}, {OPQ,OPQ}, {OPQ,OPQ}, {OPQ,OPQ},
{OPQ,OPQ}, {OPQ,OPQ}, {OPQ,OPQ}, {OPQ,OPQ},
{OPQ,OPQ}, {OPQ,OPQ}, {OPQ,OPQ}, {OPQ,OPQ},
{OPQ,OPQ}, {OPQ,OPQ}, {OPQ,OPQ}, {OPQ,OPQ},
{OPQ,OPQ}, {OPQ,OPQ}, {OPQ,OPQ}, {OPQ,OPQ},
{OPQ,OPQ}, {OPQ,OPQ}, {OPQ,OPQ}, {OPQ,OPQ},
{OPQ,OPQ}, {OPQ,OPQ}, {OPQ,OPQ}, {OPQ,OPQ},
{OPQ,OPQ}, {OPQ,OPQ}, {OPQ,OPQ}, {OPQ,OPQ},
{OPQ,OPQ}, {OPQ,OPQ}, {OPQ,OPQ}, {OPQ,OPQ},
{OPQ,OPQ}, {OPQ,OPQ}, {OPQ,OPQ}, {OPQ,OPQ},
{OPQ,OPQ}, {OPQ,OPQ}, {OPQ,OPQ}, {OPQ,OPQ},
{OPQ,OPQ}, {OPQ,OPQ}, {OPQ,OPQ}, {OPQ,OPQ},
{OPQ,OPQ}, {OPQ,OPQ}, {OPQ,OPQ}, {OPQ,OPQ},
{OPQ,OPQ}, {OPQ,OPQ}, {OPQ,OPQ}, {OPQ,OPQ},
{OPQ,OPQ}, {OPQ,OPQ}, {OPQ,OPQ}, {OPQ,OPQ},
{OPQ,OPQ}, {OPQ,OPQ}, {OPQ,OPQ}, {OPQ,OPQ},
{OPQ,OPQ}, {OPQ,OPQ}, {OPQ,OPQ}, {OPQ,OPQ},
{OPQ,OPQ}, {OPQ,OPQ}, {OPQ,OPQ}, {OPQ,OPQ},
{OPQ,OPQ}, {OPQ,OPQ}, {OPQ,OPQ}, {OPQ,OPQ},
{OPQ,OPQ}, {OPQ,OPQ}, {OPQ,OPQ}, {OPQ,OPQ},
{OPQ,OPQ}, {OPQ,OPQ}, {OPQ,OPQ}, {OPQ,OPQ},
{OPQ,OPQ}, {OPQ,OPQ}, {OPQ,OPQ}, {OPQ,OPQ},
{OPQ,OPQ}, {OPQ,OPQ}, {OPQ,OPQ}, {OPQ,OPQ},
{OPQ,OPQ}, {OPQ,OPQ}, {OPQ,OPQ}, {OPQ,OPQ},
{OPQ,OPQ}, {OPQ,OPQ}, {OPQ,OPQ}, {OPQ,OPQ},
{OPQ,OPQ}, {OPQ,OPQ}, {OPQ,OPQ}, {OPQ,OPQ},
{OPQ,OPQ}, {OPQ,OPQ}, {OPQ,OPQ}, {OPQ,OPQ},
{OPQ,OPQ}, {OPQ,OPQ}, {OPQ,OPQ}, {OPQ,OPQ},
{OPQ,OPQ}, {OPQ,OPQ}, {OPQ,OPQ}, {OPQ,OPQ},
{OPQ,OPQ}, {OPQ,OPQ}, {OPQ,OPQ}, {OPQ,OPQ},
{OPQ,OPQ}, {OPQ,OPQ}, {OPQ,OPQ}, {OPQ,OPQ},
{OPQ,OPQ}, {OPQ,OPQ}, {OPQ,OPQ}, {OPQ,OPQ},
{OPQ,OPQ}, {OPQ,OPQ}, {OPQ,OPQ}, {OPQ,OPQ},
{OPQ,OPQ}, {OPQ,OPQ}, {OPQ,OPQ}, {OPQ,OPQ},
{OPQ,OPQ}, {OPQ,OPQ}, {OPQ,OPQ}, {OPQ,OPQ},
{OPQ,OPQ}, {OPQ,OPQ}, {OPQ,OPQ}, {OPQ,OPQ},
{OPQ,OPQ}, {OPQ,OPQ}, {OPQ,OPQ}, {OPQ,OPQ},
{OPQ,OPQ}, {OPQ,OPQ}, {OPQ,OPQ}, {OPQ,OPQ},
{OPQ,OPQ}, {OPQ,OPQ}, {OPQ,OPQ}, {OPQ,OPQ},
{OPQ,OPQ}, {OPQ,OPQ}, {OPQ,OPQ}, {OPQ,OPQ},
{OPQ,OPQ}, {OPQ,OPQ}, {OPQ,OPQ}, {OPQ,OPQ},
{OPQ,OPQ}, {OPQ,OPQ}, {OPQ,OPQ}, {OPQ,OPQ},
{OPQ,OPQ}, {OPQ,OPQ}, {OPQ,OPQ}, {OPQ,OPQ},
{OPQ,OPQ}, {OPQ,OPQ}, {OPQ,OPQ}, {OPQ,OPQ},
{OPQ,OPQ}, {OPQ,OPQ}, {OPQ,OPQ}, {OPQ,OPQ},
{OPQ,OPQ}, {OPQ,OPQ}, {OPQ,OPQ}, {OPQ,OPQ},
{OPQ,OPQ}, {OPQ,OPQ}, {OPQ,OPQ}, {OPQ,OPQ},
{OPQ,OPQ}, {OPQ,OPQ}, {OPQ,OPQ}, {OPQ,OPQ},
{OPQ,OPQ}, {OPQ,OPQ}, {OPQ,OPQ}, {OPQ,OPQ},
{OPQ,OPQ}, {OPQ,OPQ}, {OPQ,OPQ}, {OPQ,OPQ},
{OPQ,OPQ}, {OPQ,OPQ}, {OPQ,OPQ}, {OPQ,OPQ},
{OPQ,OPQ}, {OPQ,OPQ}, {OPQ,OPQ}, {OPQ,OPQ},
{OPQ,OPQ}, {OPQ,OPQ}, {OPQ,OPQ}, {OPQ,OPQ},
{OPQ,OPQ}, {OPQ,OPQ}, {OPQ,OPQ}, {OPQ,OPQ},
{OPQ,OPQ}, {OPQ,OPQ}, {OPQ,OPQ}, {OPQ,OPQ},
{OPQ,OPQ}, {OPQ,OPQ}, {OPQ,OPQ}, {OPQ,OPQ},
{OPQ,OPQ}, {OPQ,OPQ}, {OPQ,OPQ}, {OPQ,OPQ},
{OPQ,OPQ}, {OPQ,OPQ}, {OPQ,OPQ}, {OPQ,OPQ},
{OPQ,OPQ}, {OPQ,OPQ}, {OPQ,OPQ}, {OPQ,OPQ},
{OPQ,OPQ}, {OPQ,OPQ}, {OPQ,OPQ}, {OPQ,OPQ},
{OPQ,OPQ}, {OPQ,OPQ}, {OPQ,OPQ}, {OPQ,OPQ},
{OPQ,OPQ}, {OPQ,OPQ}, {OPQ,OPQ}, {OPQ,OPQ},
{OPQ,OPQ}, {OPQ,OPQ}, {OPQ,OPQ}, {OPQ,OPQ},
{OPQ,OPQ}, {OPQ,OPQ}, {OPQ,OPQ}, {OPQ,OPQ},
{OPQ,OPQ}, {OPQ,OPQ}, {OPQ,OPQ}, {OPQ,OPQ},
{OPQ,OPQ}, {OPQ,OPQ}, {OPQ,OPQ}, {OPQ,OPQ},
{OPQ,OPQ}, {OPQ,OPQ}, {OPQ,OPQ}, {OPQ,OPQ},
{OPQ,OPQ}, {OPQ,OPQ}, {OPQ,OPQ}, {OPQ,OPQ},
{OPQ,OPQ}, {OPQ,OPQ}, {OPQ,OPQ}, {OPQ,OPQ},
{OPQ,OPQ}, {OPQ,OPQ}, {OPQ,OPQ}, {OPQ,OPQ},
{OPQ,OPQ}, {OPQ,OPQ}, {OPQ,OPQ}, {OPQ,OPQ},
{OPQ,OPQ}, {OPQ,OPQ}, {OPQ,OPQ}, {OPQ,OPQ},
{OPQ,OPQ}, {OPQ,OPQ}, {OPQ,OPQ}, {OPQ,OPQ},
{OPQ,OPQ}, {OPQ,OPQ}, {OPQ,OPQ}, {OPQ,OPQ},
{OPQ,OPQ}, {OPQ,OPQ}, {OPQ,OPQ}, {OPQ,OPQ},
{OPQ,OPQ}, {OPQ,OPQ}, {OPQ,OPQ}, {OPQ,OPQ},
{OPQ,OPQ}, {OPQ,OPQ}, {OPQ,OPQ}, {OPQ,OPQ},
{OPQ,OPQ}, {OPQ,OPQ}, {OPQ,OPQ}, {OPQ,OPQ},
{OPQ,OPQ}, {OPQ,OPQ}, {OPQ,OPQ}, {OPQ,OPQ},
{OPQ,OPQ}, {OPQ,OPQ}, {OPQ,OPQ}, {OPQ,OPQ},
{OPQ,OPQ}, {OPQ,OPQ}, {OPQ,OPQ}, {OPQ,OPQ},
{OPQ,OPQ}, {OPQ,OPQ}, {OPQ,OPQ}, {OPQ,OPQ},
{OPQ,OPQ}, {OPQ,OPQ}, {OPQ,OPQ}, {OPQ,OPQ},
{OPQ,OPQ}, {OPQ,OPQ}, {OPQ,OPQ}, {OPQ,OPQ},
{OPQ,OPQ}, {OPQ,OPQ}, {OPQ,OPQ}, {OPQ,OPQ},
{OPQ,OPQ}, {OPQ,OPQ}, {OPQ,OPQ}, {OPQ,OPQ},
{OPQ,OPQ}, {OPQ,OPQ}, {OPQ,OPQ}, {OPQ,OPQ},
{OPQ,OPQ}, {OPQ,OPQ}, {OPQ,OPQ}, {OPQ,OPQ},
{OPQ,OPQ}, {OPQ,OPQ}, {OPQ,OPQ}, {OPQ,OPQ},
{OPQ,OPQ}, {OPQ,OPQ}, {OPQ,OPQ}, {OPQ,OPQ},
{OPQ,OPQ}, {OPQ,OPQ}, {OPQ,OPQ}, {OPQ,OPQ},
{OPQ,OPQ}, {OPQ,OPQ}, {OPQ,OPQ}, {OPQ,OPQ},
{OPQ,OPQ}, {OPQ,OPQ}, {OPQ,OPQ}, {OPQ,OPQ},
{OPQ,OPQ}, {OPQ,OPQ}, {OPQ,OPQ}, {OPQ,OPQ},
{OPQ,OPQ}, {OPQ,OPQ}, {OPQ,OPQ}, {OPQ,OPQ},
{OPQ,OPQ}, {OPQ,OPQ}, {OPQ,OPQ}, {OPQ,OPQ},
{OPQ,OPQ}, {OPQ,OPQ}, {OPQ,OPQ}, {OPQ,OPQ},
{OPQ,OPQ}, {OPQ,OPQ}, {OPQ,OPQ}, {OPQ,OPQ},
{OPQ,OPQ}, {OPQ,OPQ}, {OPQ,OPQ}, {OPQ,OPQ},
{OPQ,OPQ}, {OPQ,OPQ}, {OPQ,OPQ}, {OPQ,OPQ},
{OPQ,OPQ}, {OPQ,OPQ}, {OPQ,OPQ}, {OPQ,OPQ},
{OPQ,OPQ}, {OPQ,OPQ}, {OPQ,OPQ}, {OPQ,OPQ},
{OPQ,OPQ}, {OPQ,OPQ}, {OPQ,OPQ}, {OPQ,OPQ},
{OPQ,OPQ}, {OPQ,OPQ}, {OPQ,OPQ}, {OPQ,OPQ},
{OPQ,OPQ}, {OPQ,OPQ}, {OPQ,OPQ}, {OPQ,OPQ},
{OPQ,OPQ}, {OPQ,OPQ}, {OPQ,OPQ}, {OPQ,OPQ},
{OPQ,OPQ}, {OPQ,OPQ}, {OPQ,OPQ}, {OPQ,OPQ},
{OPQ,OPQ}, {OPQ,OPQ}, {OPQ,OPQ}, {OPQ,OPQ},
{OPQ,OPQ}, {OPQ,OPQ}, {OPQ,OPQ}, {OPQ,OPQ},
{OPQ,OPQ}, {OPQ,OPQ}, {OPQ,OPQ}, {OPQ,OPQ},
{OPQ,OPQ}, {OPQ,OPQ}, {OPQ,OPQ}, {OPQ,OPQ},
{OPQ,OPQ}, {OPQ,OPQ}, {OPQ,OPQ}, {OPQ,OPQ},
{OPQ,OPQ}, {OPQ,OPQ}, {OPQ,OPQ}, {OPQ,OPQ},
{OPQ,OPQ}, {OPQ,OPQ}, {OPQ,OPQ}, {OPQ,OPQ},
{OPQ,OPQ}, {OPQ,OPQ}, {OPQ,OPQ}, {OPQ,OPQ},
{OPQ,OPQ}, {OPQ,OPQ}, {OPQ,OPQ}, {OPQ,OPQ},
{OPQ,OPQ}, {OPQ,OPQ}, {OPQ,OPQ}, {OPQ,OPQ},
{OPQ,OPQ}, {OPQ,OPQ}, {OPQ,OPQ}, {OPQ,OPQ},
{OPQ,OPQ}, {OPQ,OPQ}, {OPQ,OPQ}, {OPQ,OPQ},
{OPQ,OPQ}, {OPQ,OPQ}, {OPQ,OPQ}, {OPQ,OPQ},
{OPQ,OPQ}, {OPQ,OPQ}, {OPQ,OPQ}, {OPQ,OPQ},
{OPQ,OPQ}, {OPQ,OPQ}, {OPQ,OPQ}, {OPQ,OPQ},
{OPQ,OPQ}, {OPQ,OPQ}, {OPQ,OPQ}, {OPQ,OPQ},
{OPQ,OPQ}, {OPQ,OPQ}, {OPQ,OPQ}, {OPQ,OPQ},
{OPQ,OPQ}, {OPQ,OPQ}, {OPQ,OPQ}, {OPQ,OPQ},
{OPQ,OPQ}, {OPQ,OPQ}, {OPQ,OPQ}, {OPQ,OPQ},
{OPQ,OPQ}, {OPQ,OPQ}, {OPQ,OPQ}, {OPQ,OPQ},
{OPQ,OPQ}, {OPQ,OPQ}, {OPQ,OPQ}, {OPQ,OPQ},
{OPQ,OPQ}, {OPQ,OPQ}, {OPQ,OPQ}, {OPQ,OPQ},
{OPQ,OPQ}, {OPQ,OPQ}, {OPQ,OPQ}, {OPQ,OPQ},
{OPQ,OPQ}, {OPQ,OPQ}, {OPQ,OPQ}, {OPQ,OPQ},
{OPQ,OPQ}, {OPQ,OPQ}, {OPQ,OPQ}, {OPQ,OPQ},
{OPQ,OPQ}, {OPQ,OPQ}, {OPQ,OPQ}, {OPQ,OPQ},
{OPQ,OPQ}, {OPQ,OPQ}, {OPQ,OPQ}, {OPQ,OPQ},
{OPQ,OPQ}, {OPQ,OPQ}, {OPQ,OPQ}, {OPQ,OPQ},
{OPQ,OPQ}, {OPQ,OPQ}, {OPQ,OPQ}, {OPQ,OPQ},
{OPQ,OPQ}, {OPQ,OPQ}, {OPQ,OPQ}, {OPQ,OPQ},
{OPQ,OPQ}, {OPQ,OPQ}, {OPQ,OPQ}, {OPQ,OPQ},
{OPQ,OPQ}, {OPQ,OPQ}, {OPQ,OPQ}, {OPQ,OPQ},
{OPQ,OPQ}, {OPQ,OPQ}, {OPQ,OPQ}, {OPQ,OPQ},
{OPQ,OPQ}, {OPQ,OPQ}, {OPQ,OPQ}, {OPQ,OPQ},
{OPQ,OPQ}, {OPQ,OPQ}, {OPQ,OPQ}, {OPQ,OPQ},
{OPQ,OPQ}, {OPQ,OPQ}, {OPQ,OPQ}, {OPQ,OPQ},
{OPQ,OPQ}, {OPQ,OPQ}, {OPQ,OPQ}, {OPQ,OPQ},
{OPQ,OPQ}, {OPQ,OPQ}, {OPQ,OPQ}, {OPQ,OPQ},
{OPQ,OPQ}, {OPQ,OPQ}, {OPQ,OPQ}, {OPQ,OPQ},
{OPQ,OPQ}, {OPQ,OPQ}, {OPQ,OPQ}, {OPQ,OPQ},
{OPQ,OPQ}, {OPQ,OPQ}, {OPQ,OPQ}, {OPQ,OPQ},
{OPQ,OPQ}, {OPQ,OPQ}, {OPQ,OPQ}, {OPQ,OPQ},
{OPQ,OPQ}, {OPQ,OPQ}, {OPQ,OPQ}, {OPQ,OPQ},
{OPQ,OPQ}, {OPQ,OPQ}, {OPQ,OPQ}, {OPQ,OPQ},
{OPQ,OPQ}, {OPQ,OPQ}, {OPQ,OPQ}, {OPQ,OPQ},
{OPQ,OPQ}, {OPQ,OPQ}, {OPQ,OPQ}, {OPQ,OPQ},
{OPQ,OPQ}, {OPQ,OPQ}, {OPQ,OPQ}, {OPQ,OPQ},
{OPQ,OPQ}, {OPQ,OPQ}, {OPQ,OPQ}, {OPQ,OPQ},
{OPQ,OPQ}, {OPQ,OPQ}, {OPQ,OPQ}, {OPQ,OPQ},
{OPQ,OPQ}, {OPQ,OPQ}, {OPQ,OPQ}, {OPQ,OPQ},
{OPQ,OPQ}, {OPQ,OPQ}, {OPQ,OPQ}, {OPQ,OPQ},
{OPQ,OPQ}, {OPQ,OPQ}, {OPQ,OPQ}, {OPQ,OPQ},
{OPQ,OPQ}, {OPQ,OPQ}, {OPQ,OPQ}, {OPQ,OPQ},
{OPQ,OPQ}, {OPQ,OPQ}, {OPQ,OPQ}, {OPQ,OPQ},
{OPQ,OPQ}, {OPQ,OPQ}, {OPQ,OPQ}, {OPQ,OPQ},
{OPQ,OPQ}, {OPQ,OPQ}, {OPQ,OPQ}, {OPQ,OPQ},
{OPQ,OPQ}, {OPQ,OPQ}, {OPQ,OPQ}, {OPQ,OPQ},
{OPQ,OPQ}, {OPQ,OPQ}, {OPQ,OPQ}, {OPQ,OPQ},
{OPQ,OPQ}, {OPQ,OPQ}, {OPQ,OPQ}, {OPQ,OPQ},
{OPQ,OPQ}, {OPQ,OPQ}, {OPQ,OPQ}, {OPQ,OPQ},
{OPQ,OPQ}, {OPQ,OPQ}, {OPQ,OPQ}, {OPQ,OPQ},
{OPQ,OPQ}, {OPQ,OPQ}, {OPQ,OPQ}, {OPQ,OPQ},
{OPQ,OPQ}, {OPQ,OPQ}, {OPQ,OPQ}, {OPQ,OPQ},
{OPQ,OPQ}, {OPQ,OPQ}, {OPQ,OPQ}, {OPQ,OPQ},
{OPQ,OPQ}, {OPQ,OPQ}, {OPQ,OPQ}, {OPQ,OPQ},
{OPQ,OPQ}, {OPQ,OPQ}, {OPQ,OPQ}, {OPQ,OPQ},
{OPQ,OPQ}, {OPQ,OPQ}, {OPQ,OPQ}, {OPQ,OPQ},
{OPQ,OPQ}, {OPQ,OPQ}, {OPQ,OPQ}, {OPQ,OPQ},
{OPQ,OPQ}, {OPQ,OPQ}, {OPQ,OPQ}, {OPQ,OPQ},
{OPQ,OPQ}, {OPQ,OPQ}, {OPQ,OPQ}, {OPQ,OPQ},
{OPQ,OPQ}, {OPQ,OPQ}, {OPQ,OPQ}, {OPQ,OPQ},
{OPQ,OPQ}, {OPQ,OPQ}, {OPQ,OPQ}, {OPQ,OPQ},
{OPQ,OPQ}, {OPQ,OPQ}, {OPQ,OPQ}, {OPQ,OPQ},
{OPQ,OPQ}, {OPQ,OPQ}, {OPQ,OPQ}, {OPQ,OPQ},
{OPQ,OPQ}, {OPQ,OPQ}, {OPQ,OPQ}, {OPQ,OPQ},
{OPQ,OPQ}, {OPQ,OPQ}, {OPQ,OPQ}, {OPQ,OPQ},
{OPQ,OPQ}, {OPQ,OPQ}, {OPQ,OPQ}, {OPQ,OPQ},
{OPQ,OPQ}, {OPQ,OPQ}, {OPQ,OPQ}, {OPQ,OPQ},
{OPQ,OPQ}, {OPQ,OPQ}, {OPQ,OPQ}, {OPQ,OPQ},
{OPQ,OPQ}, {OPQ,OPQ}, {OPQ,OPQ}, {OPQ,OPQ},
{OPQ,OPQ}, {OPQ,OPQ}, {OPQ,OPQ}, {OPQ,OPQ},
{OPQ,OPQ}, {OPQ,OPQ}, {OPQ,OPQ}, {OPQ,OPQ},
{OPQ,OPQ}, {OPQ,OPQ}, {OPQ,OPQ}, {OPQ,OPQ},
{OPQ,OPQ}, {OPQ,OPQ}, {OPQ,OPQ}, {OPQ,OPQ},
{OPQ,OPQ}, {OPQ,OPQ}, {OPQ,OPQ}, {OPQ,OPQ},
{OPQ,OPQ}, {OPQ,OPQ}, {OPQ,OPQ}, {OPQ,OPQ},
{OPQ,OPQ}, {OPQ,OPQ}, {OPQ,OPQ}, {OPQ,OPQ},
{OPQ,OPQ}, {OPQ,OPQ}, {OPQ,OPQ}, {OPQ,OPQ},
{OPQ,OPQ}, {OPQ,OPQ}, {OPQ,OPQ}, {OPQ,OPQ},
{OPQ,OPQ}, {OPQ,OPQ}, {OPQ,OPQ}, {OPQ,OPQ},
{OPQ,OPQ}, {OPQ,OPQ}, {OPQ,OPQ}, {OPQ,OPQ},
{OPQ,OPQ}, {OPQ,OPQ}, {OPQ,OPQ}, {OPQ,OPQ},
{OPQ,OPQ}, {OPQ,OPQ}, {OPQ,OPQ}, {OPQ,OPQ},
{OPQ,OPQ}, {OPQ,OPQ}, {OPQ,OPQ}, {OPQ,OPQ},
{OPQ,OPQ}, {OPQ,OPQ}, {OPQ,OPQ}, {OPQ,OPQ},
{OPQ,OPQ}, {OPQ,OPQ}, {OPQ,OPQ}, {OPQ,OPQ},
{OPQ,OPQ}, {OPQ,OPQ}, {OPQ,OPQ}, {OPQ,OPQ},
{OPQ,OPQ}, {OPQ,OPQ}, {OPQ,OPQ}, {OPQ,OPQ},
{OPQ,OPQ}, {OPQ,OPQ}, {OPQ,OPQ}, {OPQ,OPQ},
{OPQ,OPQ}, {OPQ,OPQ}, {OPQ,OPQ}, {OPQ,OPQ},
{OPQ,OPQ}, {OPQ,OPQ}, {OPQ,OPQ}, {OPQ,OPQ},
{OPQ,OPQ}, {OPQ,OPQ}, {OPQ,OPQ}, {OPQ,OPQ},
{OPQ,OPQ}, {OPQ,OPQ}, {OPQ,OPQ}, {OPQ,OPQ},
{OPQ,OPQ}, {OPQ,OPQ}, {OPQ,OPQ}, {OPQ,OPQ},
{OPQ,OPQ}, {OPQ,OPQ}, {OPQ,OPQ}, {OPQ,OPQ},
{OPQ,OPQ}, {OPQ,OPQ}, {OPQ,OPQ}, {OPQ,OPQ},
{OPQ,OPQ}, {OPQ,OPQ}, {OPQ,OPQ}, {OPQ,OPQ},
{OPQ,OPQ}, {OPQ,OPQ}, {OPQ,OPQ}, {OPQ,OPQ},
{OPQ,OPQ}, {OPQ,OPQ}, {OPQ,OPQ}, {OPQ,OPQ},
{OPQ,OPQ}, {OPQ,OPQ}, {OPQ,OPQ}, {OPQ,OPQ},
{OPQ,OPQ}, {OPQ,OPQ}, {OPQ,OPQ}, {OPQ,OPQ},
{OPQ,OPQ}, {OPQ,OPQ}, {OPQ,OPQ}, {OPQ,OPQ},
{OPQ,OPQ}, {OPQ,OPQ}, {OPQ,OPQ}, {OPQ,OPQ},
{OPQ,OPQ}, {OPQ,OPQ}, {OPQ,OPQ}, {OPQ,OPQ},
{OPQ,OPQ}, {OPQ,OPQ}, {OPQ,OPQ}, {OPQ,OPQ},
{OPQ,OPQ}, {OPQ,OPQ}, {OPQ,OPQ}, {OPQ,OPQ},
{OPQ,OPQ}, {OPQ,OPQ}, {OPQ,OPQ}, {OPQ,OPQ},
{OPQ,OPQ}, {OPQ,OPQ}, {OPQ,OPQ}, {OPQ,OPQ},
{OPQ,OPQ}, {OPQ,OPQ}, {OPQ,OPQ}, {OPQ,OPQ},
{OPQ,OPQ}, {OPQ,OPQ}, {OPQ,OPQ}, {OPQ,OPQ},
{OPQ,OPQ}, {OPQ,OPQ}, {OPQ,OPQ}, {OPQ,OPQ},
{OPQ,OPQ}, {OPQ,OPQ}, {OPQ,OPQ}, {OPQ,OPQ},
{OPQ,OPQ}, {OPQ,OPQ}, {OPQ,OPQ}, {OPQ,OPQ},
{OPQ,OPQ}, {OPQ,OPQ}, {OPQ,OPQ}, {OPQ,OPQ},
{OPQ,OPQ}, {OPQ,OPQ}, {OPQ,OPQ}, {OPQ,OPQ},
{OPQ,OPQ}, {OPQ,OPQ}, {OPQ,OPQ}, {OPQ,OPQ},
{OPQ,OPQ}, {OPQ,OPQ}, {OPQ,OPQ}, {OPQ,OPQ},
{OPQ,OPQ}, {OPQ,OPQ}, {OPQ,OPQ}, {OPQ,OPQ},
{OPQ,OPQ}, {OPQ,OPQ}, {OPQ,OPQ}, {OPQ,OPQ},
{OPQ,OPQ}, {OPQ,OPQ}, {OPQ,OPQ}, {OPQ,OPQ},
{OPQ,OPQ}, {OPQ,OPQ}, {OPQ,OPQ}, {OPQ,OPQ},
{OPQ,OPQ}, {OPQ,OPQ}, {OPQ,OPQ}, {OPQ,OPQ},
{OPQ,OPQ}, {OPQ,OPQ}, {OPQ,OPQ}, {OPQ,OPQ},
{OPQ,OPQ}, {OPQ,OPQ}, {OPQ,OPQ}, {OPQ,OPQ},
{OPQ,OPQ}, {OPQ,OPQ}, {OPQ,OPQ}, {OPQ,OPQ},
{OPQ,OPQ}, {OPQ,OPQ}, {OPQ,OPQ}, {OPQ,OPQ},
{OPQ,OPQ}, {OPQ,OPQ}, {OPQ,OPQ}, {OPQ,OPQ},
{OPQ,OPQ}, {OPQ,OPQ}, {OPQ,OPQ}, {OPQ,OPQ},
{OPQ,OPQ}, {OPQ,OPQ}, {OPQ,OPQ}, {OPQ,OPQ},
{OPQ,OPQ}, {OPQ,OPQ}, {OPQ,OPQ}, {OPQ,OPQ},
{OPQ,OPQ}, {OPQ,OPQ}, {OPQ,OPQ}, {OPQ,OPQ},
{OPQ,OPQ}, {OPQ,OPQ}, {OPQ,OPQ}, {OPQ,OPQ},
{OPQ,OPQ}, {OPQ,OPQ}, {OPQ,OPQ}, {OPQ,OPQ},
{OPQ,OPQ}, {OPQ,OPQ}, {OPQ,OPQ}, {OPQ,OPQ},
};

__device__ __forceinline__ float bf2f(u16 v) { return __uint_as_float(((unsigned)v) << 16); }
__device__ __forceinline__ u16 f2bf(float f) {
  __hip_bfloat16 b = __float2bfloat16(f);
  return *reinterpret_cast<u16*>(&b);
}
__device__ __forceinline__ float bfr(float f) { return bf2f(f2bf(f)); }
__device__ __forceinline__ int row_of_pos(int p) { return 4 * (p & 511) + (p >> 9); }

__device__ __forceinline__ float2 cadd(float2 a, float2 b) { return make_float2(a.x + b.x, a.y + b.y); }
__device__ __forceinline__ float2 csub(float2 a, float2 b) { return make_float2(a.x - b.x, a.y - b.y); }
__device__ __forceinline__ float2 cmul(float2 a, float2 b) {
  return make_float2(a.x * b.x - a.y * b.y, a.x * b.y + a.y * b.x);
}
template <bool INV>
__device__ __forceinline__ float2 twmul(float2 a, float2 w) {
  if (INV) return make_float2(a.x * w.x + a.y * w.y, a.y * w.x - a.x * w.y);
  else return make_float2(a.x * w.x - a.y * w.y, a.x * w.y + a.y * w.x);
}
template <bool INV>
__device__ __forceinline__ float2 mul_mi(float2 a) {
  if (INV) return make_float2(-a.y, a.x);
  else return make_float2(a.y, -a.x);
}
template <bool INV>
__device__ __forceinline__ void fft4(float2& v0, float2& v1, float2& v2, float2& v3) {
  float2 a0 = cadd(v0, v2), a1 = csub(v0, v2);
  float2 b0 = cadd(v1, v3), b1 = mul_mi<INV>(csub(v1, v3));
  v0 = cadd(a0, b0);
  v2 = csub(a0, b0);
  v1 = cadd(a1, b1);
  v3 = csub(a1, b1);
}
template <bool INV>
__device__ __forceinline__ void fft8(float2 (&v)[8]) {
  const float r = 0.70710678118654752440f;
  float2 e0 = cadd(v[0], v[4]), o0 = csub(v[0], v[4]);
  float2 e1 = cadd(v[1], v[5]), o1 = csub(v[1], v[5]);
  float2 e2 = cadd(v[2], v[6]), o2 = csub(v[2], v[6]);
  float2 e3 = cadd(v[3], v[7]), o3 = csub(v[3], v[7]);
  if (INV) {
    o1 = make_float2((o1.x - o1.y) * r, (o1.x + o1.y) * r);
    o3 = make_float2((-o3.x - o3.y) * r, (o3.x - o3.y) * r);
  } else {
    o1 = make_float2((o1.x + o1.y) * r, (o1.y - o1.x) * r);
    o3 = make_float2((o3.y - o3.x) * r, (-o3.x - o3.y) * r);
  }
  o2 = mul_mi<INV>(o2);
  fft4<INV>(e0, e1, e2, e3);
  fft4<INV>(o0, o1, o2, o3);
  v[0] = e0; v[2] = e1; v[4] = e2; v[6] = e3;
  v[1] = o0; v[3] = o1; v[5] = o2; v[7] = o3;
}
__device__ __forceinline__ int padi(int i) { return i + (i >> 4); }

template <int CTRL>
__device__ __forceinline__ float dpp_mov(float v) {
  return __int_as_float(__builtin_amdgcn_update_dpp(0, __float_as_int(v), CTRL, 0xF, 0xF, true));
}
__device__ __forceinline__ float wave_sum(float v) {
  v += dpp_mov<0xB1>(v);
  v += dpp_mov<0x4E>(v);
  v += dpp_mov<0x141>(v);
  v += dpp_mov<0x140>(v);
  const float a = __int_as_float(__builtin_amdgcn_readlane(__float_as_int(v), 0));
  const float b = __int_as_float(__builtin_amdgcn_readlane(__float_as_int(v), 16));
  const float c = __int_as_float(__builtin_amdgcn_readlane(__float_as_int(v), 32));
  const float d = __int_as_float(__builtin_amdgcn_readlane(__float_as_int(v), 48));
  return (a + b) + (c + d);
}

__device__ __forceinline__ void ln_rows2(float (&xa)[8], float (&xb)[8]) {
  float sa = 0.f, sb = 0.f;
#pragma unroll
  for (int m = 0; m < 8; ++m) { sa += xa[m]; sb += xb[m]; }
  sa = wave_sum(sa);
  sb = wave_sum(sb);
  const float ma = sa * (1.0f / 512.0f), mbn = sb * (1.0f / 512.0f);
  float qa = 0.f, qb = 0.f;
#pragma unroll
  for (int m = 0; m < 8; ++m) {
    float da = xa[m] - ma, db = xb[m] - mbn;
    qa += da * da; qb += db * db;
  }
  qa = wave_sum(qa);
  qb = wave_sum(qb);
  const float mab = bfr(ma), mbb = bfr(mbn);
  const float dena = bfr(sqrtf(bfr(bfr(qa * (1.0f / 512.0f)) + 1e-5f)));
  const float denb = bfr(sqrtf(bfr(bfr(qb * (1.0f / 512.0f)) + 1e-5f)));
  const float ra = __builtin_amdgcn_rcpf(dena), rb = __builtin_amdgcn_rcpf(denb);
#pragma unroll
  for (int m = 0; m < 8; ++m) {
    const float na = bfr(xa[m] - mab), nb = bfr(xb[m] - mbb);
    float q0 = na * ra, q1 = nb * rb;
    q0 = fmaf(fmaf(-q0, dena, na), ra, q0);
    q1 = fmaf(fmaf(-q1, denb, nb), rb, q1);
    xa[m] = bfr(q0);
    xb[m] = bfr(q1);
  }
}

template <bool INV>
__device__ __forceinline__ void wave_fft512(float2 (&v)[8], float2* ex, int j, const float2 (&tw1)[7],
                                            const float2 (&tw2)[7]) {
  fft8<INV>(v);
#pragma unroll
  for (int m = 0; m < 8; ++m) ex[padi(j * 8 + m)] = v[m];
  __builtin_amdgcn_wave_barrier();
#pragma unroll
  for (int m = 0; m < 8; ++m) v[m] = ex[padi(j + 64 * m)];
  __builtin_amdgcn_wave_barrier();
#pragma unroll
  for (int m = 1; m < 8; ++m) v[m] = twmul<INV>(v[m], tw1[m - 1]);
  fft8<INV>(v);
#pragma unroll
  for (int m = 0; m < 8; ++m) ex[padi((j >> 3) * 64 + (j & 7) + 8 * m)] = v[m];
  __builtin_amdgcn_wave_barrier();
#pragma unroll
  for (int m = 0; m < 8; ++m) v[m] = ex[padi(j + 64 * m)];
  __builtin_amdgcn_wave_barrier();
#pragma unroll
  for (int m = 1; m < 8; ++m) v[m] = twmul<INV>(v[m], tw2[m - 1]);
  fft8<INV>(v);
}

#define EXW 544
#define TSTR 10

__device__ __forceinline__ void transpose8x8_store(const uint4 (&r)[8], uint4* __restrict__ dst) {
#pragma unroll
  for (int c = 0; c < 8; ++c) {
    unsigned o[4];
#pragma unroll
    for (int d = 0; d < 4; ++d) {
      const unsigned* a = reinterpret_cast<const unsigned*>(&r[2 * d]);
      const unsigned* b = reinterpret_cast<const unsigned*>(&r[2 * d + 1]);
      o[d] = __builtin_amdgcn_perm(b[c >> 1], a[c >> 1], (c & 1) ? 0x07060302u : 0x05040100u);
    }
    dst[c] = make_uint4(o[0], o[1], o[2], o[3]);
  }
}
#define NG_GU (172 * 64 * 2)
#define NG_D (64 * KS2 * 4)
#define SIDE_W ((NG_GU + NG_D + 255) / 256)

__global__ __launch_bounds__(256) void k1_ln_rfft(const u16* __restrict__ x, const u16* __restrict__ wr,
                                                  const u16* __restrict__ wi, float2* __restrict__ spec,
                                                  unsigned* __restrict__ wt, const u16* __restrict__ wg,
                                                  const u16* __restrict__ wu, const u16* __restrict__ wd,
                                                  uint4* __restrict__ wgu_t, uint4* __restrict__ wd_t DIAG_PARAM) {
  __shared__ __attribute__((aligned(16))) float2 sm[4 * EXW + 256 * TSTR];
  const int t = threadIdx.x;
  STAMP(0, 0);
  if (blockIdx.x >= 512) {
    const int g = (blockIdx.x - 512) * 256 + t;
    uint4 r[8];
    if (g < NG_GU) {
      const int n8 = g % 172, sq = (g / 172) & 63, which = g / (172 * 64);
      const u16* src = (which ? wu : wg) + (8 * sq) * F_DIM + 8 * n8;
#pragma unroll
      for (int i = 0; i < 8; ++i) r[i] = *reinterpret_cast<const uint4*>(src + i * F_DIM);
      transpose8x8_store(r, wgu_t + (((n8 >> 1) * 16 + (sq >> 2)) * 2 + which) * 64 + 16 * (sq & 3) + 8 * (n8 & 1));
    } else if (g < NG_GU + NG_D) {
      const int g2 = g - NG_GU;
      const int n8 = g2 & 63, q = (g2 >> 6) & 3, s = g2 >> 8;
      const u16* src = wd + (32 * s + 4 * q) * D_DIM + 8 * n8;
#pragma unroll
      for (int i = 0; i < 8; ++i) r[i] = *reinterpret_cast<const uint4*>(src + ((i & 3) + 16 * (i >> 2)) * D_DIM);
      transpose8x8_store(r, wd_t + ((n8 >> 1) * KS2 + s) * 64 + 16 * q + 8 * (n8 & 1));
    }
    STAMP(0, 9);
    return;
  }
  if (blockIdx.x >= 256) {
    const int m0 = (blockIdx.x - 256) * 8;
    unsigned p[8];
#pragma unroll
    for (int r = 0; r < 8; ++r)
      p[r] = (unsigned)wr[(m0 + r) * DF + t] | ((unsigned)wi[(m0 + r) * DF + t] << 16);
    uint4* dst = reinterpret_cast<uint4*>(wt + t * S_LEN + m0);
    dst[0] = make_uint4(p[0], p[1], p[2], p[3]);
    dst[1] = make_uint4(p[4], p[5], p[6], p[7]);
    if (t < 8) wt[256 * S_LEN + m0 + t] = (unsigned)wr[(m0 + t) * DF + 256] | ((unsigned)wi[(m0 + t) * DF + 256] << 16);
    STAMP(0, 8);
    return;
  }
  const int w = t >> 6, j = t & 63;
  const int p0 = blockIdx.x * 8;
  const int ra = row_of_pos(p0 + 2 * w), rb = row_of_pos(p0 + 2 * w + 1);
  float2* ex = sm + w * EXW;
  float2* tile = sm + 4 * EXW;

  float xa[8], xb[8];
#pragma unroll
  for (int m = 0; m < 8; ++m) {
    xa[m] = bf2f(x[ra * D_DIM + j + 64 * m]);
    xb[m] = bf2f(x[rb * D_DIM + j + 64 * m]);
  }
  float2 tw1[7], tw2[7];
#pragma unroll
  for (int m = 1; m < 8; ++m) {
    tw1[m - 1] = g_tw[32 * (j & 7) * m];
    tw2[m - 1] = g_tw[4 * j * m];
  }
  STAMPW(0, 1);
  ln_rows2(xa, xb);
  float2 v[8];
#pragma unroll
  for (int m = 0; m < 8; ++m) v[m] = make_float2(xa[m], xb[m]);
  STAMP(0, 2);
  wave_fft512<false>(v, ex, j, tw1, tw2);
  STAMP(0, 3);
  __builtin_amdgcn_wave_barrier();
#pragma unroll
  for (int m = 4; m < 8; ++m) ex[padi(j + 64 * m)] = v[m];
  if (j == 0) ex[padi(0)] = v[0];
  __builtin_amdgcn_wave_barrier();
#pragma unroll
  for (int m = 0; m < 4; ++m) {
    const int k = j + 64 * m;
    const float2 zc = ex[padi((512 - k) & 511)];
    float4 o;
    if (k == 0) {
      const float2 zn = ex[padi(256)];
      o = make_float4(v[0].x, zn.x, v[0].y, zn.y);
    } else {
      const float2 z = v[m];
      o.x = 0.5f * (z.x + zc.x);
      o.y = 0.5f * (z.y - zc.y);
      o.z = 0.5f * (z.y + zc.y);
      o.w = 0.5f * (zc.x - z.x);
    }
    *reinterpret_cast<float4*>(tile + k * TSTR + 2 * w) = o;
  }
  __syncthreads();
#pragma unroll
  for (int i = 0; i < 4; ++i) {
    const int idx = t + 256 * i;
    const int k = idx >> 2, part = idx & 3;
    const float4 o = *reinterpret_cast<const float4*>(tile + k * TSTR + 2 * part);
    *reinterpret_cast<float4*>(spec + k * S_LEN + p0 + 2 * part) = o;
  }
  STAMP(0, 4);
  STAMPW(0, 5);
}

__device__ __forceinline__ float2 unpack_w(unsigned p) {
  return make_float2(__uint_as_float(p << 16), __uint_as_float(p & 0xffff0000u));
}

__global__ __launch_bounds__(256) void k2_colfft(float2* __restrict__ spec, const unsigned* __restrict__ wt DIAG_PARAM) {
  __shared__ __attribute__((aligned(16))) float2 sm[4 * EXW + 3 * 2048];
  const int t = threadIdx.x;
  STAMP(1, 0);
  const int k = blockIdx.x;
  const int w = t >> 6, j = t & 63;
  float2* ex = sm + w * EXW;
  float2* xb1 = sm + 4 * EXW;
  float2* xb2 = xb1 + 2048;
  float2* zb = xb2 + 2048;
  float2* col = spec + k * S_LEN;
  float2 v[8];
#pragma unroll
  for (int m = 0; m < 8; ++m) v[m] = col[w * 512 + j + 64 * m];
  const int k0 = j + 128 * w;
  unsigned wp[8];
#pragma unroll
  for (int q = 0; q < 4; ++q) {
    wp[q] = wt[k * S_LEN + k0 + 512 * q];
    wp[4 + q] = wt[k * S_LEN + k0 + 64 + 512 * q];
  }
  float2 tw1[7], tw2[7], twa[3], twb[3];
#pragma unroll
  for (int m = 1; m < 8; ++m) {
    tw1[m - 1] = g_tw[32 * (j & 7) * m];
    tw2[m - 1] = g_tw[4 * j * m];
  }
#pragma unroll
  for (int ww = 1; ww < 4; ++ww) {
    twa[ww - 1] = g_tw[k0 * ww];
    twb[ww - 1] = g_tw[(k0 + 64) * ww];
  }
  STAMPW(1, 1);
  wave_fft512<false>(v, ex, j, tw1, tw2);
#pragma unroll
  for (int m = 0; m < 8; ++m) xb1[w * 512 + m * 64 + j] = v[m];
  __syncthreads();
  float2 a[4], b[4];
#pragma unroll
  for (int ww = 0; ww < 4; ++ww) {
    a[ww] = xb1[ww * 512 + (2 * w) * 64 + j];
    b[ww] = xb1[ww * 512 + (2 * w + 1) * 64 + j];
  }
#pragma unroll
  for (int ww = 1; ww < 4; ++ww) {
    a[ww] = twmul<false>(a[ww], twa[ww - 1]);
    b[ww] = twmul<false>(b[ww], twb[ww - 1]);
  }
  fft4<false>(a[0], a[1], a[2], a[3]);
  fft4<false>(b[0], b[1], b[2], b[3]);
  STAMP(1, 2);
  if (k != 0) {
#pragma unroll
    for (int q = 0; q < 4; ++q) {
      a[q] = cmul(a[q], unpack_w(wp[q]));
      b[q] = cmul(b[q], unpack_w(wp[4 + q]));
    }
  } else {
#pragma unroll
    for (int q = 0; q < 4; ++q) {
      zb[k0 + 512 * q] = a[q];
      zb[k0 + 64 + 512 * q] = b[q];
    }
    __syncthreads();
#pragma unroll
    for (int i = 0; i < 8; ++i) {
      const int f = k0 + (i >> 2) * 64 + 512 * (i & 3), fn = (2048 - f) & 2047;
      const float2 z = (i < 4) ? a[i & 3] : b[i & 3];
      const float2 zc = zb[fn];
      const float2 x0 = make_float2(0.5f * (z.x + zc.x), 0.5f * (z.y - zc.y));
      const float2 x1 = make_float2(0.5f * (z.y + zc.y), 0.5f * (zc.x - z.x));
      const float2 w0 = unpack_w(wp[i]), w0n = unpack_w(wt[fn]);
      const float2 w1 = unpack_w(wt[256 * S_LEN + f]), w1n = unpack_w(wt[256 * S_LEN + fn]);
      const float2 g0 = make_float2(0.5f * (w0.x + w0n.x), 0.5f * (w0.y - w0n.y));
      const float2 g1 = make_float2(0.5f * (w1.x + w1n.x), 0.5f * (w1.y - w1n.y));
      const float2 a0 = cmul(x0, g0), a1 = cmul(x1, g1);
      const float2 r = make_float2(a0.x - a1.y, a0.y + a1.x);
      if (i < 4) a[i & 3] = r; else b[i & 3] = r;
    }
  }
  STAMP(1, 3);
  fft4<true>(a[0], a[1], a[2], a[3]);
  fft4<true>(b[0], b[1], b[2], b[3]);
#pragma unroll
  for (int ww = 1; ww < 4; ++ww) {
    a[ww] = twmul<true>(a[ww], twa[ww - 1]);
    b[ww] = twmul<true>(b[ww], twb[ww - 1]);
  }
#pragma unroll
  for (int ww = 0; ww < 4; ++ww) {
    xb2[ww * 512 + (2 * w) * 64 + j] = a[ww];
    xb2[ww * 512 + (2 * w + 1) * 64 + j] = b[ww];
  }
  __syncthreads();
#pragma unroll
  for (int m = 0; m < 8; ++m) v[m] = xb2[w * 512 + m * 64 + j];
  wave_fft512<true>(v, ex, j, tw1, tw2);
#pragma unroll
  for (int m = 0; m < 8; ++m) col[w * 512 + j + 64 * m] = v[m];
  STAMP(1, 4);
  STAMPW(1, 5);
}

__global__ __launch_bounds__(256) void k3_irfft_ln(const float2* __restrict__ spec, const u16* __restrict__ x,
                                                   u16* __restrict__ h, u16* __restrict__ xn_t DIAG_PARAM) {
  __shared__ __attribute__((aligned(16))) float2 sm[4 * EXW + 256 * TSTR];
  const int t = threadIdx.x;
  STAMP(2, 0);
  const int w = t >> 6, j = t & 63;
  const int p0 = blockIdx.x * 8;
  const int ra = row_of_pos(p0 + 2 * w), rb = row_of_pos(p0 + 2 * w + 1);
  float2* ex = sm + w * EXW;
  float2* tile = sm + 4 * EXW;
  float4 ld[4];
#pragma unroll
  for (int i = 0; i < 4; ++i) {
    const int idx = t + 256 * i;
    const int k = idx >> 2, part = idx & 3;
    ld[i] = *reinterpret_cast<const float4*>(spec + k * S_LEN + p0 + 2 * part);
  }
  float xa[8], xb[8];
#pragma unroll
  for (int m = 0; m < 8; ++m) {
    xa[m] = bf2f(x[ra * D_DIM + j + 64 * m]);
    xb[m] = bf2f(x[rb * D_DIM + j + 64 * m]);
  }
  float2 tw1[7], tw2[7];
#pragma unroll
  for (int m = 1; m < 8; ++m) {
    tw1[m - 1] = g_tw[32 * (j & 7) * m];
    tw2[m - 1] = g_tw[4 * j * m];
  }
#pragma unroll
  for (int i = 0; i < 4; ++i) {
    const int idx = t + 256 * i;
    const int k = idx >> 2, part = idx & 3;
    *reinterpret_cast<float4*>(tile + k * TSTR + 2 * part) = ld[i];
  }
  __syncthreads();
  STAMP(2, 1);
  float2 v[8];
#pragma unroll
  for (int m = 0; m < 8; ++m) {
    const int n = j + 64 * m;
    const int k = (m < 4) ? n : (512 - n);
    const float4 y = *reinterpret_cast<const float4*>(tile + (k & 255) * TSTR + 2 * w);
    if (n == 0) v[m] = make_float2(y.x, y.z);
    else if (n == 256) v[m] = make_float2(y.y, y.w);
    else if (m < 4) v[m] = make_float2(y.x - y.w, y.y + y.z);
    else v[m] = make_float2(y.x + y.w, y.z - y.y);
  }
  wave_fft512<true>(v, ex, j, tw1, tw2);
  STAMP(2, 2);
  const float sc = 1.0f / (2048.0f * 512.0f);
#pragma unroll
  for (int m = 0; m < 8; ++m) {
    xa[m] = bfr(xa[m] + bfr(v[m].x * sc));
    xb[m] = bfr(xb[m] + bfr(v[m].y * sc));
  }
  u16* st_h = reinterpret_cast<u16*>(ex);
  u16* st_x = st_h + 2 * 512;
  __builtin_amdgcn_wave_barrier();
#pragma unroll
  for (int m = 0; m < 8; ++m) {
    st_h[j + 64 * m] = f2bf(xa[m]);
    st_h[512 + j + 64 * m] = f2bf(xb[m]);
  }
  ln_rows2(xa, xb);
#pragma unroll
  for (int m = 0; m < 8; ++m) {
    st_x[j + 64 * m] = f2bf(xa[m]);
    st_x[512 + j + 64 * m] = f2bf(xb[m]);
  }
  __builtin_amdgcn_wave_barrier();
  STAMP(2, 3);
  {
    const int pr = p0 + 2 * w;
    uint4* dst = reinterpret_cast<uint4*>(h + pr * D_DIM);
    dst[j] = reinterpret_cast<const uint4*>(st_h)[j];
    dst[64 + j] = reinterpret_cast<const uint4*>(st_h)[64 + j];
    const int mt = pr >> 4, r16 = pr & 15;
    uint4* xd = reinterpret_cast<uint4*>(xn_t) + (mt * 16 + (j >> 2)) * 64 + (j & 3) * 16 + r16;
    xd[0] = reinterpret_cast<const uint4*>(st_x)[j];
    xd[1] = reinterpret_cast<const uint4*>(st_x)[64 + j];
  }
  STAMP(2, 4);
  STAMPW(2, 5);
}

__device__ __forceinline__ unsigned geglu2(float g0, float g1, float u0, float u1) {
  g0 = bfr(g0); g1 = bfr(g1); u0 = bfr(u0); u1 = bfr(u1);
  const float c1 = -2.0f * 0.7978845608028654f * 1.4426950408889634f;
  const float c2 = c1 * 0.044715f;
  const float z0 = g0 * fmaf(g0 * g0, c2, c1), z1 = g1 * fmaf(g1 * g1, c2, c1);
  const float e0 = __builtin_amdgcn_exp2f(z0), e1 = __builtin_amdgcn_exp2f(z1);
  const float a0 = bfr(g0 * __builtin_amdgcn_rcpf(1.0f + e0));
  const float a1 = bfr(g1 * __builtin_amdgcn_rcpf(1.0f + e1));
  return (unsigned)f2bf(a0 * u0) | ((unsigned)f2bf(a1 * u1) << 16);
}

#define K4_PF 7
template <int NT>
__device__ __forceinline__ void k4_body(uint4* __restrict__ As, const uint4* __restrict__ xn_src,
                                        const uint4* __restrict__ wgu_t, uint2* __restrict__ act_t, int mt64,
                                        int jt0, int t, int lane DIAG_PARAM) {
  uint4 at[8];
#pragma unroll
  for (int i = 0; i < 8; ++i) at[i] = xn_src[i * 512 + t];
  const uint4* bp = wgu_t + (size_t)jt0 * 16 * 2 * 64 + lane;
  uint4 bg[16][NT > 0 ? NT : 1], bu[16][NT > 0 ? NT : 1];
  if (NT > 0) {
#pragma unroll
    for (int s = 0; s < K4_PF; ++s)
#pragma unroll
      for (int n = 0; n < NT; ++n) {
        bg[s][n] = bp[((n * 16 + s) * 2 + 0) * 64];
        bu[s][n] = bp[((n * 16 + s) * 2 + 1) * 64];
      }
  }
#pragma unroll
  for (int i = 0; i < 8; ++i) As[i * 512 + t] = at[i];
  __syncthreads();
  STAMP(3, 1);
  if (NT == 0) return;
  f32x4 ag[NT > 0 ? NT : 1][4], au[NT > 0 ? NT : 1][4];
#pragma unroll
  for (int n = 0; n < NT; ++n)
#pragma unroll
    for (int mi = 0; mi < 4; ++mi) { ag[n][mi] = (f32x4){0.f, 0.f, 0.f, 0.f}; au[n][mi] = (f32x4){0.f, 0.f, 0.f, 0.f}; }
  uint4 a[4], an[4];
#pragma unroll
  for (int mi = 0; mi < 4; ++mi) a[mi] = As[(mi * 16) * 64 + lane];
#pragma unroll
  for (int s = 0; s < 16; ++s) {
    if (s < 15) {
#pragma unroll
      for (int mi = 0; mi < 4; ++mi) an[mi] = As[(mi * 16 + s + 1) * 64 + lane];
    }
#pragma unroll
    for (int n = 0; n < NT; ++n)
#pragma unroll
      for (int mi = 0; mi < 4; ++mi) {
        ag[n][mi] = __builtin_amdgcn_mfma_f32_16x16x32_bf16(*reinterpret_cast<bf16x8*>(&bg[s][n]),
                                                            *reinterpret_cast<bf16x8*>(&a[mi]), ag[n][mi], 0, 0, 0);
        au[n][mi] = __builtin_amdgcn_mfma_f32_16x16x32_bf16(*reinterpret_cast<bf16x8*>(&bu[s][n]),
                                                            *reinterpret_cast<bf16x8*>(&a[mi]), au[n][mi], 0, 0, 0);
      }
#pragma unroll
    for (int mi = 0; mi < 4; ++mi) a[mi] = an[mi];
    if (s + K4_PF < 16) {
      __builtin_amdgcn_sched_barrier(0);
#pragma unroll
      for (int n = 0; n < NT; ++n) {
        bg[s + K4_PF][n] = bp[((n * 16 + s + K4_PF) * 2 + 0) * 64];
        bu[s + K4_PF][n] = bp[((n * 16 + s + K4_PF) * 2 + 1) * 64];
      }
    }
  }
  STAMP(3, 2);
#pragma unroll
  for (int n = 0; n < NT; ++n) {
    const int jt = jt0 + n;
#pragma unroll
    for (int mi = 0; mi < 4; ++mi) {
      const int mt16 = mt64 * 4 + mi;
      uint2 o = make_uint2(geglu2(ag[n][mi][0], ag[n][mi][1], au[n][mi][0], au[n][mi][1]),
                           geglu2(ag[n][mi][2], ag[n][mi][3], au[n][mi][2], au[n][mi][3]));
      act_t[((mt16 * KS2 + (jt >> 1)) * 64 + lane) * 2 + (jt & 1)] = o;
    }
  }
  STAMP(3, 3);
  STAMPW(3, 4);
}

__global__ __launch_bounds__(512, 2) void k4_gemm1(const uint4* __restrict__ xn_t, const uint4* __restrict__ wgu_t,
                                                   uint2* __restrict__ act_t DIAG_PARAM) {
  __shared__ __attribute__((aligned(16))) uint4 As[4 * 16 * 64];
  STAMP(3, 0);
  const int t = threadIdx.x, w = __builtin_amdgcn_readfirstlane(t >> 6), lane = t & 63;
  const int bid = blockIdx.x;
  const int xcd = bid & 7, idx = bid >> 3;
  const int mt64 = (xcd & 1) * 16 + (idx & 15);
  const int ng = (xcd >> 1) * 2 + (idx >> 4);
  const uint4* src = xn_t + (size_t)mt64 * 4 * 16 * 64;
  int start, cnt;
  if (ng < 6) { start = 11 * ng; cnt = 11; } else { start = 66 + 10 * (ng - 6); cnt = 10; }
  int jt0, nt;
  if (w < 4) { jt0 = start + 2 * w; nt = 2; }
  else { jt0 = start + 8 + (w - 4); nt = (w - 4 < cnt - 8) ? 1 : 0; }
#define K4_DBG
  if (nt == 2) k4_body<2>(As, src, wgu_t, act_t, mt64, jt0, t, lane K4_DBG);
  else if (nt == 1) k4_body<1>(As, src, wgu_t, act_t, mt64, jt0, t, lane K4_DBG);
  else k4_body<0>(As, src, wgu_t, act_t, mt64, jt0, t, lane K4_DBG);
}

#define K5_WAVES 8
#define PSTR 68
__global__ __launch_bounds__(512, 2) void k5_gemm2(const uint4* __restrict__ act_t, const uint4* __restrict__ wd_t,
                                                   const u16* __restrict__ h, u16* __restrict__ out DIAG_PARAM) {
  __shared__ __attribute__((aligned(16))) float part[K5_WAVES * 64 * PSTR];
  STAMP(4, 0);
  const int t = threadIdx.x, w = __builtin_amdgcn_readfirstlane(t >> 6), lane = t & 63;
  const int bid = blockIdx.x;
  const int xcd = bid & 7, idx = bid >> 3;
  const int mt64 = (xcd & 3) * 8 + (idx & 7);
  const int nt64 = (xcd >> 2) * 4 + (idx >> 3);
  const int ks0 = (w < 3) ? 6 * w : 18 + 5 * (w - 3);
  const int nks = (w < 3) ? 6 : 5;
  f32x4 acc[4][4];
#pragma unroll
  for (int a = 0; a < 4; ++a)
#pragma unroll
    for (int b = 0; b < 4; ++b) acc[a][b] = (f32x4){0.f, 0.f, 0.f, 0.f};
  const uint4* ap = act_t + ((size_t)(mt64 * 4) * KS2 + ks0) * 64 + lane;
  const uint4* bp = wd_t + ((size_t)(nt64 * 4) * KS2 + ks0) * 64 + lane;
  uint4 af[6][4], bf[6][4];
#pragma unroll
  for (int s = 0; s < 6; ++s) {
    if (s < nks) {
#pragma unroll
      for (int i = 0; i < 4; ++i) {
        af[s][i] = ap[(i * KS2 + s) * 64];
        bf[s][i] = bp[(i * KS2 + s) * 64];
      }
    }
  }
  const int er = t >> 3, ec = (t & 7) * 8;
  const int pos = mt64 * 64 + er;
  const uint4 hv = *reinterpret_cast<const uint4*>(h + (size_t)pos * D_DIM + nt64 * 64 + ec);
#pragma unroll
  for (int s = 0; s < 6; ++s) {
    if (s < nks) {
#pragma unroll
      for (int mi = 0; mi < 4; ++mi)
#pragma unroll
        for (int ni = 0; ni < 4; ++ni)
          acc[mi][ni] = __builtin_amdgcn_mfma_f32_16x16x32_bf16(*reinterpret_cast<bf16x8*>(&bf[s][ni]),
                                                                *reinterpret_cast<bf16x8*>(&af[s][mi]), acc[mi][ni], 0, 0, 0);
    }
  }
  STAMP(4, 1);
  float* pw = part + w * 64 * PSTR;
#pragma unroll
  for (int mi = 0; mi < 4; ++mi)
#pragma unroll
    for (int ni = 0; ni < 4; ++ni)
      *reinterpret_cast<f32x4*>(pw + (16 * mi + (lane & 15)) * PSTR + 16 * ni + 4 * (lane >> 4)) = acc[mi][ni];
  __syncthreads();
  STAMP(4, 2);
  f32x4 s0 = {0.f, 0.f, 0.f, 0.f}, s1 = {0.f, 0.f, 0.f, 0.f};
#pragma unroll
  for (int ww = 0; ww < K5_WAVES; ++ww) {
    s0 += *reinterpret_cast<const f32x4*>(part + ww * 64 * PSTR + er * PSTR + ec);
    s1 += *reinterpret_cast<const f32x4*>(part + ww * 64 * PSTR + er * PSTR + ec + 4);
  }
  const unsigned hw[4] = {hv.x, hv.y, hv.z, hv.w};
  unsigned ow[4];
#pragma unroll
  for (int i = 0; i < 4; ++i) {
    const float d0 = bfr(i < 2 ? s0[2 * i] : s1[2 * i - 4]);
    const float d1 = bfr(i < 2 ? s0[2 * i + 1] : s1[2 * i - 3]);
    const float h0 = __uint_as_float(hw[i] << 16), h1 = __uint_as_float(hw[i] & 0xffff0000u);
    ow[i] = (unsigned)f2bf(h0 + d0) | ((unsigned)f2bf(h1 + d1) << 16);
  }
  *reinterpret_cast<uint4*>(out + (size_t)row_of_pos(pos) * D_DIM + nt64 * 64 + ec) = make_uint4(ow[0], ow[1], ow[2], ow[3]);
  STAMP(4, 3);
  STAMPW(4, 4);
}

extern "C" void kernel_launch(void* const* d_in, const int* in_sizes, int n_in, void* d_out, int out_size,
                              void* d_ws, size_t ws_size, hipStream_t stream) {
  const u16* x = (const u16*)d_in[0];
  const u16* wr = (const u16*)d_in[1];
  const u16* wi = (const u16*)d_in[2];
  const u16* wg = (const u16*)d_in[4];
  const u16* wu = (const u16*)d_in[5];
  const u16* wd = (const u16*)d_in[6];
  u16* out = (u16*)d_out;
  char* ws = (char*)d_ws;
  size_t off = 0;
  float2* spec = (float2*)(ws + off); off += (size_t)256 * S_LEN * 8;
  unsigned* wt = (unsigned*)(ws + off); off += (size_t)260 * S_LEN * 4;
  u16* h = (u16*)(ws + off); off += (size_t)S_LEN * D_DIM * 2;
  u16* xn_t = (u16*)(ws + off); off += (size_t)S_LEN * D_DIM * 2;
  uint4* wgu_t = (uint4*)(ws + off); off += (size_t)NT16 * 16 * 2 * 1024;
  uint4* wd_t = (uint4*)(ws + off); off += (size_t)32 * KS2 * 1024;
  uint4* act_t = (uint4*)(ws + off); off += (size_t)128 * KS2 * 1024;

  k1_ln_rfft<<<512 + SIDE_W, 256, 0, stream>>>(x, wr, wi, spec, wt, wg, wu, wd, wgu_t, wd_t DIAG_ARG);
  k2_colfft<<<256, 256, 0, stream>>>(spec, wt DIAG_ARG);
  k3_irfft_ln<<<256, 256, 0, stream>>>(spec, x, h, xn_t DIAG_ARG);
  k4_gemm1<<<256, 512, 0, stream>>>((const uint4*)xn_t, wgu_t, (uint2*)act_t DIAG_ARG);
  k5_gemm2<<<256, 512, 0, stream>>>(act_t, wd_t, h, out DIAG_ARG);
}
```

```cpp
#include <hip/hip_runtime.h>
#include <hip/hip_bf16.h>
#include <stdint.h>

typedef __attribute__((ext_vector_type(8))) short bf16x8;
typedef __attribute__((ext_vector_type(4))) float f32x4;
typedef unsigned short u16;

#define S_LEN 2048
#define D_DIM 512
#define F_DIM 1376
#define DF 257
#define NT16 86
#define KS2 43

__device__ const float2 g_twl[14 * 64] = {
{0x1.0000000000000p+0f,-0x0.0p+0f}, {0x1.fd88da0000000p-1f,-0x1.917a6c0000000p-4f}, {0x1.f6297c0000000p-1f,-0x1.8f8b840000000p-3f}, {0x1.e9f4160000000p-1f,-0x1.2940620000000p-2f},
{0x1.d906bc0000000p-1f,-0x1.87de2a0000000p-2f}, {0x1.c38b300000000p-1f,-0x1.e2b5d40000000p-2f}, {0x1.a9b6620000000p-1f,-0x1.1c73b40000000p-1f}, {0x1.8bc8060000000p-1f,-0x1.44cf320000000p-1f},
{0x1.0000000000000p+0f,-0x0.0p+0f}, {0x1.fd88da0000000p-1f,-0x1.917a6c0000000p-4f}, {0x1.f6297c0000000p-1f,-0x1.8f8b840000000p-3f}, {0x1.e9f4160000000p-1f,-0x1.2940620000000p-2f},
{0x1.d906bc0000000p-1f,-0x1.87de2a0000000p-2f}, {0x1.c38b300000000p-1f,-0x1.e2b5d40000000p-2f}, {0x1.a9b6620000000p-1f,-0x1.1c73b40000000p-1f}, {0x1.8bc8060000000p-1f,-0x1.44cf320000000p-1f},
{0x1.0000000000000p+0f,-0x0.0p+0f}, {0x1.fd88da0000000p-1f,-0x1.917a6c0000000p-4f}, {0x1.f6297c0000000p-1f,-0x1.8f8b840000000p-3f}, {0x1.e9f4160000000p-1f,-0x1.2940620000000p-2f},
{0x1.d906bc0000000p-1f,-0x1.87de2a0000000p-2f}, {0x1.c38b300000000p-1f,-0x1.e2b5d40000000p-2f}, {0x1.a9b6620000000p-1f,-0x1.1c73b40000000p-1f}, {0x1.8bc8060000000p-1f,-0x1.44cf320000000p-1f},
{0x1.0000000000000p+0f,-0x0.0p+0f}, {0x1.fd88da0000000p-1f,-0x1.917a6c0000000p-4f}, {0x1.f6297c0000000p-1f,-0x1.8f8b840000000p-3f}, {0x1.e9f4160000000p-1f,-0x1.2940620000000p-2f},
{0x1.d906bc0000000p-1f,-0x1.87de2a0000000p-2f}, {0x1.c38b300000000p-1f,-0x1.e2b5d40000000p-2f}, {0x1.a9b6620000000p-1f,-0x1.1c73b40000000p-1f}, {0x1.8bc8060000000p-1f,-0x1.44cf320000000p-1f},
{0x1.0000000000000p+0f,-0x0.0p+0f}, {0x1.fd88da0000000p-1f,-0x1.917a6c0000000p-4f}, {0x1.f6297c0000000p-1f,-0x1.8f8b840000000p-3f}, {0x1.e9f4160000000p-1f,-0x1.2940620000000p-2f},
{0x1.d906bc0000000p-1f,-0x1.87de2a0000000p-2f}, {0x1.c38b300000000p-1f,-0x1.e2b5d40000000p-2f}, {0x1.a9b6620000000p-1f,-0x1.1c73b40000000p-1f}, {0x1.8bc8060000000p-1f,-0x1.44cf320000000p-1f},
{0x1.0000000000000p+0f,-0x0.0p+0f}, {0x1.fd88da0000000p-1f,-0x1.917a6c0000000p-4f}, {0x1.f6297c0000000p-1f,-0x1.8f8b840000000p-3f}, {0x1.e9f4160000000p-1f,-0x1.2940620000000p-2f},
{0x1.d906bc0000000p-1f,-0x1.87de2a0000000p-2f}, {0x1.c38b300000000p-1f,-0x1.e2b5d40000000p-2f}, {0x1.a9b6620000000p-1f,-0x1.1c73b40000000p-1f}, {0x1.8bc8060000000p-1f,-0x1.44cf320000000p-1f},
{0x1.0000000000000p+0f,-0x0.0p+0f}, {0x1.fd88da0000000p-1f,-0x1.917a6c0000000p-4f}, {0x1.f6297c0000000p-1f,-0x1.8f8b840000000p-3f}, {0x1.e9f4160000000p-1f,-0x1.2940620000000p-2f},
{0x1.d906bc0000000p-1f,-0x1.87de2a0000000p-2f}, {0x1.c38b300000000p-1f,-0x1.e2b5d40000000p-2f}, {0x1.a9b6620000000p-1f,-0x1.1c73b40000000p-1f}, {0x1.8bc8060000000p-1f,-0x1.44cf320000000p-1f},
{0x1.0000000000000p+0f,-0x0.0p+0f}, {0x1.fd88da0000000p-1f,-0x1.917a6c0000000p-4f}, {0x1.f6297c0000000p-1f,-0x1.8f8b840000000p-3f}, {0x1.e9f4160000000p-1f,-0x1.2940620000000p-2f},
{0x1.d906bc0000000p-1f,-0x1.87de2a0000000p-2f}, {0x1.c38b300000000p-1f,-0x1.e2b5d40000000p-2f}, {0x1.a9b6620000000p-1f,-0x1.1c73b40000000p-1f}, {0x1.8bc8060000000p-1f,-0x1.44cf320000000p-1f},
{0x1.0000000000000p+0f,-0x0.0p+0f}, {0x1.f6297c0000000p-1f,-0x1.8f8b840000000p-3f}, {0x1.d906bc0000000p-1f,-0x1.87de2a0000000p-2f}, {0x1.a9b6620000000p-1f,-0x1.1c73b40000000p-1f},
{0x1.6a09e60000000p-1f,-0x1.6a09e60000000p-1f}, {0x1.1c73b40000000p-1f,-0x1.a9b6620000000p-1f}, {0x1.87de2a0000000p-2f,-0x1.d906bc0000000p-1f}, {0x1.8f8b840000000p-3f,-0x1.f6297c0000000p-1f},
{0x1.0000000000000p+0f,-0x0.0p+0f}, {0x1.f6297c0000000p-1f,-0x1.8f8b840000000p-3f}, {0x1.d906bc0000000p-1f,-0x1.87de2a0000000p-2f}, {0x1.a9b6620000000p-1f,-0x1.1c73b40000000p-1f},
{0x1.6a09e60000000p-1f,-0x1.6a09e60000000p-1f}, {0x1.1c73b40000000p-1f,-0x1.a9b6620000000p-1f}, {0x1.87de2a0000000p-2f,-0x1.d906bc0000000p-1f}, {0x1.8f8b840000000p-3f,-0x1.f6297c0000000p-1f},
{0x1.0000000000000p+0f,-0x0.0p+0f}, {0x1.f6297c0000000p-1f,-0x1.8f8b840000000p-3f}, {0x1.d906bc0000000p-1f,-0x1.87de2a0000000p-2f}, {0x1.a9b6620000000p-1f,-0x1.1c73b40000000p-1f},
{0x1.6a09e60000000p-1f,-0x1.6a09e60000000p-1f}, {0x1.1c73b40000000p-1f,-0x1.a9b6620000000p-1f}, {0x1.87de2a0000000p-2f,-0x1.d906bc0000000p-1f}, {0x1.8f8b840000000p-3f,-0x1.f6297c0000000p-1f},
{0x1.0000000000000p+0f,-0x0.0p+0f}, {0x1.f6297c0000000p-1f,-0x1.8f8b840000000p-3f}, {0x1.d906bc0000000p-1f,-0x1.87de2a0000000p-2f}, {0x1.a9b6620000000p-1f,-0x1.1c73b40000000p-1f},
{0x1.6a09e60000000p-1f,-0x1.6a09e60000000p-1f}, {0x1.1c73b40000000p-1f,-0x1.a9b6620000000p-1f}, {0x1.87de2a0000000p-2f,-0x1.d906bc0000000p-1f}, {0x1.8f8b840000000p-3f,-0x1.f6297c0000000p-1f},
{0x1.0000000000000p+0f,-0x0.0p+0f}, {0x1.f6297c0000000p-1f,-0x1.8f8b840000000p-3f}, {0x1.d906bc0000000p-1f,-0x1.87de2a0000000p-2f}, {0x1.a9b6620000000p-1f,-0x1.1c73b40000000p-1f},
{0x1.6a09e60000000p-1f,-0x1.6a09e60000000p-1f}, {0x1.1c73b40000000p-1f,-0x1.a9b6620000000p-1f}, {0x1.87de2a0000000p-2f,-0x1.d906bc0000000p-1f}, {0x1.8f8b840000000p-3f,-0x1.f6297c0000000p-1f},
{0x1.0000000000000p+0f,-0x0.0p+0f}, {0x1.f6297c0000000p-1f,-0x1.8f8b840000000p-3f}, {0x1.d906bc0000000p-1f,-0x1.87de2a0000000p-2f}, {0x1.a9b6620000000p-1f,-0x1.1c73b40000000p-1f},
{0x1.6a09e60000000p-1f,-0x1.6a09e60000000p-1f}, {0x1.1c73b40000000p-1f,-0x1.a9b6620000000p-1f}, {0x1.87de2a0000000p-2f,-0x1.d906bc0000000p-1f}, {0x1.8f8b840000000p-3f,-0x1.f6297c0000000p-1f},
{0x1.0000000000000p+0f,-0x0.0p+0f}, {0x1.f6297c0000000p-1f,-0x1.8f8b840000000p-3f}, {0x1.d906bc0000000p-1f,-0x1.87de2a0000000p-2f}, {0x1.a9b6620000000p-1f,-0x1.1c73b40000000p-1f},
{0x1.6a09e60000000p-1f,-0x1.6a09e60000000p-1f}, {0x1.1c73b40000000p-1f,-0x1.a9b6620000000p-1f}, {0x1.87de2a0000000p-2f,-0x1.d906bc0000000p-1f}, {0x1.8f8b840000000p-3f,-0x1.f6297c0000000p-1f},
{0x1.0000000000000p+0f,-0x0.0p+0f}, {0x1.f6297c0000000p-1f,-0x1.8f8b840000000p-3f}, {0x1.d906bc0000000p-1f,-0x1.87de2a0000000p-2f}, {0x1.a9b6620000000p-1f,-0x1.1c73b40000000p-1f},
{0x1.6a09e60000000p-1f,-0x1.6a09e60000000p-1f}, {0x1.1c73b40000000p-1f,-0x1.a9b6620000000p-1f}, {0x1.87de2a0000000p-2f,-0x1.d906bc0000000p-1f}, {0x1.8f8b840000000p-3f,-0x1.f6297c0000000p-1f},
{0x1.0000000000000p+0f,-0x0.0p+0f}, {0x1.e9f4160000000p-1f,-0x1.2940620000000p-2f}, {0x1.a9b6620000000p-1f,-0x1.1c73b40000000p-1f}, {0x1.44cf320000000p-1f,-0x1.8bc8060000000p-1f},
{0x1.87de2a0000000p-2f,-0x1.d906bc0000000p-1f}, {0x1.917a6c0000000p-4f,-0x1.fd88da0000000p-1f}, {-0x1.8f8b840000000p-3f,-0x1.f6297c0000000p-1f}, {-0x1.e2b5d40000000p-2f,-0x1.c38b300000000p-1f},
{0x1.0000000000000p+0f,-0x0.0p+0f}, {0x1.e9f4160000000p-1f,-0x1.2940620000000p-2f}, {0x1.a9b6620000000p-1f,-0x1.1c73b40000000p-1f}, {0x1.44cf320000000p-1f,-0x1.8bc8060000000p-1f},
{0x1.87de2a0000000p-2f,-0x1.d906bc0000000p-1f}, {0x1.917a6c0000000p-4f,-0x1.fd88da0000000p-1f}, {-0x1.8f8b840000000p-3f,-0x1.f6297c0000000p-1f}, {-0x1.e2b5d40000000p-2f,-0x1.c38b300000000p-1f},
{0x1.0000000000000p+0f,-0x0.0p+0f}, {0x1.e9f4160000000p-1f,-0x1.2940620000000p-2f}, {0x1.a9b6620000000p-1f,-0x1.1c73b40000000p-1f}, {0x1.44cf320000000p-1f,-0x1.8bc8060000000p-1f},
{0x1.87de2a0000000p-2f,-0x1.d906bc0000000p-1f}, {0x1.917a6c0000000p-4f,-0x1.fd88da0000000p-1f}, {-0x1.8f8b840000000p-3f,-0x1.f6297c0000000p-1f}, {-0x1.e2b5d40000000p-2f,-0x1.c38b300000000p-1f},
{0x1.0000000000000p+0f,-0x0.0p+0f}, {0x1.e9f4160000000p-1f,-0x1.2940620000000p-2f}, {0x1.a9b6620000000p-1f,-0x1.1c73b40000000p-1f}, {0x1.44cf320000000p-1f,-0x1.8bc8060000000p-1f},
{0x1.87de2a0000000p-2f,-0x1.d906bc0000000p-1f}, {0x1.917a6c0000000p-4f,-0x1.fd88da0000000p-1f}, {-0x1.8f8b840000000p-3f,-0x1.f6297c0000000p-1f}, {-0x1.e2b5d40000000p-2f,-0x1.c38b300000000p-1f},
{0x1.0000000000000p+0f,-0x0.0p+0f}, {0x1.e9f4160000000p-1f,-0x1.2940620000000p-2f}, {0x1.a9b6620000000p-1f,-0x1.1c73b40000000p-1f}, {0x1.44cf320000000p-1f,-0x1.8bc8060000000p-1f},
{0x1.87de2a0000000p-2f,-0x1.d906bc0000000p-1f}, {0x1.917a6c0000000p-4f,-0x1.fd88da0000000p-1f}, {-0x1.8f8b840000000p-3f,-0x1.f6297c0000000p-1f}, {-0x1.e2b5d40000000p-2f,-0x1.c38b300000000p-1f},
{0x1.0000000000000p+0f,-0x0.0p+0f}, {0x1.e9f4160000000p-1f,-0x1.2940620000000p-2f}, {0x1.a9b6620000000p-1f,-0x1.1c73b40000000p-1f}, {0x1.44cf320000000p-1f,-0x1.8bc8060000000p-1f},
{0x1.87de2a0000000p-2f,-0x1.d906bc0000000p-1f}, {0x1.917a6c0000000p-4f,-0x1.fd88da0000000p-1f}, {-0x1.8f8b840000000p-3f,-0x1.f6297c0000000p-1f}, {-0x1.e2b5d40000000p-2f,-0x1.c38b300000000p-1f},
{0x1.0000000000000p+0f,-0x0.0p+0f}, {0x1.e9f4160000000p-1f,-0x1.2940620000000p-2f}, {0x1.a9b6620000000p-1f,-0x1.1c73b40000000p-1f}, {0x1.44cf320000000p-1f,-0x1.8bc8060000000p-1f},
{0x1.87de2a0000000p-2f,-0x1.d906bc0000000p-1f}, {0x1.917a6c0000000p-4f,-0x1.fd88da0000000p-1f}, {-0x1.8f8b840000000p-3f,-0x1.f6297c0000000p-1f}, {-0x1.e2b5d40000000p-2f,-0x1.c38b300000000p-1f},
{0x1.0000000000000p+0f,-0x0.0p+0f}, {0x1.e9f4160000000p-1f,-0x1.2940620000000p-2f}, {0x1.a9b6620000000p-1f,-0x1.1c73b40000000p-1f}, {0x1.44cf320000000p-1f,-0x1.8bc8060000000p-1f},
{0x1.87de2a0000000p-2f,-0x1.d906bc0000000p-1f}, {0x1.917a6c0000000p-4f,-0x1.fd88da0000000p-1f}, {-0x1.8f8b840000000p-3f,-0x1.f6297c0000000p-1f}, {-0x1.e2b5d40000000p-2f,-0x1.c38b300000000p-1f},
{0x1.0000000000000p+0f,-0x0.0p+0f}, {0x1.d906bc0000000p-1f,-0x1.87de2a0000000p-2f}, {0x1.6a09e60000000p-1f,-0x1.6a09e60000000p-1f}, {0x1.87de2a0000000p-2f,-0x1.d906bc0000000p-1f},
{0x1.1a62640000000p-54f,-0x1.0000000000000p+0f}, {-0x1.87de2a0000000p-2f,-0x1.d906bc0000000p-1f}, {-0x1.6a09e60000000p-1f,-0x1.6a09e60000000p-1f}, {-0x1.d906bc0000000p-1f,-0x1.87de2a0000000p-2f},
{0x1.0000000000000p+0f,-0x0.0p+0f}, {0x1.d906bc0000000p-1f,-0x1.87de2a0000000p-2f}, {0x1.6a09e60000000p-1f,-0x1.6a09e60000000p-1f}, {0x1.87de2a0000000p-2f,-0x1.d906bc0000000p-1f},
{0x1.1a62640000000p-54f,-0x1.0000000000000p+0f}, {-0x1.87de2a0000000p-2f,-0x1.d906bc0000000p-1f}, {-0x1.6a09e60000000p-1f,-0x1.6a09e60000000p-1f}, {-0x1.d906bc0000000p-1f,-0x1.87de2a0000000p-2f},
{0x1.0000000000000p+0f,-0x0.0p+0f}, {0x1.d906bc0000000p-1f,-0x1.87de2a0000000p-2f}, {0x1.6a09e60000000p-1f,-0x1.6a09e60000000p-1f}, {0x1.87de2a0000000p-2f,-0x1.d906bc0000000p-1f},
{0x1.1a62640000000p-54f,-0x1.0000000000000p+0f}, {-0x1.87de2a0000000p-2f,-0x1.d906bc0000000p-1f}, {-0x1.6a09e60000000p-1f,-0x1.6a09e60000000p-1f}, {-0x1.d906bc0000000p-1f,-0x1.87de2a0000000p-2f},
{0x1.0000000000000p+0f,-0x0.0p+0f}, {0x1.d906bc0000000p-1f,-0x1.87de2a0000000p-2f}, {0x1.6a09e60000000p-1f,-0x1.6a09e60000000p-1f}, {0x1.87de2a0000000p-2f,-0x1.d906bc0000000p-1f},
{0x1.1a62640000000p-54f,-0x1.0000000000000p+0f}, {-0x1.87de2a0000000p-2f,-0x1.d906bc0000000p-1f}, {-0x1.6a09e60000000p-1f,-0x1.6a09e60000000p-1f}, {-0x1.d906bc0000000p-1f,-0x1.87de2a0000000p-2f},
{0x1.0000000000000p+0f,-0x0.0p+0f}, {0x1.d906bc0000000p-1f,-0x1.87de2a0000000p-2f}, {0x1.6a09e60000000p-1f,-0x1.6a09e60000000p-1f}, {0x1.87de2a0000000p-2f,-0x1.d906bc0000000p-1f},
{0x1.1a62640000000p-54f,-0x1.0000000000000p+0f}, {-0x1.87de2a0000000p-2f,-0x1.d906bc0000000p-1f}, {-0x1.6a09e60000000p-1f,-0x1.6a09e60000000p-1f}, {-0x1.d906bc0000000p-1f,-0x1.87de2a0000000p-2f},
{0x1.0000000000000p+0f,-0x0.0p+0f}, {0x1.d906bc0000000p-1f,-0x1.87de2a0000000p-2f}, {0x1.6a09e60000000p-1f,-0x1.6a09e60000000p-1f}, {0x1.87de2a0000000p-2f,-0x1.d906bc0000000p-1f},
{0x1.1a62640000000p-54f,-0x1.0000000000000p+0f}, {-0x1.87de2a0000000p-2f,-0x1.d906bc0000000p-1f}, {-0x1.6a09e60000000p-1f,-0x1.6a09e60000000p-1f}, {-0x1.d906bc0000000p-1f,-0x1.87de2a0000000p-2f},
{0x1.0000000000000p+0f,-0x0.0p+0f}, {0x1.d906bc0000000p-1f,-0x1.87de2a0000000p-2f}, {0x1.6a09e60000000p-1f,-0x1.6a09e60000000p-1f}, {0x1.87de2a0000000p-2f,-0x1.d906bc0000000p-1f},
{0x1.1a62640000000p-54f,-0x1.0000000000000p+0f}, {-0x1.87de2a0000000p-2f,-0x1.d906bc0000000p-1f}, {-0x1.6a09e60000000p-1f,-0x1.6a09e60000000p-1f}, {-0x1.d906bc0000000p-1f,-0x1.87de2a0000000p-2f},
{0x1.0000000000000p+0f,-0x0.0p+0f}, {0x1.d906bc0000000p-1f,-0x1.87de2a0000000p-2f}, {0x1.6a09e60000000p-1f,-0x1.6a09e60000000p-1f}, {0x1.87de2a0000000p-2f,-0x1.d906bc0000000p-1f},
{0x1.1a62640000000p-54f,-0x1.0000000000000p+0f}, {-0x1.87de2a0000000p-2f,-0x1.d906bc0000000p-1f}, {-0x1.6a09e60000000p-1f,-0x1.6a09e60000000p-1f}, {-0x1.d906bc0000000p-1f,-0x1.87de2a0000000p-2f},
{0x1.0000000000000p+0f,-0x0.0p+0f}, {0x1.c38b300000000p-1f,-0x1.e2b5d40000000p-2f}, {0x1.1c73b40000000p-1f,-0x1.a9b6620000000p-1f}, {0x1.917a6c0000000p-4f,-0x1.fd88da0000000p-1f},
{-0x1.87de2a0000000p-2f,-0x1.d906bc0000000p-1f}, {-0x1.8bc8060000000p-1f,-0x1.44cf320000000p-1f}, {-0x1.f6297c0000000p-1f,-0x1.8f8b840000000p-3f}, {-0x1.e9f4160000000p-1f,0x1.2940620000000p-2f},
{0x1.0000000000000p+0f,-0x0.0p+0f}, {0x1.c38b300000000p-1f,-0x1.e2b5d40000000p-2f}, {0x1.1c73b40000000p-1f,-0x1.a9b6620000000p-1f}, {0x1.917a6c0000000p-4f,-0x1.fd88da0000000p-1f},
{-0x1.87de2a0000000p-2f,-0x1.d906bc0000000p-1f}, {-0x1.8bc8060000000p-1f,-0x1.44cf320000000p-1f}, {-0x1.f6297c0000000p-1f,-0x1.8f8b840000000p-3f}, {-0x1.e9f4160000000p-1f,0x1.2940620000000p-2f},
{0x1.0000000000000p+0f,-0x0.0p+0f}, {0x1.c38b300000000p-1f,-0x1.e2b5d40000000p-2f}, {0x1.1c73b40000000p-1f,-0x1.a9b6620000000p-1f}, {0x1.917a6c0000000p-4f,-0x1.fd88da0000000p-1f},
{-0x1.87de2a0000000p-2f,-0x1.d906bc0000000p-1f}, {-0x1.8bc8060000000p-1f,-0x1.44cf320000000p-1f}, {-0x1.f6297c0000000p-1f,-0x1.8f8b840000000p-3f}, {-0x1.e9f4160000000p-1f,0x1.2940620000000p-2f},
{0x1.0000000000000p+0f,-0x0.0p+0f}, {0x1.c38b300000000p-1f,-0x1.e2b5d40000000p-2f}, {0x1.1c73b40000000p-1f,-0x1.a9b6620000000p-1f}, {0x1.917a6c0000000p-4f,-0x1.fd88da0000000p-1f},
{-0x1.87de2a0000000p-2f,-0x1.d906bc0000000p-1f}, {-0x1.8bc8060000000p-1f,-0x1.44cf320000000p-1f}, {-0x1.f6297c0000000p-1f,-0x1.8f8b840000000p-3f}, {-0x1.e9f4160000000p-1f,0x1.2940620000000p-2f},
{0x1.0000000000000p+0f,-0x0.0p+0f}, {0x1.c38b300000000p-1f,-0x1.e2b5d40000000p-2f}, {0x1.1c73b40000000p-1f,-0x1.a9b6620000000p-1f}, {0x1.917a6c0000000p-4f,-0x1.fd88da0000000p-1f},
{-0x1.87de2a0000000p-2f,-0x1.d906bc0000000p-1f}, {-0x1.8bc8060000000p-1f,-0x1.44cf320000000p-1f}, {-0x1.f6297c0000000p-1f,-0x1.8f8b840000000p-3f}, {-0x1.e9f4160000000p-1f,0x1.2940620000000p-2f},
{0x1.0000000000000p+0f,-0x0.0p+0f}, {0x1.c38b300000000p-1f,-0x1.e2b5d40000000p-2f}, {0x1.1c73b40000000p-1f,-0x1.a9b6620000000p-1f}, {0x1.917a6c0000000p-4f,-0x1.fd88da0000000p-1f},
{-0x1.87de2a0000000p-2f,-0x1.d906bc0000000p-1f}, {-0x1.8bc8060000000p-1f,-0x1.44cf320000000p-1f}, {-0x1.f6297c0000000p-1f,-0x1.8f8b840000000p-3f}, {-0x1.e9f4160000000p-1f,0x1.2940620000000p-2f},
{0x1.0000000000000p+0f,-0x0.0p+0f}, {0x1.c38b300000000p-1f,-0x1.e2b5d40000000p-2f}, {0x1.1c73b40000000p-1f,-0x1.a9b6620000000p-1f}, {0x1.917a6c0000000p-4f,-0x1.fd88da0000000p-1f},
{-0x1.87de2a0000000p-2f,-0x1.d906bc0000000p-1f}, {-0x1.8bc8060000000p-1f,-0x1.44cf320000000p-1f}, {-0x1.f6297c0000000p-1f,-0x1.8f8b840000000p-3f}, {-0x1.e9f4160000000p-1f,0x1.2940620000000p-2f},
{0x1.0000000000000p+0f,-0x0.0p+0f}, {0x1.c38b300000000p-1f,-0x1.e2b5d40000000p-2f}, {0x1.1c73b40000000p-1f,-0x1.a9b6620000000p-1f}, {0x1.917a6c0000000p-4f,-0x1.fd88da0000000p-1f},
{-0x1.87de2a0000000p-2f,-0x1.d906bc0000000p-1f}, {-0x1.8bc8060000000p-1f,-0x1.44cf320000000p-1f}, {-0x1.f6297c0000000p-1f,-0x1.8f8b840000000p-3f}, {-0x1.e9f4160000000p-1f,0x1.2940620000000p-2f},
{0x1.0000000000000p+0f,-0x0.0p+0f}, {0x1.a9b6620000000p-1f,-0x1.1c73b40000000p-1f}, {0x1.87de2a0000000p-2f,-0x1.d906bc0000000p-1f}, {-0x1.8f8b840000000p-3f,-0x1.f6297c0000000p-1f},
{-0x1.6a09e60000000p-1f,-0x1.6a09e60000000p-1f}, {-0x1.f6297c0000000p-1f,-0x1.8f8b840000000p-3f}, {-0x1.d906bc0000000p-1f,0x1.87de2a0000000p-2f}, {-0x1.1c73b40000000p-1f,0x1.a9b6620000000p-1f},
{0x1.0000000000000p+0f,-0x0.0p+0f}, {0x1.a9b6620000000p-1f,-0x1.1c73b40000000p-1f}, {0x1.87de2a0000000p-2f,-0x1.d906bc0000000p-1f}, {-0x1.8f8b840000000p-3f,-0x1.f6297c0000000p-1f},
{-0x1.6a09e60000000p-1f,-0x1.6a09e60000000p-1f}, {-0x1.f6297c0000000p-1f,-0x1.8f8b840000000p-3f}, {-0x1.d906bc0000000p-1f,0x1.87de2a0000000p-2f}, {-0x1.1c73b40000000p-1f,0x1.a9b6620000000p-1f},
{0x1.0000000000000p+0f,-0x0.0p+0f}, {0x1.a9b6620000000p-1f,-0x1.1c73b40000000p-1f}, {0x1.87de2a0000000p-2f,-0x1.d906bc0000000p-1f}, {-0x1.8f8b840000000p-3f,-0x1.f6297c0000000p-1f},
{-0x1.6a09e60000000p-1f,-0x1.6a09e60000000p-1f}, {-0x1.f6297c0000000p-1f,-0x1.8f8b840000000p-3f}, {-0x1.d906bc0000000p-1f,0x1.87de2a0000000p-2f}, {-0x1.1c73b40000000p-1f,0x1.a9b6620000000p-1f},
{0x1.0000000000000p+0f,-0x0.0p+0f}, {0x1.a9b6620000000p-1f,-0x1.1c73b40000000p-1f}, {0x1.87de2a0000000p-2f,-0x1.d906bc0000000p-1f}, {-0x1.8f8b840000000p-3f,-0x1.f6297c0000000p-1f},
{-0x1.6a09e60000000p-1f,-0x1.6a09e60000000p-1f}, {-0x1.f6297c0000000p-1f,-0x1.8f8b840000000p-3f}, {-0x1.d906bc0000000p-1f,0x1.87de2a0000000p-2f}, {-0x1.1c73b40000000p-1f,0x1.a9b6620000000p-1f},
{0x1.0000000000000p+0f,-0x0.0p+0f}, {0x1.a9b6620000000p-1f,-0x1.1c73b40000000p-1f}, {0x1.87de2a0000000p-2f,-0x1.d906bc0000000p-1f}, {-0x1.8f8b840000000p-3f,-0x1.f6297c0000000p-1f},
{-0x1.6a09e60000000p-1f,-0x1.6a09e60000000p-1f}, {-0x1.f6297c0000000p-1f,-0x1.8f8b840000000p-3f}, {-0x1.d906bc0000000p-1f,0x1.87de2a0000000p-2f}, {-0x1.1c73b40000000p-1f,0x1.a9b6620000000p-1f},
{0x1.0000000000000p+0f,-0x0.0p+0f}, {0x1.a9b6620000000p-1f,-0x1.1c73b40000000p-1f}, {0x1.87de2a0000000p-2f,-0x1.d906bc0000000p-1f}, {-0x1.8f8b840000000p-3f,-0x1.f6297c0000000p-1f},
{-0x1.6a09e60000000p-1f,-0x1.6a09e60000000p-1f}, {-0x1.f6297c0000000p-1f,-0x1.8f8b840000000p-3f}, {-0x1.d906bc0000000p-1f,0x1.87de2a0000000p-2f}, {-0x1.1c73b40000000p-1f,0x1.a9b6620000000p-1f},
{0x1.0000000000000p+0f,-0x0.0p+0f}, {0x1.a9b6620000000p-1f,-0x1.1c73b40000000p-1f}, {0x1.87de2a0000000p-2f,-0x1.d906bc0000000p-1f}, {-0x1.8f8b840000000p-3f,-0x1.f6297c0000000p-1f},
{-0x1.6a09e60000000p-1f,-0x1.6a09e60000000p-1f}, {-0x1.f6297c0000000p-1f,-0x1.8f8b840000000p-3f}, {-0x1.d906bc0000000p-1f,0x1.87de2a0000000p-2f}, {-0x1.1c73b40000000p-1f,0x1.a9b6620000000p-1f},
{0x1.0000000000000p+0f,-0x0.0p+0f}, {0x1.a9b6620000000p-1f,-0x1.1c73b40000000p-1f}, {0x1.87de2a0000000p-2f,-0x1.d906bc0000000p-1f}, {-0x1.8f8b840000000p-3f,-0x1.f6297c0000000p-1f},
{-0x1.6a09e60000000p-1f,-0x1.6a09e60000000p-1f}, {-0x1.f6297c0000000p-1f,-0x1.8f8b840000000p-3f}, {-0x1.d906bc0000000p-1f,0x1.87de2a0000000p-2f}, {-0x1.1c73b40000000p-1f,0x1.a9b6620000000p-1f},
{0x1.0000000000000p+0f,-0x0.0p+0f}, {0x1.8bc8060000000p-1f,-0x1.44cf320000000p-1f}, {0x1.8f8b840000000p-3f,-0x1.f6297c0000000p-1f}, {-0x1.e2b5d40000000p-2f,-0x1.c38b300000000p-1f},
{-0x1.d906bc0000000p-1f,-0x1.87de2a0000000p-2f}, {-0x1.e9f4160000000p-1f,0x1.2940620000000p-2f}, {-0x1.1c73b40000000p-1f,0x1.a9b6620000000p-1f}, {0x1.917a6c0000000p-4f,0x1.fd88da0000000p-1f},
{0x1.0000000000000p+0f,-0x0.0p+0f}, {0x1.8bc8060000000p-1f,-0x1.44cf320000000p-1f}, {0x1.8f8b840000000p-3f,-0x1.f6297c0000000p-1f}, {-0x1.e2b5d40000000p-2f,-0x1.c38b300000000p-1f},
{-0x1.d906bc0000000p-1f,-0x1.87de2a0000000p-2f}, {-0x1.e9f4160000000p-1f,0x1.2940620000000p-2f}, {-0x1.1c73b40000000p-1f,0x1.a9b6620000000p-1f}, {0x1.917a6c0000000p-4f,0x1.fd88da0000000p-1f},
{0x1.0000000000000p+0f,-0x0.0p+0f}, {0x1.8bc8060000000p-1f,-0x1.44cf320000000p-1f}, {0x1.8f8b840000000p-3f,-0x1.f6297c0000000p-1f}, {-0x1.e2b5d40000000p-2f,-0x1.c38b300000000p-1f},
{-0x1.d906bc0000000p-1f,-0x1.87de2a0000000p-2f}, {-0x1.e9f4160000000p-1f,0x1.2940620000000p-2f}, {-0x1.1c73b40000000p-1f,0x1.a9b6620000000p-1f}, {0x1.917a6c0000000p-4f,0x1.fd88da0000000p-1f},
{0x1.0000000000000p+0f,-0x0.0p+0f}, {0x1.8bc8060000000p-1f,-0x1.44cf320000000p-1f}, {0x1.8f8b840000000p-3f,-0x1.f6297c0000000p-1f}, {-0x1.e2b5d40000000p-2f,-0x1.c38b300000000p-1f},
{-0x1.d906bc0000000p-1f,-0x1.87de2a0000000p-2f}, {-0x1.e9f4160000000p-1f,0x1.2940620000000p-2f}, {-0x1.1c73b40000000p-1f,0x1.a9b6620000000p-1f}, {0x1.917a6c0000000p-4f,0x1.fd88da0000000p-1f},
{0x1.0000000000000p+0f,-0x0.0p+0f}, {0x1.8bc8060000000p-1f,-0x1.44cf320000000p-1f}, {0x1.8f8b840000000p-3f,-0x1.f6297c0000000p-1f}, {-0x1.e2b5d40000000p-2f,-0x1.c38b300000000p-1f},
{-0x1.d906bc0000000p-1f,-0x1.87de2a0000000p-2f}, {-0x1.e9f4160000000p-1f,0x1.2940620000000p-2f}, {-0x1.1c73b40000000p-1f,0x1.a9b6620000000p-1f}, {0x1.917a6c0000000p-4f,0x1.fd88da0000000p-1f},
{0x1.0000000000000p+0f,-0x0.0p+0f}, {0x1.8bc8060000000p-1f,-0x1.44cf320000000p-1f}, {0x1.8f8b840000000p-3f,-0x1.f6297c0000000p-1f}, {-0x1.e2b5d40000000p-2f,-0x1.c38b300000000p-1f},
{-0x1.d906bc0000000p-1f,-0x1.87de2a0000000p-2f}, {-0x1.e9f4160000000p-1f,0x1.2940620000000p-2f}, {-0x1.1c73b40000000p-1f,0x1.a9b6620000000p-1f}, {0x1.917a6c0000000p-4f,0x1.fd88da0000000p-1f},
{0x1.0000000000000p+0f,-0x0.0p+0f}, {0x1.8bc8060000000p-1f,-0x1.44cf320000000p-1f}, {0x1.8f8b840000000p-3f,-0x1.f6297c0000000p-1f}, {-0x1.e2b5d40000000p-2f,-0x1.c38b300000000p-1f},
{-0x1.d906bc0000000p-1f,-0x1.87de2a0000000p-2f}, {-0x1.e9f4160000000p-1f,0x1.2940620000000p-2f}, {-0x1.1c73b40000000p-1f,0x1.a9b6620000000p-1f}, {0x1.917a6c0000000p-4f,0x1.fd88da0000000p-1f},
{0x1.0000000000000p+0f,-0x0.0p+0f}, {0x1.8bc8060000000p-1f,-0x1.44cf320000000p-1f}, {0x1.8f8b840000000p-3f,-0x1.f6297c0000000p-1f}, {-0x1.e2b5d40000000p-2f,-0x1.c38b300000000p-1f},
{-0x1.d906bc0000000p-1f,-0x1.87de2a0000000p-2f}, {-0x1.e9f4160000000p-1f,0x1.2940620000000p-2f}, {-0x1.1c73b40000000p-1f,0x1.a9b6620000000p-1f}, {0x1.917a6c0000000p-4f,0x1.fd88da0000000p-1f},
{0x1.0000000000000p+0f,-0x0.0p+0f}, {0x1.fff6220000000p-1f,-0x1.921d200000000p-7f}, {0x1.ffd8860000000p-1f,-0x1.9215600000000p-6f}, {0x1.ffa72e0000000p-1f,-0x1.2d86580000000p-5f},
{0x1.ff621e0000000p-1f,-0x1.91f6600000000p-5f}, {0x1.ff09560000000p-1f,-0x1.f656e80000000p-5f}, {0x1.fe9cda0000000p-1f,-0x1.2d520a0000000p-4f}, {0x1.fe1cb00000000p-1f,-0x1.5f6d000000000p-4f},
{0x1.fd88da0000000p-1f,-0x1.917a6c0000000p-4f}, {0x1.fce1600000000p-1f,-0x1.c3785c0000000p-4f}, {0x1.fc26480000000p-1f,-0x1.f564e60000000p-4f}, {0x1.fb57980000000p-1f,-0x1.139f0c0000000p-3f},
{0x1.fa75580000000p-1f,-0x1.2c81060000000p-3f}, {0x1.f97f920000000p-1f,-0x1.45576c0000000p-3f}, {0x1.f876500000000p-1f,-0x1.5e21440000000p-3f}, {0x1.f7599a0000000p-1f,-0x1.76dd9e0000000p-3f},
{0x1.f6297c0000000p-1f,-0x1.8f8b840000000p-3f}, {0x1.f4e6040000000p-1f,-0x1.a82a020000000p-3f}, {0x1.f38f3a0000000p-1f,-0x1.c0b8260000000p-3f}, {0x1.f225300000000p-1f,-0x1.d934fe0000000p-3f},
{0x1.f0a7f00000000p-1f,-0x1.f19f980000000p-3f}, {0x1.ef178a0000000p-1f,-0x1.04fb800000000p-2f}, {0x1.ed740e0000000p-1f,-0x1.111d260000000p-2f}, {0x1.ebbd8c0000000p-1f,-0x1.1d34440000000p-2f},
{0x1.e9f4160000000p-1f,-0x1.2940620000000p-2f}, {0x1.e817ba0000000p-1f,-0x1.35410c0000000p-2f}, {0x1.e6288e0000000p-1f,-0x1.4135ca0000000p-2f}, {0x1.e426a40000000p-1f,-0x1.4d1e240000000p-2f},
{0x1.e212100000000p-1f,-0x1.58f9a80000000p-2f}, {0x1.dfeae60000000p-1f,-0x1.64c7de0000000p-2f}, {0x1.ddb13c0000000p-1f,-0x1.7088540000000p-2f}, {0x1.db65260000000p-1f,-0x1.7c3a940000000p-2f},
{0x1.d906bc0000000p-1f,-0x1.87de2a0000000p-2f}, {0x1.d696180000000p-1f,-0x1.9372a60000000p-2f}, {0x1.d4134e0000000p-1f,-0x1.9ef7940000000p-2f}, {0x1.d17e780000000p-1f,-0x1.aa6c820000000p-2f},
{0x1.ced7b00000000p-1f,-0x1.b5d1000000000p-2f}, {0x1.cc1f100000000p-1f,-0x1.c1249e0000000p-2f}, {0x1.c954b20000000p-1f,-0x1.cc66ea0000000p-2f}, {0x1.c678b40000000p-1f,-0x1.d797760000000p-2f},
{0x1.c38b300000000p-1f,-0x1.e2b5d40000000p-2f}, {0x1.c08c420000000p-1f,-0x1.edc1960000000p-2f}, {0x1.bd7c0a0000000p-1f,-0x1.f8ba4e0000000p-2f}, {0x1.ba5aa60000000p-1f,-0x1.01cfc80000000p-1f},
{0x1.b728340000000p-1f,-0x1.07387a0000000p-1f}, {0x1.b3e4d40000000p-1f,-0x1.0c97040000000p-1f}, {0x1.b090a60000000p-1f,-0x1.11eb360000000p-1f}, {0x1.ad2bca0000000p-1f,-0x1.1734d60000000p-1f},
{0x1.a9b6620000000p-1f,-0x1.1c73b40000000p-1f}, {0x1.a630920000000p-1f,-0x1.21a79a0000000p-1f}, {0x1.a29a7a0000000p-1f,-0x1.26d0540000000p-1f}, {0x1.9ef43e0000000p-1f,-0x1.2bedb20000000p-1f},
{0x1.9b3e040000000p-1f,-0x1.30ff800000000p-1f}, {0x1.9777f00000000p-1f,-0x1.36058c0000000p-1f}, {0x1.93a2240000000p-1f,-0x1.3affa20000000p-1f}, {0x1.8fbcca0000000p-1f,-0x1.3fed960000000p-1f},
{0x1.8bc8060000000p-1f,-0x1.44cf320000000p-1f}, {0x1.87c4000000000p-1f,-0x1.49a44a0000000p-1f}, {0x1.83b0e00000000p-1f,-0x1.4e6cac0000000p-1f}, {0x1.7f8ece0000000p-1f,-0x1.53282a0000000p-1f},
{0x1.7b5df20000000p-1f,-0x1.57d6940000000p-1f}, {0x1.771e760000000p-1f,-0x1.5c77bc0000000p-1f}, {0x1.72d0840000000p-1f,-0x1.610b760000000p-1f}, {0x1.6e74460000000p-1f,-0x1.6591920000000p-1f},
{0x1.0000000000000p+0f,-0x0.0p+0f}, {0x1.ffd8860000000p-1f,-0x1.9215600000000p-6f}, {0x1.ff621e0000000p-1f,-0x1.91f6600000000p-5f}, {0x1.fe9cda0000000p-1f,-0x1.2d520a0000000p-4f},
{0x1.fd88da0000000p-1f,-0x1.917a6c0000000p-4f}, {0x1.fc26480000000p-1f,-0x1.f564e60000000p-4f}, {0x1.fa75580000000p-1f,-0x1.2c81060000000p-3f}, {0x1.f876500000000p-1f,-0x1.5e21440000000p-3f},
{0x1.f6297c0000000p-1f,-0x1.8f8b840000000p-3f}, {0x1.f38f3a0000000p-1f,-0x1.c0b8260000000p-3f}, {0x1.f0a7f00000000p-1f,-0x1.f19f980000000p-3f}, {0x1.ed740e0000000p-1f,-0x1.111d260000000p-2f},
{0x1.e9f4160000000p-1f,-0x1.2940620000000p-2f}, {0x1.e6288e0000000p-1f,-0x1.4135ca0000000p-2f}, {0x1.e212100000000p-1f,-0x1.58f9a80000000p-2f}, {0x1.ddb13c0000000p-1f,-0x1.7088540000000p-2f},
{0x1.d906bc0000000p-1f,-0x1.87de2a0000000p-2f}, {0x1.d4134e0000000p-1f,-0x1.9ef7940000000p-2f}, {0x1.ced7b00000000p-1f,-0x1.b5d1000000000p-2f}, {0x1.c954b20000000p-1f,-0x1.cc66ea0000000p-2f},
{0x1.c38b300000000p-1f,-0x1.e2b5d40000000p-2f}, {0x1.bd7c0a0000000p-1f,-0x1.f8ba4e0000000p-2f}, {0x1.b728340000000p-1f,-0x1.07387a0000000p-1f}, {0x1.b090a60000000p-1f,-0x1.11eb360000000p-1f},
{0x1.a9b6620000000p-1f,-0x1.1c73b40000000p-1f}, {0x1.a29a7a0000000p-1f,-0x1.26d0540000000p-1f}, {0x1.9b3e040000000p-1f,-0x1.30ff800000000p-1f}, {0x1.93a2240000000p-1f,-0x1.3affa20000000p-1f},
{0x1.8bc8060000000p-1f,-0x1.44cf320000000p-1f}, {0x1.83b0e00000000p-1f,-0x1.4e6cac0000000p-1f}, {0x1.7b5df20000000p-1f,-0x1.57d6940000000p-1f}, {0x1.72d0840000000p-1f,-0x1.610b760000000p-1f},
{0x1.6a09e60000000p-1f,-0x1.6a09e60000000p-1f}, {0x1.610b760000000p-1f,-0x1.72d0840000000p-1f}, {0x1.57d6940000000p-1f,-0x1.7b5df20000000p-1f}, {0x1.4e6cac0000000p-1f,-0x1.83b0e00000000p-1f},
{0x1.44cf320000000p-1f,-0x1.8bc8060000000p-1f}, {0x1.3affa20000000p-1f,-0x1.93a2240000000p-1f}, {0x1.30ff800000000p-1f,-0x1.9b3e040000000p-1f}, {0x1.26d0540000000p-1f,-0x1.a29a7a0000000p-1f},
{0x1.1c73b40000000p-1f,-0x1.a9b6620000000p-1f}, {0x1.11eb360000000p-1f,-0x1.b090a60000000p-1f}, {0x1.07387a0000000p-1f,-0x1.b728340000000p-1f}, {0x1.f8ba4e0000000p-2f,-0x1.bd7c0a0000000p-1f},
{0x1.e2b5d40000000p-2f,-0x1.c38b300000000p-1f}, {0x1.cc66ea0000000p-2f,-0x1.c954b20000000p-1f}, {0x1.b5d1000000000p-2f,-0x1.ced7b00000000p-1f}, {0x1.9ef7940000000p-2f,-0x1.d4134e0000000p-1f},
{0x1.87de2a0000000p-2f,-0x1.d906bc0000000p-1f}, {0x1.7088540000000p-2f,-0x1.ddb13c0000000p-1f}, {0x1.58f9a80000000p-2f,-0x1.e212100000000p-1f}, {0x1.4135ca0000000p-2f,-0x1.e6288e0000000p-1f},
{0x1.2940620000000p-2f,-0x1.e9f4160000000p-1f}, {0x1.111d260000000p-2f,-0x1.ed740e0000000p-1f}, {0x1.f19f980000000p-3f,-0x1.f0a7f00000000p-1f}, {0x1.c0b8260000000p-3f,-0x1.f38f3a0000000p-1f},
{0x1.8f8b840000000p-3f,-0x1.f6297c0000000p-1f}, {0x1.5e21440000000p-3f,-0x1.f876500000000p-1f}, {0x1.2c81060000000p-3f,-0x1.fa75580000000p-1f}, {0x1.f564e60000000p-4f,-0x1.fc26480000000p-1f},
{0x1.917a6c0000000p-4f,-0x1.fd88da0000000p-1f}, {0x1.2d520a0000000p-4f,-0x1.fe9cda0000000p-1f}, {0x1.91f6600000000p-5f,-0x1.ff621e0000000p-1f}, {0x1.9215600000000p-6f,-0x1.ffd8860000000p-1f},
{0x1.0000000000000p+0f,-0x0.0p+0f}, {0x1.ffa72e0000000p-1f,-0x1.2d86580000000p-5f}, {0x1.fe9cda0000000p-1f,-0x1.2d520a0000000p-4f}, {0x1.fce1600000000p-1f,-0x1.c3785c0000000p-4f},
{0x1.fa75580000000p-1f,-0x1.2c81060000000p-3f}, {0x1.f7599a0000000p-1f,-0x1.76dd9e0000000p-3f}, {0x1.f38f3a0000000p-1f,-0x1.c0b8260000000p-3f}, {0x1.ef178a0000000p-1f,-0x1.04fb800000000p-2f},
{0x1.e9f4160000000p-1f,-0x1.2940620000000p-2f}, {0x1.e426a40000000p-1f,-0x1.4d1e240000000p-2f}, {0x1.ddb13c0000000p-1f,-0x1.7088540000000p-2f}, {0x1.d696180000000p-1f,-0x1.9372a60000000p-2f},
{0x1.ced7b00000000p-1f,-0x1.b5d1000000000p-2f}, {0x1.c678b40000000p-1f,-0x1.d797760000000p-2f}, {0x1.bd7c0a0000000p-1f,-0x1.f8ba4e0000000p-2f}, {0x1.b3e4d40000000p-1f,-0x1.0c97040000000p-1f},
{0x1.a9b6620000000p-1f,-0x1.1c73b40000000p-1f}, {0x1.9ef43e0000000p-1f,-0x1.2bedb20000000p-1f}, {0x1.93a2240000000p-1f,-0x1.3affa20000000p-1f}, {0x1.87c4000000000p-1f,-0x1.49a44a0000000p-1f},
{0x1.7b5df20000000p-1f,-0x1.57d6940000000p-1f}, {0x1.6e74460000000p-1f,-0x1.6591920000000p-1f}, {0x1.610b760000000p-1f,-0x1.72d0840000000p-1f}, {0x1.53282a0000000p-1f,-0x1.7f8ece0000000p-1f},
{0x1.44cf320000000p-1f,-0x1.8bc8060000000p-1f}, {0x1.36058c0000000p-1f,-0x1.9777f00000000p-1f}, {0x1.26d0540000000p-1f,-0x1.a29a7a0000000p-1f}, {0x1.1734d60000000p-1f,-0x1.ad2bca0000000p-1f},
{0x1.07387a0000000p-1f,-0x1.b728340000000p-1f}, {0x1.edc1960000000p-2f,-0x1.c08c420000000p-1f}, {0x1.cc66ea0000000p-2f,-0x1.c954b20000000p-1f}, {0x1.aa6c820000000p-2f,-0x1.d17e780000000p-1f},
{0x1.87de2a0000000p-2f,-0x1.d906bc0000000p-1f}, {0x1.64c7de0000000p-2f,-0x1.dfeae60000000p-1f}, {0x1.4135ca0000000p-2f,-0x1.e6288e0000000p-1f}, {0x1.1d34440000000p-2f,-0x1.ebbd8c0000000p-1f},
{0x1.f19f980000000p-3f,-0x1.f0a7f00000000p-1f}, {0x1.a82a020000000p-3f,-0x1.f4e6040000000p-1f}, {0x1.5e21440000000p-3f,-0x1.f876500000000p-1f}, {0x1.139f0c0000000p-3f,-0x1.fb57980000000p-1f},
{0x1.917a6c0000000p-4f,-0x1.fd88da0000000p-1f}, {0x1.f656e80000000p-5f,-0x1.ff09560000000p-1f}, {0x1.9215600000000p-6f,-0x1.ffd8860000000p-1f}, {-0x1.921d200000000p-7f,-0x1.fff6220000000p-1f},
{-0x1.91f6600000000p-5f,-0x1.ff621e0000000p-1f}, {-0x1.5f6d000000000p-4f,-0x1.fe1cb00000000p-1f}, {-0x1.f564e60000000p-4f,-0x1.fc26480000000p-1f}, {-0x1.45576c0000000p-3f,-0x1.f97f920000000p-1f},
{-0x1.8f8b840000000p-3f,-0x1.f6297c0000000p-1f}, {-0x1.d934fe0000000p-3f,-0x1.f225300000000p-1f}, {-0x1.111d260000000p-2f,-0x1.ed740e0000000p-1f}, {-0x1.35410c0000000p-2f,-0x1.e817ba0000000p-1f},
{-0x1.58f9a80000000p-2f,-0x1.e212100000000p-1f}, {-0x1.7c3a940000000p-2f,-0x1.db65260000000p-1f}, {-0x1.9ef7940000000p-2f,-0x1.d4134e0000000p-1f}, {-0x1.c1249e0000000p-2f,-0x1.cc1f100000000p-1f},
{-0x1.e2b5d40000000p-2f,-0x1.c38b300000000p-1f}, {-0x1.01cfc80000000p-1f,-0x1.ba5aa60000000p-1f}, {-0x1.11eb360000000p-1f,-0x1.b090a60000000p-1f}, {-0x1.21a79a0000000p-1f,-0x1.a630920000000p-1f},
{-0x1.30ff800000000p-1f,-0x1.9b3e040000000p-1f}, {-0x1.3fed960000000p-1f,-0x1.8fbcca0000000p-1f}, {-0x1.4e6cac0000000p-1f,-0x1.83b0e00000000p-1f}, {-0x1.5c77bc0000000p-1f,-0x1.771e760000000p-1f},
{0x1.0000000000000p+0f,-0x0.0p+0f}, {0x1.ff621e0000000p-1f,-0x1.91f6600000000p-5f}, {0x1.fd88da0000000p-1f,-0x1.917a6c0000000p-4f}, {0x1.fa75580000000p-1f,-0x1.2c81060000000p-3f},
{0x1.f6297c0000000p-1f,-0x1.8f8b840000000p-3f}, {0x1.f0a7f00000000p-1f,-0x1.f19f980000000p-3f}, {0x1.e9f4160000000p-1f,-0x1.2940620000000p-2f}, {0x1.e212100000000p-1f,-0x1.58f9a80000000p-2f},
{0x1.d906bc0000000p-1f,-0x1.87de2a0000000p-2f}, {0x1.ced7b00000000p-1f,-0x1.b5d1000000000p-2f}, {0x1.c38b300000000p-1f,-0x1.e2b5d40000000p-2f}, {0x1.b728340000000p-1f,-0x1.07387a0000000p-1f},
{0x1.a9b6620000000p-1f,-0x1.1c73b40000000p-1f}, {0x1.9b3e040000000p-1f,-0x1.30ff800000000p-1f}, {0x1.8bc8060000000p-1f,-0x1.44cf320000000p-1f}, {0x1.7b5df20000000p-1f,-0x1.57d6940000000p-1f},
{0x1.6a09e60000000p-1f,-0x1.6a09e60000000p-1f}, {0x1.57d6940000000p-1f,-0x1.7b5df20000000p-1f}, {0x1.44cf320000000p-1f,-0x1.8bc8060000000p-1f}, {0x1.30ff800000000p-1f,-0x1.9b3e040000000p-1f},
{0x1.1c73b40000000p-1f,-0x1.a9b6620000000p-1f}, {0x1.07387a0000000p-1f,-0x1.b728340000000p-1f}, {0x1.e2b5d40000000p-2f,-0x1.c38b300000000p-1f}, {0x1.b5d1000000000p-2f,-0x1.ced7b00000000p-1f},
{0x1.87de2a0000000p-2f,-0x1.d906bc0000000p-1f}, {0x1.58f9a80000000p-2f,-0x1.e212100000000p-1f}, {0x1.2940620000000p-2f,-0x1.e9f4160000000p-1f}, {0x1.f19f980000000p-3f,-0x1.f0a7f00000000p-1f},
{0x1.8f8b840000000p-3f,-0x1.f6297c0000000p-1f}, {0x1.2c81060000000p-3f,-0x1.fa75580000000p-1f}, {0x1.917a6c0000000p-4f,-0x1.fd88da0000000p-1f}, {0x1.91f6600000000p-5f,-0x1.ff621e0000000p-1f},
{0x1.1a62640000000p-54f,-0x1.0000000000000p+0f}, {-0x1.91f6600000000p-5f,-0x1.ff621e0000000p-1f}, {-0x1.917a6c0000000p-4f,-0x1.fd88da0000000p-1f}, {-0x1.2c81060000000p-3f,-0x1.fa75580000000p-1f},
{-0x1.8f8b840000000p-3f,-0x1.f6297c0000000p-1f}, {-0x1.f19f980000000p-3f,-0x1.f0a7f00000000p-1f}, {-0x1.2940620000000p-2f,-0x1.e9f4160000000p-1f}, {-0x1.58f9a80000000p-2f,-0x1.e212100000000p-1f},
{-0x1.87de2a0000000p-2f,-0x1.d906bc0000000p-1f}, {-0x1.b5d1000000000p-2f,-0x1.ced7b00000000p-1f}, {-0x1.e2b5d40000000p-2f,-0x1.c38b300000000p-1f}, {-0x1.07387a0000000p-1f,-0x1.b728340000000p-1f},
{-0x1.1c73b40000000p-1f,-0x1.a9b6620000000p-1f}, {-0x1.30ff800000000p-1f,-0x1.9b3e040000000p-1f}, {-0x1.44cf320000000p-1f,-0x1.8bc8060000000p-1f}, {-0x1.57d6940000000p-1f,-0x1.7b5df20000000p-1f},
{-0x1.6a09e60000000p-1f,-0x1.6a09e60000000p-1f}, {-0x1.7b5df20000000p-1f,-0x1.57d6940000000p-1f}, {-0x1.8bc8060000000p-1f,-0x1.44cf320000000p-1f}, {-0x1.9b3e040000000p-1f,-0x1.30ff800000000p-1f},
{-0x1.a9b6620000000p-1f,-0x1.1c73b40000000p-1f}, {-0x1.b728340000000p-1f,-0x1.07387a0000000p-1f}, {-0x1.c38b300000000p-1f,-0x1.e2b5d40000000p-2f}, {-0x1.ced7b00000000p-1f,-0x1.b5d1000000000p-2f},
{-0x1.d906bc0000000p-1f,-0x1.87de2a0000000p-2f}, {-0x1.e212100000000p-1f,-0x1.58f9a80000000p-2f}, {-0x1.e9f4160000000p-1f,-0x1.2940620000000p-2f}, {-0x1.f0a7f00000000p-1f,-0x1.f19f980000000p-3f},
{-0x1.f6297c0000000p-1f,-0x1.8f8b840000000p-3f}, {-0x1.fa75580000000p-1f,-0x1.2c81060000000p-3f}, {-0x1.fd88da0000000p-1f,-0x1.917a6c0000000p-4f}, {-0x1.ff621e0000000p-1f,-0x1.91f6600000000p-5f},
{0x1.0000000000000p+0f,-0x0.0p+0f}, {0x1.ff09560000000p-1f,-0x1.f656e80000000p-5f}, {0x1.fc26480000000p-1f,-0x1.f564e60000000p-4f}, {0x1.f7599a0000000p-1f,-0x1.76dd9e0000000p-3f},
{0x1.f0a7f00000000p-1f,-0x1.f19f980000000p-3f}, {0x1.e817ba0000000p-1f,-0x1.35410c0000000p-2f}, {0x1.ddb13c0000000p-1f,-0x1.7088540000000p-2f}, {0x1.d17e780000000p-1f,-0x1.aa6c820000000p-2f},
{0x1.c38b300000000p-1f,-0x1.e2b5d40000000p-2f}, {0x1.b3e4d40000000p-1f,-0x1.0c97040000000p-1f}, {0x1.a29a7a0000000p-1f,-0x1.26d0540000000p-1f}, {0x1.8fbcca0000000p-1f,-0x1.3fed960000000p-1f},
{0x1.7b5df20000000p-1f,-0x1.57d6940000000p-1f}, {0x1.6591920000000p-1f,-0x1.6e74460000000p-1f}, {0x1.4e6cac0000000p-1f,-0x1.83b0e00000000p-1f}, {0x1.36058c0000000p-1f,-0x1.9777f00000000p-1f},
{0x1.1c73b40000000p-1f,-0x1.a9b6620000000p-1f}, {0x1.01cfc80000000p-1f,-0x1.ba5aa60000000p-1f}, {0x1.cc66ea0000000p-2f,-0x1.c954b20000000p-1f}, {0x1.9372a60000000p-2f,-0x1.d696180000000p-1f},
{0x1.58f9a80000000p-2f,-0x1.e212100000000p-1f}, {0x1.1d34440000000p-2f,-0x1.ebbd8c0000000p-1f}, {0x1.c0b8260000000p-3f,-0x1.f38f3a0000000p-1f}, {0x1.45576c0000000p-3f,-0x1.f97f920000000p-1f},
{0x1.917a6c0000000p-4f,-0x1.fd88da0000000p-1f}, {0x1.2d86580000000p-5f,-0x1.ffa72e0000000p-1f}, {-0x1.9215600000000p-6f,-0x1.ffd8860000000p-1f}, {-0x1.5f6d000000000p-4f,-0x1.fe1cb00000000p-1f},
{-0x1.2c81060000000p-3f,-0x1.fa75580000000p-1f}, {-0x1.a82a020000000p-3f,-0x1.f4e6040000000p-1f}, {-0x1.111d260000000p-2f,-0x1.ed740e0000000p-1f}, {-0x1.4d1e240000000p-2f,-0x1.e426a40000000p-1f},
{-0x1.87de2a0000000p-2f,-0x1.d906bc0000000p-1f}, {-0x1.c1249e0000000p-2f,-0x1.cc1f100000000p-1f}, {-0x1.f8ba4e0000000p-2f,-0x1.bd7c0a0000000p-1f}, {-0x1.1734d60000000p-1f,-0x1.ad2bca0000000p-1f},
{-0x1.30ff800000000p-1f,-0x1.9b3e040000000p-1f}, {-0x1.49a44a0000000p-1f,-0x1.87c4000000000p-1f}, {-0x1.610b760000000p-1f,-0x1.72d0840000000p-1f}, {-0x1.771e760000000p-1f,-0x1.5c77bc0000000p-1f},
{-0x1.8bc8060000000p-1f,-0x1.44cf320000000p-1f}, {-0x1.9ef43e0000000p-1f,-0x1.2bedb20000000p-1f}, {-0x1.b090a60000000p-1f,-0x1.11eb360000000p-1f}, {-0x1.c08c420000000p-1f,-0x1.edc1960000000p-2f},
{-0x1.ced7b00000000p-1f,-0x1.b5d1000000000p-2f}, {-0x1.db65260000000p-1f,-0x1.7c3a940000000p-2f}, {-0x1.e6288e0000000p-1f,-0x1.4135ca0000000p-2f}, {-0x1.ef178a0000000p-1f,-0x1.04fb800000000p-2f},
{-0x1.f6297c0000000p-1f,-0x1.8f8b840000000p-3f}, {-0x1.fb57980000000p-1f,-0x1.139f0c0000000p-3f}, {-0x1.fe9cda0000000p-1f,-0x1.2d520a0000000p-4f}, {-0x1.fff6220000000p-1f,-0x1.921d200000000p-7f},
{-0x1.ff621e0000000p-1f,0x1.91f6600000000p-5f}, {-0x1.fce1600000000p-1f,0x1.c3785c0000000p-4f}, {-0x1.f876500000000p-1f,0x1.5e21440000000p-3f}, {-0x1.f225300000000p-1f,0x1.d934fe0000000p-3f},
{-0x1.e9f4160000000p-1f,0x1.2940620000000p-2f}, {-0x1.dfeae60000000p-1f,0x1.64c7de0000000p-2f}, {-0x1.d4134e0000000p-1f,0x1.9ef7940000000p-2f}, {-0x1.c678b40000000p-1f,0x1.d797760000000p-2f},
{-0x1.b728340000000p-1f,0x1.07387a0000000p-1f}, {-0x1.a630920000000p-1f,0x1.21a79a0000000p-1f}, {-0x1.93a2240000000p-1f,0x1.3affa20000000p-1f}, {-0x1.7f8ece0000000p-1f,0x1.53282a0000000p-1f},
{0x1.0000000000000p+0f,-0x0.0p+0f}, {0x1.fe9cda0000000p-1f,-0x1.2d520a0000000p-4f}, {0x1.fa75580000000p-1f,-0x1.2c81060000000p-3f}, {0x1.f38f3a0000000p-1f,-0x1.c0b8260000000p-3f},
{0x1.e9f4160000000p-1f,-0x1.2940620000000p-2f}, {0x1.ddb13c0000000p-1f,-0x1.7088540000000p-2f}, {0x1.ced7b00000000p-1f,-0x1.b5d1000000000p-2f}, {0x1.bd7c0a0000000p-1f,-0x1.f8ba4e0000000p-2f},
{0x1.a9b6620000000p-1f,-0x1.1c73b40000000p-1f}, {0x1.93a2240000000p-1f,-0x1.3affa20000000p-1f}, {0x1.7b5df20000000p-1f,-0x1.57d6940000000p-1f}, {0x1.610b760000000p-1f,-0x1.72d0840000000p-1f},
{0x1.44cf320000000p-1f,-0x1.8bc8060000000p-1f}, {0x1.26d0540000000p-1f,-0x1.a29a7a0000000p-1f}, {0x1.07387a0000000p-1f,-0x1.b728340000000p-1f}, {0x1.cc66ea0000000p-2f,-0x1.c954b20000000p-1f},
{0x1.87de2a0000000p-2f,-0x1.d906bc0000000p-1f}, {0x1.4135ca0000000p-2f,-0x1.e6288e0000000p-1f}, {0x1.f19f980000000p-3f,-0x1.f0a7f00000000p-1f}, {0x1.5e21440000000p-3f,-0x1.f876500000000p-1f},
{0x1.917a6c0000000p-4f,-0x1.fd88da0000000p-1f}, {0x1.9215600000000p-6f,-0x1.ffd8860000000p-1f}, {-0x1.91f6600000000p-5f,-0x1.ff621e0000000p-1f}, {-0x1.f564e60000000p-4f,-0x1.fc26480000000p-1f},
{-0x1.8f8b840000000p-3f,-0x1.f6297c0000000p-1f}, {-0x1.111d260000000p-2f,-0x1.ed740e0000000p-1f}, {-0x1.58f9a80000000p-2f,-0x1.e212100000000p-1f}, {-0x1.9ef7940000000p-2f,-0x1.d4134e0000000p-1f},
{-0x1.e2b5d40000000p-2f,-0x1.c38b300000000p-1f}, {-0x1.11eb360000000p-1f,-0x1.b090a60000000p-1f}, {-0x1.30ff800000000p-1f,-0x1.9b3e040000000p-1f}, {-0x1.4e6cac0000000p-1f,-0x1.83b0e00000000p-1f},
{-0x1.6a09e60000000p-1f,-0x1.6a09e60000000p-1f}, {-0x1.83b0e00000000p-1f,-0x1.4e6cac0000000p-1f}, {-0x1.9b3e040000000p-1f,-0x1.30ff800000000p-1f}, {-0x1.b090a60000000p-1f,-0x1.11eb360000000p-1f},
{-0x1.c38b300000000p-1f,-0x1.e2b5d40000000p-2f}, {-0x1.d4134e0000000p-1f,-0x1.9ef7940000000p-2f}, {-0x1.e212100000000p-1f,-0x1.58f9a80000000p-2f}, {-0x1.ed740e0000000p-1f,-0x1.111d260000000p-2f},
{-0x1.f6297c0000000p-1f,-0x1.8f8b840000000p-3f}, {-0x1.fc26480000000p-1f,-0x1.f564e60000000p-4f}, {-0x1.ff621e0000000p-1f,-0x1.91f6600000000p-5f}, {-0x1.ffd8860000000p-1f,0x1.9215600000000p-6f},
{-0x1.fd88da0000000p-1f,0x1.917a6c0000000p-4f}, {-0x1.f876500000000p-1f,0x1.5e21440000000p-3f}, {-0x1.f0a7f00000000p-1f,0x1.f19f980000000p-3f}, {-0x1.e6288e0000000p-1f,0x1.4135ca0000000p-2f},
{-0x1.d906bc0000000p-1f,0x1.87de2a0000000p-2f}, {-0x1.c954b20000000p-1f,0x1.cc66ea0000000p-2f}, {-0x1.b728340000000p-1f,0x1.07387a0000000p-1f}, {-0x1.a29a7a0000000p-1f,0x1.26d0540000000p-1f},
{-0x1.8bc8060000000p-1f,0x1.44cf320000000p-1f}, {-0x1.72d0840000000p-1f,0x1.610b760000000p-1f}, {-0x1.57d6940000000p-1f,0x1.7b5df20000000p-1f}, {-0x1.3affa20000000p-1f,0x1.93a2240000000p-1f},
{-0x1.1c73b40000000p-1f,0x1.a9b6620000000p-1f}, {-0x1.f8ba4e0000000p-2f,0x1.bd7c0a0000000p-1f}, {-0x1.b5d1000000000p-2f,0x1.ced7b00000000p-1f}, {-0x1.7088540000000p-2f,0x1.ddb13c0000000p-1f},
{-0x1.2940620000000p-2f,0x1.e9f4160000000p-1f}, {-0x1.c0b8260000000p-3f,0x1.f38f3a0000000p-1f}, {-0x1.2c81060000000p-3f,0x1.fa75580000000p-1f}, {-0x1.2d520a0000000p-4f,0x1.fe9cda0000000p-1f},
{0x1.0000000000000p+0f,-0x0.0p+0f}, {0x1.fe1cb00000000p-1f,-0x1.5f6d000000000p-4f}, {0x1.f876500000000p-1f,-0x1.5e21440000000p-3f}, {0x1.ef178a0000000p-1f,-0x1.04fb800000000p-2f},
{0x1.e212100000000p-1f,-0x1.58f9a80000000p-2f}, {0x1.d17e780000000p-1f,-0x1.aa6c820000000p-2f}, {0x1.bd7c0a0000000p-1f,-0x1.f8ba4e0000000p-2f}, {0x1.a630920000000p-1f,-0x1.21a79a0000000p-1f},
{0x1.8bc8060000000p-1f,-0x1.44cf320000000p-1f}, {0x1.6e74460000000p-1f,-0x1.6591920000000p-1f}, {0x1.4e6cac0000000p-1f,-0x1.83b0e00000000p-1f}, {0x1.2bedb20000000p-1f,-0x1.9ef43e0000000p-1f},
{0x1.07387a0000000p-1f,-0x1.b728340000000p-1f}, {0x1.c1249e0000000p-2f,-0x1.cc1f100000000p-1f}, {0x1.7088540000000p-2f,-0x1.ddb13c0000000p-1f}, {0x1.1d34440000000p-2f,-0x1.ebbd8c0000000p-1f},
{0x1.8f8b840000000p-3f,-0x1.f6297c0000000p-1f}, {0x1.c3785c0000000p-4f,-0x1.fce1600000000p-1f}, {0x1.9215600000000p-6f,-0x1.ffd8860000000p-1f}, {-0x1.f656e80000000p-5f,-0x1.ff09560000000p-1f},
{-0x1.2c81060000000p-3f,-0x1.fa75580000000p-1f}, {-0x1.d934fe0000000p-3f,-0x1.f225300000000p-1f}, {-0x1.4135ca0000000p-2f,-0x1.e6288e0000000p-1f}, {-0x1.9372a60000000p-2f,-0x1.d696180000000p-1f},
{-0x1.e2b5d40000000p-2f,-0x1.c38b300000000p-1f}, {-0x1.1734d60000000p-1f,-0x1.ad2bca0000000p-1f}, {-0x1.3affa20000000p-1f,-0x1.93a2240000000p-1f}, {-0x1.5c77bc0000000p-1f,-0x1.771e760000000p-1f},
{-0x1.7b5df20000000p-1f,-0x1.57d6940000000p-1f}, {-0x1.9777f00000000p-1f,-0x1.36058c0000000p-1f}, {-0x1.b090a60000000p-1f,-0x1.11eb360000000p-1f}, {-0x1.c678b40000000p-1f,-0x1.d797760000000p-2f},
{-0x1.d906bc0000000p-1f,-0x1.87de2a0000000p-2f}, {-0x1.e817ba0000000p-1f,-0x1.35410c0000000p-2f}, {-0x1.f38f3a0000000p-1f,-0x1.c0b8260000000p-3f}, {-0x1.fb57980000000p-1f,-0x1.139f0c0000000p-3f},
{-0x1.ff621e0000000p-1f,-0x1.91f6600000000p-5f}, {-0x1.ffa72e0000000p-1f,0x1.2d86580000000p-5f}, {-0x1.fc26480000000p-1f,0x1.f564e60000000p-4f}, {-0x1.f4e6040000000p-1f,0x1.a82a020000000p-3f},
{-0x1.e9f4160000000p-1f,0x1.2940620000000p-2f}, {-0x1.db65260000000p-1f,0x1.7c3a940000000p-2f}, {-0x1.c954b20000000p-1f,0x1.cc66ea0000000p-2f}, {-0x1.b3e4d40000000p-1f,0x1.0c97040000000p-1f},
{-0x1.9b3e040000000p-1f,0x1.30ff800000000p-1f}, {-0x1.7f8ece0000000p-1f,0x1.53282a0000000p-1f}, {-0x1.610b760000000p-1f,0x1.72d0840000000p-1f}, {-0x1.3fed960000000p-1f,0x1.8fbcca0000000p-1f},
{-0x1.1c73b40000000p-1f,0x1.a9b6620000000p-1f}, {-0x1.edc1960000000p-2f,0x1.c08c420000000p-1f}, {-0x1.9ef7940000000p-2f,0x1.d4134e0000000p-1f}, {-0x1.4d1e240000000p-2f,0x1.e426a40000000p-1f},
{-0x1.f19f980000000p-3f,0x1.f0a7f00000000p-1f}, {-0x1.45576c0000000p-3f,0x1.f97f920000000p-1f}, {-0x1.2d520a0000000p-4f,0x1.fe9cda0000000p-1f}, {0x1.921d200000000p-7f,0x1.fff6220000000p-1f},
{0x1.917a6c0000000p-4f,0x1.fd88da0000000p-1f}, {0x1.76dd9e0000000p-3f,0x1.f7599a0000000p-1f}, {0x1.111d260000000p-2f,0x1.ed740e0000000p-1f}, {0x1.64c7de0000000p-2f,0x1.dfeae60000000p-1f},
{0x1.b5d1000000000p-2f,0x1.ced7b00000000p-1f}, {0x1.01cfc80000000p-1f,0x1.ba5aa60000000p-1f}, {0x1.26d0540000000p-1f,0x1.a29a7a0000000p-1f}, {0x1.49a44a0000000p-1f,0x1.87c4000000000p-1f},
};
__device__ const float2 g_twx[3 * 512] = {
{0x1.0000000000000p+0f,-0x0.0p+0f}, {0x1.ffff620000000p-1f,-0x1.921f8c0000000p-9f}, {0x1.fffd880000000p-1f,-0x1.921f100000000p-8f}, {0x1.fffa720000000p-1f,-0x1.2d96b00000000p-7f},
{0x1.fff6220000000p-1f,-0x1.921d200000000p-7f}, {0x1.fff0940000000p-1f,-0x1.f6a2960000000p-7f}, {0x1.ffe9cc0000000p-1f,-0x1.2d936c0000000p-6f}, {0x1.ffe1c60000000p-1f,-0x1.5fd4d20000000p-6f},
{0x1.ffd8860000000p-1f,-0x1.9215600000000p-6f}, {0x1.ffce0a0000000p-1f,-0x1.c454f40000000p-6f}, {0x1.ffc2520000000p-1f,-0x1.f693740000000p-6f}, {0x1.ffb55e0000000p-1f,-0x1.14685e0000000p-5f},
{0x1.ffa72e0000000p-1f,-0x1.2d86580000000p-5f}, {0x1.ff97c40000000p-1f,-0x1.46a3960000000p-5f}, {0x1.ff871e0000000p-1f,-0x1.5fc00e0000000p-5f}, {0x1.ff753c0000000p-1f,-0x1.78dbaa0000000p-5f},
{0x1.ff621e0000000p-1f,-0x1.91f6600000000p-5f}, {0x1.ff4dc60000000p-1f,-0x1.ab101c0000000p-5f}, {0x1.ff38300000000p-1f,-0x1.c428d20000000p-5f}, {0x1.ff21620000000p-1f,-0x1.dd40700000000p-5f},
{0x1.ff09560000000p-1f,-0x1.f656e80000000p-5f}, {0x1.fef0100000000p-1f,-0x1.07b6140000000p-4f}, {0x1.fed58e0000000p-1f,-0x1.1440140000000p-4f}, {0x1.feb9d20000000p-1f,-0x1.20c9680000000p-4f},
{0x1.fe9cda0000000p-1f,-0x1.2d520a0000000p-4f}, {0x1.fe7ea80000000p-1f,-0x1.39d9f20000000p-4f}, {0x1.fe5f3a0000000p-1f,-0x1.4661180000000p-4f}, {0x1.fe3e920000000p-1f,-0x1.52e7740000000p-4f},
{0x1.fe1cb00000000p-1f,-0x1.5f6d000000000p-4f}, {0x1.fdf9920000000p-1f,-0x1.6bf1b40000000p-4f}, {0x1.fdd53a0000000p-1f,-0x1.7875860000000p-4f}, {0x1.fdafa80000000p-1f,-0x1.84f8720000000p-4f},
{0x1.fd88da0000000p-1f,-0x1.917a6c0000000p-4f}, {0x1.fd60d20000000p-1f,-0x1.9dfb6e0000000p-4f}, {0x1.fd37920000000p-1f,-0x1.aa7b720000000p-4f}, {0x1.fd0d160000000p-1f,-0x1.b6fa6e0000000p-4f},
{0x1.fce1600000000p-1f,-0x1.c3785c0000000p-4f}, {0x1.fcb4700000000p-1f,-0x1.cff5340000000p-4f}, {0x1.fc86460000000p-1f,-0x1.dc70ec0000000p-4f}, {0x1.fc56e40000000p-1f,-0x1.e8eb800000000p-4f},
{0x1.fc26480000000p-1f,-0x1.f564e60000000p-4f}, {0x1.fbf4700000000p-1f,-0x1.00ee8a0000000p-3f}, {0x1.fbc1620000000p-1f,-0x1.072a040000000p-3f}, {0x1.fb8d180000000p-1f,-0x1.0d64dc0000000p-3f},
{0x1.fb57980000000p-1f,-0x1.139f0c0000000p-3f}, {0x1.fb20dc0000000p-1f,-0x1.19d8940000000p-3f}, {0x1.fae8e80000000p-1f,-0x1.20116e0000000p-3f}, {0x1.faafbc0000000p-1f,-0x1.2649940000000p-3f},
{0x1.fa75580000000p-1f,-0x1.2c81060000000p-3f}, {0x1.fa39ba0000000p-1f,-0x1.32b7c00000000p-3f}, {0x1.f9fce60000000p-1f,-0x1.38edbc0000000p-3f}, {0x1.f9bed80000000p-1f,-0x1.3f22f60000000p-3f},
{0x1.f97f920000000p-1f,-0x1.45576c0000000p-3f}, {0x1.f93f140000000p-1f,-0x1.4b8b180000000p-3f}, {0x1.f8fd600000000p-1f,-0x1.51bdf80000000p-3f}, {0x1.f8ba740000000p-1f,-0x1.57f0080000000p-3f},
{0x1.f876500000000p-1f,-0x1.5e21440000000p-3f}, {0x1.f830f40000000p-1f,-0x1.6451a80000000p-3f}, {0x1.f7ea620000000p-1f,-0x1.6a81300000000p-3f}, {0x1.f7a29a0000000p-1f,-0x1.70afd80000000p-3f},
{0x1.f7599a0000000p-1f,-0x1.76dd9e0000000p-3f}, {0x1.f70f640000000p-1f,-0x1.7d0a7c0000000p-3f}, {0x1.f6c3f80000000p-1f,-0x1.83366e0000000p-3f}, {0x1.f677560000000p-1f,-0x1.8961720000000p-3f},
{0x1.f6297c0000000p-1f,-0x1.8f8b840000000p-3f}, {0x1.f5da6e0000000p-1f,-0x1.95b49e0000000p-3f}, {0x1.f58a2c0000000p-1f,-0x1.9bdcc00000000p-3f}, {0x1.f538b20000000p-1f,-0x1.a203e20000000p-3f},
{0x1.f4e6040000000p-1f,-0x1.a82a020000000p-3f}, {0x1.f492200000000p-1f,-0x1.ae4f1e0000000p-3f}, {0x1.f43d080000000p-1f,-0x1.b4732e0000000p-3f}, {0x1.f3e6bc0000000p-1f,-0x1.ba96340000000p-3f},
{0x1.f38f3a0000000p-1f,-0x1.c0b8260000000p-3f}, {0x1.f336860000000p-1f,-0x1.c6d9060000000p-3f}, {0x1.f2dc9c0000000p-1f,-0x1.ccf8cc0000000p-3f}, {0x1.f281800000000p-1f,-0x1.d317740000000p-3f},
{0x1.f225300000000p-1f,-0x1.d934fe0000000p-3f}, {0x1.f1c7ac0000000p-1f,-0x1.df51640000000p-3f}, {0x1.f168f60000000p-1f,-0x1.e56ca20000000p-3f}, {0x1.f1090c0000000p-1f,-0x1.eb86b40000000p-3f},
{0x1.f0a7f00000000p-1f,-0x1.f19f980000000p-3f}, {0x1.f045a20000000p-1f,-0x1.f7b7480000000p-3f}, {0x1.efe2200000000p-1f,-0x1.fdcdc20000000p-3f}, {0x1.ef7d6e0000000p-1f,-0x1.01f1800000000p-2f},
{0x1.ef178a0000000p-1f,-0x1.04fb800000000p-2f}, {0x1.eeb0740000000p-1f,-0x1.0804e00000000p-2f}, {0x1.ee482e0000000p-1f,-0x1.0b0d9c0000000p-2f}, {0x1.eddeb60000000p-1f,-0x1.0e15b40000000p-2f},
{0x1.ed740e0000000p-1f,-0x1.111d260000000p-2f}, {0x1.ed08360000000p-1f,-0x1.1423ee0000000p-2f}, {0x1.ec9b2e0000000p-1f,-0x1.172a0e0000000p-2f}, {0x1.ec2cf40000000p-1f,-0x1.1a2f800000000p-2f},
{0x1.ebbd8c0000000p-1f,-0x1.1d34440000000p-2f}, {0x1.eb4cf60000000p-1f,-0x1.2038580000000p-2f}, {0x1.eadb2e0000000p-1f,-0x1.233bba0000000p-2f}, {0x1.ea683a0000000p-1f,-0x1.263e6a0000000p-2f},
{0x1.e9f4160000000p-1f,-0x1.2940620000000p-2f}, {0x1.e97ec40000000p-1f,-0x1.2c41a40000000p-2f}, {0x1.e908440000000p-1f,-0x1.2f422e0000000p-2f}, {0x1.e890960000000p-1f,-0x1.3241fc0000000p-2f},
{0x1.e817ba0000000p-1f,-0x1.35410c0000000p-2f}, {0x1.e79db20000000p-1f,-0x1.383f5e0000000p-2f}, {0x1.e7227e0000000p-1f,-0x1.3b3cf00000000p-2f}, {0x1.e6a61c0000000p-1f,-0x1.3e39be0000000p-2f},
{0x1.e6288e0000000p-1f,-0x1.4135ca0000000p-2f}, {0x1.e5a9d60000000p-1f,-0x1.44310e0000000p-2f}, {0x1.e529f00000000p-1f,-0x1.472b8a0000000p-2f}, {0x1.e4a8e00000000p-1f,-0x1.4a253e0000000p-2f},
{0x1.e426a40000000p-1f,-0x1.4d1e240000000p-2f}, {0x1.e3a33e0000000p-1f,-0x1.50163e0000000p-2f}, {0x1.e31eae0000000p-1f,-0x1.530d880000000p-2f}, {0x1.e298f40000000p-1f,-0x1.5604020000000p-2f},
{0x1.e212100000000p-1f,-0x1.58f9a80000000p-2f}, {0x1.e18a020000000p-1f,-0x1.5bee780000000p-2f}, {0x1.e100cc0000000p-1f,-0x1.5ee2740000000p-2f}, {0x1.e0766e0000000p-1f,-0x1.61d5960000000p-2f},
{0x1.dfeae60000000p-1f,-0x1.64c7de0000000p-2f}, {0x1.df5e360000000p-1f,-0x1.67b94a0000000p-2f}, {0x1.ded0600000000p-1f,-0x1.6aa9d80000000p-2f}, {0x1.de41600000000p-1f,-0x1.6d99860000000p-2f},
{0x1.ddb13c0000000p-1f,-0x1.7088540000000p-2f}, {0x1.dd1ff00000000p-1f,-0x1.73763c0000000p-2f}, {0x1.dc8d7c0000000p-1f,-0x1.7663400000000p-2f}, {0x1.dbf9e40000000p-1f,-0x1.794f5e0000000p-2f},
{0x1.db65260000000p-1f,-0x1.7c3a940000000p-2f}, {0x1.dacf420000000p-1f,-0x1.7f24de0000000p-2f}, {0x1.da383a0000000p-1f,-0x1.820e3c0000000p-2f}, {0x1.d9a00e0000000p-1f,-0x1.84f6aa0000000p-2f},
{0x1.d906bc0000000p-1f,-0x1.87de2a0000000p-2f}, {0x1.d86c480000000p-1f,-0x1.8ac4b80000000p-2f}, {0x1.d7d0b00000000p-1f,-0x1.8daa520000000p-2f}, {0x1.d733f60000000p-1f,-0x1.908ef80000000p-2f},
{0x1.d696180000000p-1f,-0x1.9372a60000000p-2f}, {0x1.d5f7180000000p-1f,-0x1.96555c0000000p-2f}, {0x1.d556f60000000p-1f,-0x1.9937160000000p-2f}, {0x1.d4b5b20000000p-1f,-0x1.9c17d40000000p-2f},
{0x1.d4134e0000000p-1f,-0x1.9ef7940000000p-2f}, {0x1.d36fc80000000p-1f,-0x1.a1d6540000000p-2f}, {0x1.d2cb220000000p-1f,-0x1.a4b4120000000p-2f}, {0x1.d2255c0000000p-1f,-0x1.a790ce0000000p-2f},
{0x1.d17e780000000p-1f,-0x1.aa6c820000000p-2f}, {0x1.d0d6720000000p-1f,-0x1.ad47320000000p-2f}, {0x1.d02d500000000p-1f,-0x1.b020d60000000p-2f}, {0x1.cf830e0000000p-1f,-0x1.b2f9720000000p-2f},
{0x1.ced7b00000000p-1f,-0x1.b5d1000000000p-2f}, {0x1.ce2b320000000p-1f,-0x1.b8a7820000000p-2f}, {0x1.cd7d980000000p-1f,-0x1.bb7cf20000000p-2f}, {0x1.cccee20000000p-1f,-0x1.be51520000000p-2f},
{0x1.cc1f100000000p-1f,-0x1.c1249e0000000p-2f}, {0x1.cb6e200000000p-1f,-0x1.c3f6d40000000p-2f}, {0x1.cabc160000000p-1f,-0x1.c6c7f40000000p-2f}, {0x1.ca08f20000000p-1f,-0x1.c997fc0000000p-2f},
{0x1.c954b20000000p-1f,-0x1.cc66ea0000000p-2f}, {0x1.c89f580000000p-1f,-0x1.cf34ba0000000p-2f}, {0x1.c7e8e60000000p-1f,-0x1.d2016e0000000p-2f}, {0x1.c731580000000p-1f,-0x1.d4cd020000000p-2f},
{0x1.c678b40000000p-1f,-0x1.d797760000000p-2f}, {0x1.c5bef60000000p-1f,-0x1.da60c60000000p-2f}, {0x1.c504200000000p-1f,-0x1.dd28f20000000p-2f}, {0x1.c448340000000p-1f,-0x1.dfeff60000000p-2f},
{0x1.c38b300000000p-1f,-0x1.e2b5d40000000p-2f}, {0x1.c2cd140000000p-1f,-0x1.e57a860000000p-2f}, {0x1.c20de40000000p-1f,-0x1.e83e0e0000000p-2f}, {0x1.c14d9e0000000p-1f,-0x1.eb006a0000000p-2f},
{0x1.c08c420000000p-1f,-0x1.edc1960000000p-2f}, {0x1.bfc9d20000000p-1f,-0x1.f081900000000p-2f}, {0x1.bf064e0000000p-1f,-0x1.f3405a0000000p-2f}, {0x1.be41b60000000p-1f,-0x1.f5fdee0000000p-2f},
{0x1.bd7c0a0000000p-1f,-0x1.f8ba4e0000000p-2f}, {0x1.bcb54c0000000p-1f,-0x1.fb75760000000p-2f}, {0x1.bbed7c0000000p-1f,-0x1.fe2f640000000p-2f}, {0x1.bb249a0000000p-1f,-0x1.00740c0000000p-1f},
{0x1.ba5aa60000000p-1f,-0x1.01cfc80000000p-1f}, {0x1.b98fa20000000p-1f,-0x1.032ae60000000p-1f}, {0x1.b8c38e0000000p-1f,-0x1.0485620000000p-1f}, {0x1.b7f6680000000p-1f,-0x1.05df3e0000000p-1f},
{0x1.b728340000000p-1f,-0x1.07387a0000000p-1f}, {0x1.b658f20000000p-1f,-0x1.0891120000000p-1f}, {0x1.b588a00000000p-1f,-0x1.09e9080000000p-1f}, {0x1.b4b7400000000p-1f,-0x1.0b40580000000p-1f},
{0x1.b3e4d40000000p-1f,-0x1.0c97040000000p-1f}, {0x1.b3115a0000000p-1f,-0x1.0ded0c0000000p-1f}, {0x1.b23cd40000000p-1f,-0x1.0f426c0000000p-1f}, {0x1.b167420000000p-1f,-0x1.1097240000000p-1f},
{0x1.b090a60000000p-1f,-0x1.11eb360000000p-1f}, {0x1.afb8fe0000000p-1f,-0x1.133e9c0000000p-1f}, {0x1.aee04c0000000p-1f,-0x1.14915a0000000p-1f}, {0x1.ae06900000000p-1f,-0x1.15e36e0000000p-1f},
{0x1.ad2bca0000000p-1f,-0x1.1734d60000000p-1f}, {0x1.ac4ffc0000000p-1f,-0x1.1885920000000p-1f}, {0x1.ab73260000000p-1f,-0x1.19d5a00000000p-1f}, {0x1.aa95480000000p-1f,-0x1.1b25020000000p-1f},
{0x1.a9b6620000000p-1f,-0x1.1c73b40000000p-1f}, {0x1.a8d6760000000p-1f,-0x1.1dc1b60000000p-1f}, {0x1.a7f5860000000p-1f,-0x1.1f0f080000000p-1f}, {0x1.a7138e0000000p-1f,-0x1.205baa0000000p-1f},
{0x1.a630920000000p-1f,-0x1.21a79a0000000p-1f}, {0x1.a54c920000000p-1f,-0x1.22f2d60000000p-1f}, {0x1.a4678c0000000p-1f,-0x1.243d600000000p-1f}, {0x1.a381840000000p-1f,-0x1.2587340000000p-1f},
{0x1.a29a7a0000000p-1f,-0x1.26d0540000000p-1f}, {0x1.a1b26e0000000p-1f,-0x1.2818be0000000p-1f}, {0x1.a0c95e0000000p-1f,-0x1.2960720000000p-1f}, {0x1.9fdf500000000p-1f,-0x1.2aa76e0000000p-1f},
{0x1.9ef43e0000000p-1f,-0x1.2bedb20000000p-1f}, {0x1.9e082e0000000p-1f,-0x1.2d333e0000000p-1f}, {0x1.9d1b200000000p-1f,-0x1.2e780e0000000p-1f}, {0x1.9c2d120000000p-1f,-0x1.2fbc240000000p-1f},
{0x1.9b3e040000000p-1f,-0x1.30ff800000000p-1f}, {0x1.9a4dfa0000000p-1f,-0x1.32421e0000000p-1f}, {0x1.995cf20000000p-1f,-0x1.3384000000000p-1f}, {0x1.986af00000000p-1f,-0x1.34c5260000000p-1f},
{0x1.9777f00000000p-1f,-0x1.36058c0000000p-1f}, {0x1.9683f40000000p-1f,-0x1.3745320000000p-1f}, {0x1.958efe0000000p-1f,-0x1.3884180000000p-1f}, {0x1.94990e0000000p-1f,-0x1.39c23e0000000p-1f},
{0x1.93a2240000000p-1f,-0x1.3affa20000000p-1f}, {0x1.92aa420000000p-1f,-0x1.3c3c440000000p-1f}, {0x1.91b1660000000p-1f,-0x1.3d78240000000p-1f}, {0x1.90b7940000000p-1f,-0x1.3eb33e0000000p-1f},
{0x1.8fbcca0000000p-1f,-0x1.3fed960000000p-1f}, {0x1.8ec10a0000000p-1f,-0x1.4127260000000p-1f}, {0x1.8dc4540000000p-1f,-0x1.425ff20000000p-1f}, {0x1.8cc6a80000000p-1f,-0x1.4397f60000000p-1f},
{0x1.8bc8060000000p-1f,-0x1.44cf320000000p-1f}, {0x1.8ac8720000000p-1f,-0x1.4605a60000000p-1f}, {0x1.89c7ea0000000p-1f,-0x1.473b520000000p-1f}, {0x1.88c66e0000000p-1f,-0x1.4870340000000p-1f},
{0x1.87c4000000000p-1f,-0x1.49a44a0000000p-1f}, {0x1.86c0a20000000p-1f,-0x1.4ad7960000000p-1f}, {0x1.85bc520000000p-1f,-0x1.4c0a140000000p-1f}, {0x1.84b7120000000p-1f,-0x1.4d3bc60000000p-1f},
{0x1.83b0e00000000p-1f,-0x1.4e6cac0000000p-1f}, {0x1.82a9c20000000p-1f,-0x1.4f9cc20000000p-1f}, {0x1.81a1b40000000p-1f,-0x1.50cc0a0000000p-1f}, {0x1.8098b80000000p-1f,-0x1.51fa820000000p-1f},
{0x1.7f8ece0000000p-1f,-0x1.53282a0000000p-1f}, {0x1.7e83f80000000p-1f,-0x1.5455000000000p-1f}, {0x1.7d78360000000p-1f,-0x1.5581040000000p-1f}, {0x1.7c6b8a0000000p-1f,-0x1.56ac360000000p-1f},
{0x1.7b5df20000000p-1f,-0x1.57d6940000000p-1f}, {0x1.7a4f700000000p-1f,-0x1.59001e0000000p-1f}, {0x1.7940060000000p-1f,-0x1.5a28d20000000p-1f}, {0x1.782fb20000000p-1f,-0x1.5b50b20000000p-1f},
{0x1.771e760000000p-1f,-0x1.5c77bc0000000p-1f}, {0x1.760c520000000p-1f,-0x1.5d9dee0000000p-1f}, {0x1.74f9480000000p-1f,-0x1.5ec34a0000000p-1f}, {0x1.73e5580000000p-1f,-0x1.5fe7cc0000000p-1f},
{0x1.72d0840000000p-1f,-0x1.610b760000000p-1f}, {0x1.71baca0000000p-1f,-0x1.622e440000000p-1f}, {0x1.70a42c0000000p-1f,-0x1.63503a0000000p-1f}, {0x1.6f8caa0000000p-1f,-0x1.6471540000000p-1f},
{0x1.6e74460000000p-1f,-0x1.6591920000000p-1f}, {0x1.6d5afe0000000p-1f,-0x1.66b0f40000000p-1f}, {0x1.6c40d80000000p-1f,-0x1.67cf780000000p-1f}, {0x1.6b25ce0000000p-1f,-0x1.68ed1e0000000p-1f},
{0x1.6a09e60000000p-1f,-0x1.6a09e60000000p-1f}, {0x1.68ed1e0000000p-1f,-0x1.6b25ce0000000p-1f}, {0x1.67cf780000000p-1f,-0x1.6c40d80000000p-1f}, {0x1.66b0f40000000p-1f,-0x1.6d5afe0000000p-1f},
{0x1.6591920000000p-1f,-0x1.6e74460000000p-1f}, {0x1.6471540000000p-1f,-0x1.6f8caa0000000p-1f}, {0x1.63503a0000000p-1f,-0x1.70a42c0000000p-1f}, {0x1.622e440000000p-1f,-0x1.71baca0000000p-1f},
{0x1.610b760000000p-1f,-0x1.72d0840000000p-1f}, {0x1.5fe7cc0000000p-1f,-0x1.73e5580000000p-1f}, {0x1.5ec34a0000000p-1f,-0x1.74f9480000000p-1f}, {0x1.5d9dee0000000p-1f,-0x1.760c520000000p-1f},
{0x1.5c77bc0000000p-1f,-0x1.771e760000000p-1f}, {0x1.5b50b20000000p-1f,-0x1.782fb20000000p-1f}, {0x1.5a28d20000000p-1f,-0x1.7940060000000p-1f}, {0x1.59001e0000000p-1f,-0x1.7a4f700000000p-1f},
{0x1.57d6940000000p-1f,-0x1.7b5df20000000p-1f}, {0x1.56ac360000000p-1f,-0x1.7c6b8a0000000p-1f}, {0x1.5581040000000p-1f,-0x1.7d78360000000p-1f}, {0x1.5455000000000p-1f,-0x1.7e83f80000000p-1f},
{0x1.53282a0000000p-1f,-0x1.7f8ece0000000p-1f}, {0x1.51fa820000000p-1f,-0x1.8098b80000000p-1f}, {0x1.50cc0a0000000p-1f,-0x1.81a1b40000000p-1f}, {0x1.4f9cc20000000p-1f,-0x1.82a9c20000000p-1f},
{0x1.4e6cac0000000p-1f,-0x1.83b0e00000000p-1f}, {0x1.4d3bc60000000p-1f,-0x1.84b7120000000p-1f}, {0x1.4c0a140000000p-1f,-0x1.85bc520000000p-1f}, {0x1.4ad7960000000p-1f,-0x1.86c0a20000000p-1f},
{0x1.49a44a0000000p-1f,-0x1.87c4000000000p-1f}, {0x1.4870340000000p-1f,-0x1.88c66e0000000p-1f}, {0x1.473b520000000p-1f,-0x1.89c7ea0000000p-1f}, {0x1.4605a60000000p-1f,-0x1.8ac8720000000p-1f},
{0x1.44cf320000000p-1f,-0x1.8bc8060000000p-1f}, {0x1.4397f60000000p-1f,-0x1.8cc6a80000000p-1f}, {0x1.425ff20000000p-1f,-0x1.8dc4540000000p-1f}, {0x1.4127260000000p-1f,-0x1.8ec10a0000000p-1f},
{0x1.3fed960000000p-1f,-0x1.8fbcca0000000p-1f}, {0x1.3eb33e0000000p-1f,-0x1.90b7940000000p-1f}, {0x1.3d78240000000p-1f,-0x1.91b1660000000p-1f}, {0x1.3c3c440000000p-1f,-0x1.92aa420000000p-1f},
{0x1.3affa20000000p-1f,-0x1.93a2240000000p-1f}, {0x1.39c23e0000000p-1f,-0x1.94990e0000000p-1f}, {0x1.3884180000000p-1f,-0x1.958efe0000000p-1f}, {0x1.3745320000000p-1f,-0x1.9683f40000000p-1f},
{0x1.36058c0000000p-1f,-0x1.9777f00000000p-1f}, {0x1.34c5260000000p-1f,-0x1.986af00000000p-1f}, {0x1.3384000000000p-1f,-0x1.995cf20000000p-1f}, {0x1.32421e0000000p-1f,-0x1.9a4dfa0000000p-1f},
{0x1.30ff800000000p-1f,-0x1.9b3e040000000p-1f}, {0x1.2fbc240000000p-1f,-0x1.9c2d120000000p-1f}, {0x1.2e780e0000000p-1f,-0x1.9d1b200000000p-1f}, {0x1.2d333e0000000p-1f,-0x1.9e082e0000000p-1f},
{0x1.2bedb20000000p-1f,-0x1.9ef43e0000000p-1f}, {0x1.2aa76e0000000p-1f,-0x1.9fdf500000000p-1f}, {0x1.2960720000000p-1f,-0x1.a0c95e0000000p-1f}, {0x1.2818be0000000p-1f,-0x1.a1b26e0000000p-1f},
{0x1.26d0540000000p-1f,-0x1.a29a7a0000000p-1f}, {0x1.2587340000000p-1f,-0x1.a381840000000p-1f}, {0x1.243d600000000p-1f,-0x1.a4678c0000000p-1f}, {0x1.22f2d60000000p-1f,-0x1.a54c920000000p-1f},
{0x1.21a79a0000000p-1f,-0x1.a630920000000p-1f}, {0x1.205baa0000000p-1f,-0x1.a7138e0000000p-1f}, {0x1.1f0f080000000p-1f,-0x1.a7f5860000000p-1f}, {0x1.1dc1b60000000p-1f,-0x1.a8d6760000000p-1f},
{0x1.1c73b40000000p-1f,-0x1.a9b6620000000p-1f}, {0x1.1b25020000000p-1f,-0x1.aa95480000000p-1f}, {0x1.19d5a00000000p-1f,-0x1.ab73260000000p-1f}, {0x1.1885920000000p-1f,-0x1.ac4ffc0000000p-1f},
{0x1.1734d60000000p-1f,-0x1.ad2bca0000000p-1f}, {0x1.15e36e0000000p-1f,-0x1.ae06900000000p-1f}, {0x1.14915a0000000p-1f,-0x1.aee04c0000000p-1f}, {0x1.133e9c0000000p-1f,-0x1.afb8fe0000000p-1f},
{0x1.11eb360000000p-1f,-0x1.b090a60000000p-1f}, {0x1.1097240000000p-1f,-0x1.b167420000000p-1f}, {0x1.0f426c0000000p-1f,-0x1.b23cd40000000p-1f}, {0x1.0ded0c0000000p-1f,-0x1.b3115a0000000p-1f},
{0x1.0c97040000000p-1f,-0x1.b3e4d40000000p-1f}, {0x1.0b40580000000p-1f,-0x1.b4b7400000000p-1f}, {0x1.09e9080000000p-1f,-0x1.b588a00000000p-1f}, {0x1.0891120000000p-1f,-0x1.b658f20000000p-1f},
{0x1.07387a0000000p-1f,-0x1.b728340000000p-1f}, {0x1.05df3e0000000p-1f,-0x1.b7f6680000000p-1f}, {0x1.0485620000000p-1f,-0x1.b8c38e0000000p-1f}, {0x1.032ae60000000p-1f,-0x1.b98fa20000000p-1f},
{0x1.01cfc80000000p-1f,-0x1.ba5aa60000000p-1f}, {0x1.00740c0000000p-1f,-0x1.bb249a0000000p-1f}, {0x1.fe2f640000000p-2f,-0x1.bbed7c0000000p-1f}, {0x1.fb75760000000p-2f,-0x1.bcb54c0000000p-1f},
{0x1.f8ba4e0000000p-2f,-0x1.bd7c0a0000000p-1f}, {0x1.f5fdee0000000p-2f,-0x1.be41b60000000p-1f}, {0x1.f3405a0000000p-2f,-0x1.bf064e0000000p-1f}, {0x1.f081900000000p-2f,-0x1.bfc9d20000000p-1f},
{0x1.edc1960000000p-2f,-0x1.c08c420000000p-1f}, {0x1.eb006a0000000p-2f,-0x1.c14d9e0000000p-1f}, {0x1.e83e0e0000000p-2f,-0x1.c20de40000000p-1f}, {0x1.e57a860000000p-2f,-0x1.c2cd140000000p-1f},
{0x1.e2b5d40000000p-2f,-0x1.c38b300000000p-1f}, {0x1.dfeff60000000p-2f,-0x1.c448340000000p-1f}, {0x1.dd28f20000000p-2f,-0x1.c504200000000p-1f}, {0x1.da60c60000000p-2f,-0x1.c5bef60000000p-1f},
{0x1.d797760000000p-2f,-0x1.c678b40000000p-1f}, {0x1.d4cd020000000p-2f,-0x1.c731580000000p-1f}, {0x1.d2016e0000000p-2f,-0x1.c7e8e60000000p-1f}, {0x1.cf34ba0000000p-2f,-0x1.c89f580000000p-1f},
{0x1.cc66ea0000000p-2f,-0x1.c954b20000000p-1f}, {0x1.c997fc0000000p-2f,-0x1.ca08f20000000p-1f}, {0x1.c6c7f40000000p-2f,-0x1.cabc160000000p-1f}, {0x1.c3f6d40000000p-2f,-0x1.cb6e200000000p-1f},
{0x1.c1249e0000000p-2f,-0x1.cc1f100000000p-1f}, {0x1.be51520000000p-2f,-0x1.cccee20000000p-1f}, {0x1.bb7cf20000000p-2f,-0x1.cd7d980000000p-1f}, {0x1.b8a7820000000p-2f,-0x1.ce2b320000000p-1f},
{0x1.b5d1000000000p-2f,-0x1.ced7b00000000p-1f}, {0x1.b2f9720000000p-2f,-0x1.cf830e0000000p-1f}, {0x1.b020d60000000p-2f,-0x1.d02d500000000p-1f}, {0x1.ad47320000000p-2f,-0x1.d0d6720000000p-1f},
{0x1.aa6c820000000p-2f,-0x1.d17e780000000p-1f}, {0x1.a790ce0000000p-2f,-0x1.d2255c0000000p-1f}, {0x1.a4b4120000000p-2f,-0x1.d2cb220000000p-1f}, {0x1.a1d6540000000p-2f,-0x1.d36fc80000000p-1f},
{0x1.9ef7940000000p-2f,-0x1.d4134e0000000p-1f}, {0x1.9c17d40000000p-2f,-0x1.d4b5b20000000p-1f}, {0x1.9937160000000p-2f,-0x1.d556f60000000p-1f}, {0x1.96555c0000000p-2f,-0x1.d5f7180000000p-1f},
{0x1.9372a60000000p-2f,-0x1.d696180000000p-1f}, {0x1.908ef80000000p-2f,-0x1.d733f60000000p-1f}, {0x1.8daa520000000p-2f,-0x1.d7d0b00000000p-1f}, {0x1.8ac4b80000000p-2f,-0x1.d86c480000000p-1f},
{0x1.87de2a0000000p-2f,-0x1.d906bc0000000p-1f}, {0x1.84f6aa0000000p-2f,-0x1.d9a00e0000000p-1f}, {0x1.820e3c0000000p-2f,-0x1.da383a0000000p-1f}, {0x1.7f24de0000000p-2f,-0x1.dacf420000000p-1f},
{0x1.7c3a940000000p-2f,-0x1.db65260000000p-1f}, {0x1.794f5e0000000p-2f,-0x1.dbf9e40000000p-1f}, {0x1.7663400000000p-2f,-0x1.dc8d7c0000000p-1f}, {0x1.73763c0000000p-2f,-0x1.dd1ff00000000p-1f},
{0x1.7088540000000p-2f,-0x1.ddb13c0000000p-1f}, {0x1.6d99860000000p-2f,-0x1.de41600000000p-1f}, {0x1.6aa9d80000000p-2f,-0x1.ded0600000000p-1f}, {0x1.67b94a0000000p-2f,-0x1.df5e360000000p-1f},
{0x1.64c7de0000000p-2f,-0x1.dfeae60000000p-1f}, {0x1.61d5960000000p-2f,-0x1.e0766e0000000p-1f}, {0x1.5ee2740000000p-2f,-0x1.e100cc0000000p-1f}, {0x1.5bee780000000p-2f,-0x1.e18a020000000p-1f},
{0x1.58f9a80000000p-2f,-0x1.e212100000000p-1f}, {0x1.5604020000000p-2f,-0x1.e298f40000000p-1f}, {0x1.530d880000000p-2f,-0x1.e31eae0000000p-1f}, {0x1.50163e0000000p-2f,-0x1.e3a33e0000000p-1f},
{0x1.4d1e240000000p-2f,-0x1.e426a40000000p-1f}, {0x1.4a253e0000000p-2f,-0x1.e4a8e00000000p-1f}, {0x1.472b8a0000000p-2f,-0x1.e529f00000000p-1f}, {0x1.44310e0000000p-2f,-0x1.e5a9d60000000p-1f},
{0x1.4135ca0000000p-2f,-0x1.e6288e0000000p-1f}, {0x1.3e39be0000000p-2f,-0x1.e6a61c0000000p-1f}, {0x1.3b3cf00000000p-2f,-0x1.e7227e0000000p-1f}, {0x1.383f5e0000000p-2f,-0x1.e79db20000000p-1f},
{0x1.35410c0000000p-2f,-0x1.e817ba0000000p-1f}, {0x1.3241fc0000000p-2f,-0x1.e890960000000p-1f}, {0x1.2f422e0000000p-2f,-0x1.e908440000000p-1f}, {0x1.2c41a40000000p-2f,-0x1.e97ec40000000p-1f},
{0x1.2940620000000p-2f,-0x1.e9f4160000000p-1f}, {0x1.263e6a0000000p-2f,-0x1.ea683a0000000p-1f}, {0x1.233bba0000000p-2f,-0x1.eadb2e0000000p-1f}, {0x1.2038580000000p-2f,-0x1.eb4cf60000000p-1f},
{0x1.1d34440000000p-2f,-0x1.ebbd8c0000000p-1f}, {0x1.1a2f800000000p-2f,-0x1.ec2cf40000000p-1f}, {0x1.172a0e0000000p-2f,-0x1.ec9b2e0000000p-1f}, {0x1.1423ee0000000p-2f,-0x1.ed08360000000p-1f},
{0x1.111d260000000p-2f,-0x1.ed740e0000000p-1f}, {0x1.0e15b40000000p-2f,-0x1.eddeb60000000p-1f}, {0x1.0b0d9c0000000p-2f,-0x1.ee482e0000000p-1f}, {0x1.0804e00000000p-2f,-0x1.eeb0740000000p-1f},
{0x1.04fb800000000p-2f,-0x1.ef178a0000000p-1f}, {0x1.01f1800000000p-2f,-0x1.ef7d6e0000000p-1f}, {0x1.fdcdc20000000p-3f,-0x1.efe2200000000p-1f}, {0x1.f7b7480000000p-3f,-0x1.f045a20000000p-1f},
{0x1.f19f980000000p-3f,-0x1.f0a7f00000000p-1f}, {0x1.eb86b40000000p-3f,-0x1.f1090c0000000p-1f}, {0x1.e56ca20000000p-3f,-0x1.f168f60000000p-1f}, {0x1.df51640000000p-3f,-0x1.f1c7ac0000000p-1f},
{0x1.d934fe0000000p-3f,-0x1.f225300000000p-1f}, {0x1.d317740000000p-3f,-0x1.f281800000000p-1f}, {0x1.ccf8cc0000000p-3f,-0x1.f2dc9c0000000p-1f}, {0x1.c6d9060000000p-3f,-0x1.f336860000000p-1f},
{0x1.c0b8260000000p-3f,-0x1.f38f3a0000000p-1f}, {0x1.ba96340000000p-3f,-0x1.f3e6bc0000000p-1f}, {0x1.b4732e0000000p-3f,-0x1.f43d080000000p-1f}, {0x1.ae4f1e0000000p-3f,-0x1.f492200000000p-1f},
{0x1.a82a020000000p-3f,-0x1.f4e6040000000p-1f}, {0x1.a203e20000000p-3f,-0x1.f538b20000000p-1f}, {0x1.9bdcc00000000p-3f,-0x1.f58a2c0000000p-1f}, {0x1.95b49e0000000p-3f,-0x1.f5da6e0000000p-1f},
{0x1.8f8b840000000p-3f,-0x1.f6297c0000000p-1f}, {0x1.8961720000000p-3f,-0x1.f677560000000p-1f}, {0x1.83366e0000000p-3f,-0x1.f6c3f80000000p-1f}, {0x1.7d0a7c0000000p-3f,-0x1.f70f640000000p-1f},
{0x1.76dd9e0000000p-3f,-0x1.f7599a0000000p-1f}, {0x1.70afd80000000p-3f,-0x1.f7a29a0000000p-1f}, {0x1.6a81300000000p-3f,-0x1.f7ea620000000p-1f}, {0x1.6451a80000000p-3f,-0x1.f830f40000000p-1f},
{0x1.5e21440000000p-3f,-0x1.f876500000000p-1f}, {0x1.57f0080000000p-3f,-0x1.f8ba740000000p-1f}, {0x1.51bdf80000000p-3f,-0x1.f8fd600000000p-1f}, {0x1.4b8b180000000p-3f,-0x1.f93f140000000p-1f},
{0x1.45576c0000000p-3f,-0x1.f97f920000000p-1f}, {0x1.3f22f60000000p-3f,-0x1.f9bed80000000p-1f}, {0x1.38edbc0000000p-3f,-0x1.f9fce60000000p-1f}, {0x1.32b7c00000000p-3f,-0x1.fa39ba0000000p-1f},
{0x1.2c81060000000p-3f,-0x1.fa75580000000p-1f}, {0x1.2649940000000p-3f,-0x1.faafbc0000000p-1f}, {0x1.20116e0000000p-3f,-0x1.fae8e80000000p-1f}, {0x1.19d8940000000p-3f,-0x1.fb20dc0000000p-1f},
{0x1.139f0c0000000p-3f,-0x1.fb57980000000p-1f}, {0x1.0d64dc0000000p-3f,-0x1.fb8d180000000p-1f}, {0x1.072a040000000p-3f,-0x1.fbc1620000000p-1f}, {0x1.00ee8a0000000p-3f,-0x1.fbf4700000000p-1f},
{0x1.f564e60000000p-4f,-0x1.fc26480000000p-1f}, {0x1.e8eb800000000p-4f,-0x1.fc56e40000000p-1f}, {0x1.dc70ec0000000p-4f,-0x1.fc86460000000p-1f}, {0x1.cff5340000000p-4f,-0x1.fcb4700000000p-1f},
{0x1.c3785c0000000p-4f,-0x1.fce1600000000p-1f}, {0x1.b6fa6e0000000p-4f,-0x1.fd0d160000000p-1f}, {0x1.aa7b720000000p-4f,-0x1.fd37920000000p-1f}, {0x1.9dfb6e0000000p-4f,-0x1.fd60d20000000p-1f},
{0x1.917a6c0000000p-4f,-0x1.fd88da0000000p-1f}, {0x1.84f8720000000p-4f,-0x1.fdafa80000000p-1f}, {0x1.7875860000000p-4f,-0x1.fdd53a0000000p-1f}, {0x1.6bf1b40000000p-4f,-0x1.fdf9920000000p-1f},
{0x1.5f6d000000000p-4f,-0x1.fe1cb00000000p-1f}, {0x1.52e7740000000p-4f,-0x1.fe3e920000000p-1f}, {0x1.4661180000000p-4f,-0x1.fe5f3a0000000p-1f}, {0x1.39d9f20000000p-4f,-0x1.fe7ea80000000p-1f},
{0x1.2d520a0000000p-4f,-0x1.fe9cda0000000p-1f}, {0x1.20c9680000000p-4f,-0x1.feb9d20000000p-1f}, {0x1.1440140000000p-4f,-0x1.fed58e0000000p-1f}, {0x1.07b6140000000p-4f,-0x1.fef0100000000p-1f},
{0x1.f656e80000000p-5f,-0x1.ff09560000000p-1f}, {0x1.dd40700000000p-5f,-0x1.ff21620000000p-1f}, {0x1.c428d20000000p-5f,-0x1.ff38300000000p-1f}, {0x1.ab101c0000000p-5f,-0x1.ff4dc60000000p-1f},
{0x1.91f6600000000p-5f,-0x1.ff621e0000000p-1f}, {0x1.78dbaa0000000p-5f,-0x1.ff753c0000000p-1f}, {0x1.5fc00e0000000p-5f,-0x1.ff871e0000000p-1f}, {0x1.46a3960000000p-5f,-0x1.ff97c40000000p-1f},
{0x1.2d86580000000p-5f,-0x1.ffa72e0000000p-1f}, {0x1.14685e0000000p-5f,-0x1.ffb55e0000000p-1f}, {0x1.f693740000000p-6f,-0x1.ffc2520000000p-1f}, {0x1.c454f40000000p-6f,-0x1.ffce0a0000000p-1f},
{0x1.9215600000000p-6f,-0x1.ffd8860000000p-1f}, {0x1.5fd4d20000000p-6f,-0x1.ffe1c60000000p-1f}, {0x1.2d936c0000000p-6f,-0x1.ffe9cc0000000p-1f}, {0x1.f6a2960000000p-7f,-0x1.fff0940000000p-1f},
{0x1.921d200000000p-7f,-0x1.fff6220000000p-1f}, {0x1.2d96b00000000p-7f,-0x1.fffa720000000p-1f}, {0x1.921f100000000p-8f,-0x1.fffd880000000p-1f}, {0x1.921f8c0000000p-9f,-0x1.ffff620000000p-1f},
{0x1.0000000000000p+0f,-0x0.0p+0f}, {0x1.fffd880000000p-1f,-0x1.921f100000000p-8f}, {0x1.fff6220000000p-1f,-0x1.921d200000000p-7f}, {0x1.ffe9cc0000000p-1f,-0x1.2d936c0000000p-6f},
{0x1.ffd8860000000p-1f,-0x1.9215600000000p-6f}, {0x1.ffc2520000000p-1f,-0x1.f693740000000p-6f}, {0x1.ffa72e0000000p-1f,-0x1.2d86580000000p-5f}, {0x1.ff871e0000000p-1f,-0x1.5fc00e0000000p-5f},
{0x1.ff621e0000000p-1f,-0x1.91f6600000000p-5f}, {0x1.ff38300000000p-1f,-0x1.c428d20000000p-5f}, {0x1.ff09560000000p-1f,-0x1.f656e80000000p-5f}, {0x1.fed58e0000000p-1f,-0x1.1440140000000p-4f},
{0x1.fe9cda0000000p-1f,-0x1.2d520a0000000p-4f}, {0x1.fe5f3a0000000p-1f,-0x1.4661180000000p-4f}, {0x1.fe1cb00000000p-1f,-0x1.5f6d000000000p-4f}, {0x1.fdd53a0000000p-1f,-0x1.7875860000000p-4f},
{0x1.fd88da0000000p-1f,-0x1.917a6c0000000p-4f}, {0x1.fd37920000000p-1f,-0x1.aa7b720000000p-4f}, {0x1.fce1600000000p-1f,-0x1.c3785c0000000p-4f}, {0x1.fc86460000000p-1f,-0x1.dc70ec0000000p-4f},
{0x1.fc26480000000p-1f,-0x1.f564e60000000p-4f}, {0x1.fbc1620000000p-1f,-0x1.072a040000000p-3f}, {0x1.fb57980000000p-1f,-0x1.139f0c0000000p-3f}, {0x1.fae8e80000000p-1f,-0x1.20116e0000000p-3f},
{0x1.fa75580000000p-1f,-0x1.2c81060000000p-3f}, {0x1.f9fce60000000p-1f,-0x1.38edbc0000000p-3f}, {0x1.f97f920000000p-1f,-0x1.45576c0000000p-3f}, {0x1.f8fd600000000p-1f,-0x1.51bdf80000000p-3f},
{0x1.f876500000000p-1f,-0x1.5e21440000000p-3f}, {0x1.f7ea620000000p-1f,-0x1.6a81300000000p-3f}, {0x1.f7599a0000000p-1f,-0x1.76dd9e0000000p-3f}, {0x1.f6c3f80000000p-1f,-0x1.83366e0000000p-3f},
{0x1.f6297c0000000p-1f,-0x1.8f8b840000000p-3f}, {0x1.f58a2c0000000p-1f,-0x1.9bdcc00000000p-3f}, {0x1.f4e6040000000p-1f,-0x1.a82a020000000p-3f}, {0x1.f43d080000000p-1f,-0x1.b4732e0000000p-3f},
{0x1.f38f3a0000000p-1f,-0x1.c0b8260000000p-3f}, {0x1.f2dc9c0000000p-1f,-0x1.ccf8cc0000000p-3f}, {0x1.f225300000000p-1f,-0x1.d934fe0000000p-3f}, {0x1.f168f60000000p-1f,-0x1.e56ca20000000p-3f},
{0x1.f0a7f00000000p-1f,-0x1.f19f980000000p-3f}, {0x1.efe2200000000p-1f,-0x1.fdcdc20000000p-3f}, {0x1.ef178a0000000p-1f,-0x1.04fb800000000p-2f}, {0x1.ee482e0000000p-1f,-0x1.0b0d9c0000000p-2f},
{0x1.ed740e0000000p-1f,-0x1.111d260000000p-2f}, {0x1.ec9b2e0000000p-1f,-0x1.172a0e0000000p-2f}, {0x1.ebbd8c0000000p-1f,-0x1.1d34440000000p-2f}, {0x1.eadb2e0000000p-1f,-0x1.233bba0000000p-2f},
{0x1.e9f4160000000p-1f,-0x1.2940620000000p-2f}, {0x1.e908440000000p-1f,-0x1.2f422e0000000p-2f}, {0x1.e817ba0000000p-1f,-0x1.35410c0000000p-2f}, {0x1.e7227e0000000p-1f,-0x1.3b3cf00000000p-2f},
{0x1.e6288e0000000p-1f,-0x1.4135ca0000000p-2f}, {0x1.e529f00000000p-1f,-0x1.472b8a0000000p-2f}, {0x1.e426a40000000p-1f,-0x1.4d1e240000000p-2f}, {0x1.e31eae0000000p-1f,-0x1.530d880000000p-2f},
{0x1.e212100000000p-1f,-0x1.58f9a80000000p-2f}, {0x1.e100cc0000000p-1f,-0x1.5ee2740000000p-2f}, {0x1.dfeae60000000p-1f,-0x1.64c7de0000000p-2f}, {0x1.ded0600000000p-1f,-0x1.6aa9d80000000p-2f},
{0x1.ddb13c0000000p-1f,-0x1.7088540000000p-2f}, {0x1.dc8d7c0000000p-1f,-0x1.7663400000000p-2f}, {0x1.db65260000000p-1f,-0x1.7c3a940000000p-2f}, {0x1.da383a0000000p-1f,-0x1.820e3c0000000p-2f},
{0x1.d906bc0000000p-1f,-0x1.87de2a0000000p-2f}, {0x1.d7d0b00000000p-1f,-0x1.8daa520000000p-2f}, {0x1.d696180000000p-1f,-0x1.9372a60000000p-2f}, {0x1.d556f60000000p-1f,-0x1.9937160000000p-2f},
{0x1.d4134e0000000p-1f,-0x1.9ef7940000000p-2f}, {0x1.d2cb220000000p-1f,-0x1.a4b4120000000p-2f}, {0x1.d17e780000000p-1f,-0x1.aa6c820000000p-2f}, {0x1.d02d500000000p-1f,-0x1.b020d60000000p-2f},
{0x1.ced7b00000000p-1f,-0x1.b5d1000000000p-2f}, {0x1.cd7d980000000p-1f,-0x1.bb7cf20000000p-2f}, {0x1.cc1f100000000p-1f,-0x1.c1249e0000000p-2f}, {0x1.cabc160000000p-1f,-0x1.c6c7f40000000p-2f},
{0x1.c954b20000000p-1f,-0x1.cc66ea0000000p-2f}, {0x1.c7e8e60000000p-1f,-0x1.d2016e0000000p-2f}, {0x1.c678b40000000p-1f,-0x1.d797760000000p-2f}, {0x1.c504200000000p-1f,-0x1.dd28f20000000p-2f},
{0x1.c38b300000000p-1f,-0x1.e2b5d40000000p-2f}, {0x1.c20de40000000p-1f,-0x1.e83e0e0000000p-2f}, {0x1.c08c420000000p-1f,-0x1.edc1960000000p-2f}, {0x1.bf064e0000000p-1f,-0x1.f3405a0000000p-2f},
{0x1.bd7c0a0000000p-1f,-0x1.f8ba4e0000000p-2f}, {0x1.bbed7c0000000p-1f,-0x1.fe2f640000000p-2f}, {0x1.ba5aa60000000p-1f,-0x1.01cfc80000000p-1f}, {0x1.b8c38e0000000p-1f,-0x1.0485620000000p-1f},
{0x1.b728340000000p-1f,-0x1.07387a0000000p-1f}, {0x1.b588a00000000p-1f,-0x1.09e9080000000p-1f}, {0x1.b3e4d40000000p-1f,-0x1.0c97040000000p-1f}, {0x1.b23cd40000000p-1f,-0x1.0f426c0000000p-1f},
{0x1.b090a60000000p-1f,-0x1.11eb360000000p-1f}, {0x1.aee04c0000000p-1f,-0x1.14915a0000000p-1f}, {0x1.ad2bca0000000p-1f,-0x1.1734d60000000p-1f}, {0x1.ab73260000000p-1f,-0x1.19d5a00000000p-1f},
{0x1.a9b6620000000p-1f,-0x1.1c73b40000000p-1f}, {0x1.a7f5860000000p-1f,-0x1.1f0f080000000p-1f}, {0x1.a630920000000p-1f,-0x1.21a79a0000000p-1f}, {0x1.a4678c0000000p-1f,-0x1.243d600000000p-1f},
{0x1.a29a7a0000000p-1f,-0x1.26d0540000000p-1f}, {0x1.a0c95e0000000p-1f,-0x1.2960720000000p-1f}, {0x1.9ef43e0000000p-1f,-0x1.2bedb20000000p-1f}, {0x1.9d1b200000000p-1f,-0x1.2e780e0000000p-1f},
{0x1.9b3e040000000p-1f,-0x1.30ff800000000p-1f}, {0x1.995cf20000000p-1f,-0x1.3384000000000p-1f}, {0x1.9777f00000000p-1f,-0x1.36058c0000000p-1f}, {0x1.958efe0000000p-1f,-0x1.3884180000000p-1f},
{0x1.93a2240000000p-1f,-0x1.3affa20000000p-1f}, {0x1.91b1660000000p-1f,-0x1.3d78240000000p-1f}, {0x1.8fbcca0000000p-1f,-0x1.3fed960000000p-1f}, {0x1.8dc4540000000p-1f,-0x1.425ff20000000p-1f},
{0x1.8bc8060000000p-1f,-0x1.44cf320000000p-1f}, {0x1.89c7ea0000000p-1f,-0x1.473b520000000p-1f}, {0x1.87c4000000000p-1f,-0x1.49a44a0000000p-1f}, {0x1.85bc520000000p-1f,-0x1.4c0a140000000p-1f},
{0x1.83b0e00000000p-1f,-0x1.4e6cac0000000p-1f}, {0x1.81a1b40000000p-1f,-0x1.50cc0a0000000p-1f}, {0x1.7f8ece0000000p-1f,-0x1.53282a0000000p-1f}, {0x1.7d78360000000p-1f,-0x1.5581040000000p-1f},
{0x1.7b5df20000000p-1f,-0x1.57d6940000000p-1f}, {0x1.7940060000000p-1f,-0x1.5a28d20000000p-1f}, {0x1.771e760000000p-1f,-0x1.5c77bc0000000p-1f}, {0x1.74f9480000000p-1f,-0x1.5ec34a0000000p-1f},
{0x1.72d0840000000p-1f,-0x1.610b760000000p-1f}, {0x1.70a42c0000000p-1f,-0x1.63503a0000000p-1f}, {0x1.6e74460000000p-1f,-0x1.6591920000000p-1f}, {0x1.6c40d80000000p-1f,-0x1.67cf780000000p-1f},
{0x1.6a09e60000000p-1f,-0x1.6a09e60000000p-1f}, {0x1.67cf780000000p-1f,-0x1.6c40d80000000p-1f}, {0x1.6591920000000p-1f,-0x1.6e74460000000p-1f}, {0x1.63503a0000000p-1f,-0x1.70a42c0000000p-1f},
{0x1.610b760000000p-1f,-0x1.72d0840000000p-1f}, {0x1.5ec34a0000000p-1f,-0x1.74f9480000000p-1f}, {0x1.5c77bc0000000p-1f,-0x1.771e760000000p-1f}, {0x1.5a28d20000000p-1f,-0x1.7940060000000p-1f},
{0x1.57d6940000000p-1f,-0x1.7b5df20000000p-1f}, {0x1.5581040000000p-1f,-0x1.7d78360000000p-1f}, {0x1.53282a0000000p-1f,-0x1.7f8ece0000000p-1f}, {0x1.50cc0a0000000p-1f,-0x1.81a1b40000000p-1f},
{0x1.4e6cac0000000p-1f,-0x1.83b0e00000000p-1f}, {0x1.4c0a140000000p-1f,-0x1.85bc520000000p-1f}, {0x1.49a44a0000000p-1f,-0x1.87c4000000000p-1f}, {0x1.473b520000000p-1f,-0x1.89c7ea0000000p-1f},
{0x1.44cf320000000p-1f,-0x1.8bc8060000000p-1f}, {0x1.425ff20000000p-1f,-0x1.8dc4540000000p-1f}, {0x1.3fed960000000p-1f,-0x1.8fbcca0000000p-1f}, {0x1.3d78240000000p-1f,-0x1.91b1660000000p-1f},
{0x1.3affa20000000p-1f,-0x1.93a2240000000p-1f}, {0x1.3884180000000p-1f,-0x1.958efe0000000p-1f}, {0x1.36058c0000000p-1f,-0x1.9777f00000000p-1f}, {0x1.3384000000000p-1f,-0x1.995cf20000000p-1f},
{0x1.30ff800000000p-1f,-0x1.9b3e040000000p-1f}, {0x1.2e780e0000000p-1f,-0x1.9d1b200000000p-1f}, {0x1.2bedb20000000p-1f,-0x1.9ef43e0000000p-1f}, {0x1.2960720000000p-1f,-0x1.a0c95e0000000p-1f},
{0x1.26d0540000000p-1f,-0x1.a29a7a0000000p-1f}, {0x1.243d600000000p-1f,-0x1.a4678c0000000p-1f}, {0x1.21a79a0000000p-1f,-0x1.a630920000000p-1f}, {0x1.1f0f080000000p-1f,-0x1.a7f5860000000p-1f},
{0x1.1c73b40000000p-1f,-0x1.a9b6620000000p-1f}, {0x1.19d5a00000000p-1f,-0x1.ab73260000000p-1f}, {0x1.1734d60000000p-1f,-0x1.ad2bca0000000p-1f}, {0x1.14915a0000000p-1f,-0x1.aee04c0000000p-1f},
{0x1.11eb360000000p-1f,-0x1.b090a60000000p-1f}, {0x1.0f426c0000000p-1f,-0x1.b23cd40000000p-1f}, {0x1.0c97040000000p-1f,-0x1.b3e4d40000000p-1f}, {0x1.09e9080000000p-1f,-0x1.b588a00000000p-1f},
{0x1.07387a0000000p-1f,-0x1.b728340000000p-1f}, {0x1.0485620000000p-1f,-0x1.b8c38e0000000p-1f}, {0x1.01cfc80000000p-1f,-0x1.ba5aa60000000p-1f}, {0x1.fe2f640000000p-2f,-0x1.bbed7c0000000p-1f},
{0x1.f8ba4e0000000p-2f,-0x1.bd7c0a0000000p-1f}, {0x1.f3405a0000000p-2f,-0x1.bf064e0000000p-1f}, {0x1.edc1960000000p-2f,-0x1.c08c420000000p-1f}, {0x1.e83e0e0000000p-2f,-0x1.c20de40000000p-1f},
{0x1.e2b5d40000000p-2f,-0x1.c38b300000000p-1f}, {0x1.dd28f20000000p-2f,-0x1.c504200000000p-1f}, {0x1.d797760000000p-2f,-0x1.c678b40000000p-1f}, {0x1.d2016e0000000p-2f,-0x1.c7e8e60000000p-1f},
{0x1.cc66ea0000000p-2f,-0x1.c954b20000000p-1f}, {0x1.c6c7f40000000p-2f,-0x1.cabc160000000p-1f}, {0x1.c1249e0000000p-2f,-0x1.cc1f100000000p-1f}, {0x1.bb7cf20000000p-2f,-0x1.cd7d980000000p-1f},
{0x1.b5d1000000000p-2f,-0x1.ced7b00000000p-1f}, {0x1.b020d60000000p-2f,-0x1.d02d500000000p-1f}, {0x1.aa6c820000000p-2f,-0x1.d17e780000000p-1f}, {0x1.a4b4120000000p-2f,-0x1.d2cb220000000p-1f},
{0x1.9ef7940000000p-2f,-0x1.d4134e0000000p-1f}, {0x1.9937160000000p-2f,-0x1.d556f60000000p-1f}, {0x1.9372a60000000p-2f,-0x1.d696180000000p-1f}, {0x1.8daa520000000p-2f,-0x1.d7d0b00000000p-1f},
{0x1.87de2a0000000p-2f,-0x1.d906bc0000000p-1f}, {0x1.820e3c0000000p-2f,-0x1.da383a0000000p-1f}, {0x1.7c3a940000000p-2f,-0x1.db65260000000p-1f}, {0x1.7663400000000p-2f,-0x1.dc8d7c0000000p-1f},
{0x1.7088540000000p-2f,-0x1.ddb13c0000000p-1f}, {0x1.6aa9d80000000p-2f,-0x1.ded0600000000p-1f}, {0x1.64c7de0000000p-2f,-0x1.dfeae60000000p-1f}, {0x1.5ee2740000000p-2f,-0x1.e100cc0000000p-1f},
{0x1.58f9a80000000p-2f,-0x1.e212100000000p-1f}, {0x1.530d880000000p-2f,-0x1.e31eae0000000p-1f}, {0x1.4d1e240000000p-2f,-0x1.e426a40000000p-1f}, {0x1.472b8a0000000p-2f,-0x1.e529f00000000p-1f},
{0x1.4135ca0000000p-2f,-0x1.e6288e0000000p-1f}, {0x1.3b3cf00000000p-2f,-0x1.e7227e0000000p-1f}, {0x1.35410c0000000p-2f,-0x1.e817ba0000000p-1f}, {0x1.2f422e0000000p-2f,-0x1.e908440000000p-1f},
{0x1.2940620000000p-2f,-0x1.e9f4160000000p-1f}, {0x1.233bba0000000p-2f,-0x1.eadb2e0000000p-1f}, {0x1.1d34440000000p-2f,-0x1.ebbd8c0000000p-1f}, {0x1.172a0e0000000p-2f,-0x1.ec9b2e0000000p-1f},
{0x1.111d260000000p-2f,-0x1.ed740e0000000p-1f}, {0x1.0b0d9c0000000p-2f,-0x1.ee482e0000000p-1f}, {0x1.04fb800000000p-2f,-0x1.ef178a0000000p-1f}, {0x1.fdcdc20000000p-3f,-0x1.efe2200000000p-1f},
{0x1.f19f980000000p-3f,-0x1.f0a7f00000000p-1f}, {0x1.e56ca20000000p-3f,-0x1.f168f60000000p-1f}, {0x1.d934fe0000000p-3f,-0x1.f225300000000p-1f}, {0x1.ccf8cc0000000p-3f,-0x1.f2dc9c0000000p-1f},
{0x1.c0b8260000000p-3f,-0x1.f38f3a0000000p-1f}, {0x1.b4732e0000000p-3f,-0x1.f43d080000000p-1f}, {0x1.a82a020000000p-3f,-0x1.f4e6040000000p-1f}, {0x1.9bdcc00000000p-3f,-0x1.f58a2c0000000p-1f},
{0x1.8f8b840000000p-3f,-0x1.f6297c0000000p-1f}, {0x1.83366e0000000p-3f,-0x1.f6c3f80000000p-1f}, {0x1.76dd9e0000000p-3f,-0x1.f7599a0000000p-1f}, {0x1.6a81300000000p-3f,-0x1.f7ea620000000p-1f},
{0x1.5e21440000000p-3f,-0x1.f876500000000p-1f}, {0x1.51bdf80000000p-3f,-0x1.f8fd600000000p-1f}, {0x1.45576c0000000p-3f,-0x1.f97f920000000p-1f}, {0x1.38edbc0000000p-3f,-0x1.f9fce60000000p-1f},
{0x1.2c81060000000p-3f,-0x1.fa75580000000p-1f}, {0x1.20116e0000000p-3f,-0x1.fae8e80000000p-1f}, {0x1.139f0c0000000p-3f,-0x1.fb57980000000p-1f}, {0x1.072a040000000p-3f,-0x1.fbc1620000000p-1f},
{0x1.f564e60000000p-4f,-0x1.fc26480000000p-1f}, {0x1.dc70ec0000000p-4f,-0x1.fc86460000000p-1f}, {0x1.c3785c0000000p-4f,-0x1.fce1600000000p-1f}, {0x1.aa7b720000000p-4f,-0x1.fd37920000000p-1f},
{0x1.917a6c0000000p-4f,-0x1.fd88da0000000p-1f}, {0x1.7875860000000p-4f,-0x1.fdd53a0000000p-1f}, {0x1.5f6d000000000p-4f,-0x1.fe1cb00000000p-1f}, {0x1.4661180000000p-4f,-0x1.fe5f3a0000000p-1f},
{0x1.2d520a0000000p-4f,-0x1.fe9cda0000000p-1f}, {0x1.1440140000000p-4f,-0x1.fed58e0000000p-1f}, {0x1.f656e80000000p-5f,-0x1.ff09560000000p-1f}, {0x1.c428d20000000p-5f,-0x1.ff38300000000p-1f},
{0x1.91f6600000000p-5f,-0x1.ff621e0000000p-1f}, {0x1.5fc00e0000000p-5f,-0x1.ff871e0000000p-1f}, {0x1.2d86580000000p-5f,-0x1.ffa72e0000000p-1f}, {0x1.f693740000000p-6f,-0x1.ffc2520000000p-1f},
{0x1.9215600000000p-6f,-0x1.ffd8860000000p-1f}, {0x1.2d936c0000000p-6f,-0x1.ffe9cc0000000p-1f}, {0x1.921d200000000p-7f,-0x1.fff6220000000p-1f}, {0x1.921f100000000p-8f,-0x1.fffd880000000p-1f},
{0x1.1a62640000000p-54f,-0x1.0000000000000p+0f}, {-0x1.921f100000000p-8f,-0x1.fffd880000000p-1f}, {-0x1.921d200000000p-7f,-0x1.fff6220000000p-1f}, {-0x1.2d936c0000000p-6f,-0x1.ffe9cc0000000p-1f},
{-0x1.9215600000000p-6f,-0x1.ffd8860000000p-1f}, {-0x1.f693740000000p-6f,-0x1.ffc2520000000p-1f}, {-0x1.2d86580000000p-5f,-0x1.ffa72e0000000p-1f}, {-0x1.5fc00e0000000p-5f,-0x1.ff871e0000000p-1f},
{-0x1.91f6600000000p-5f,-0x1.ff621e0000000p-1f}, {-0x1.c428d20000000p-5f,-0x1.ff38300000000p-1f}, {-0x1.f656e80000000p-5f,-0x1.ff09560000000p-1f}, {-0x1.1440140000000p-4f,-0x1.fed58e0000000p-1f},
{-0x1.2d520a0000000p-4f,-0x1.fe9cda0000000p-1f}, {-0x1.4661180000000p-4f,-0x1.fe5f3a0000000p-1f}, {-0x1.5f6d000000000p-4f,-0x1.fe1cb00000000p-1f}, {-0x1.7875860000000p-4f,-0x1.fdd53a0000000p-1f},
{-0x1.917a6c0000000p-4f,-0x1.fd88da0000000p-1f}, {-0x1.aa7b720000000p-4f,-0x1.fd37920000000p-1f}, {-0x1.c3785c0000000p-4f,-0x1.fce1600000000p-1f}, {-0x1.dc70ec0000000p-4f,-0x1.fc86460000000p-1f},
{-0x1.f564e60000000p-4f,-0x1.fc26480000000p-1f}, {-0x1.072a040000000p-3f,-0x1.fbc1620000000p-1f}, {-0x1.139f0c0000000p-3f,-0x1.fb57980000000p-1f}, {-0x1.20116e0000000p-3f,-0x1.fae8e80000000p-1f},
{-0x1.2c81060000000p-3f,-0x1.fa75580000000p-1f}, {-0x1.38edbc0000000p-3f,-0x1.f9fce60000000p-1f}, {-0x1.45576c0000000p-3f,-0x1.f97f920000000p-1f}, {-0x1.51bdf80000000p-3f,-0x1.f8fd600000000p-1f},
{-0x1.5e21440000000p-3f,-0x1.f876500000000p-1f}, {-0x1.6a81300000000p-3f,-0x1.f7ea620000000p-1f}, {-0x1.76dd9e0000000p-3f,-0x1.f7599a0000000p-1f}, {-0x1.83366e0000000p-3f,-0x1.f6c3f80000000p-1f},
{-0x1.8f8b840000000p-3f,-0x1.f6297c0000000p-1f}, {-0x1.9bdcc00000000p-3f,-0x1.f58a2c0000000p-1f}, {-0x1.a82a020000000p-3f,-0x1.f4e6040000000p-1f}, {-0x1.b4732e0000000p-3f,-0x1.f43d080000000p-1f},
{-0x1.c0b8260000000p-3f,-0x1.f38f3a0000000p-1f}, {-0x1.ccf8cc0000000p-3f,-0x1.f2dc9c0000000p-1f}, {-0x1.d934fe0000000p-3f,-0x1.f225300000000p-1f}, {-0x1.e56ca20000000p-3f,-0x1.f168f60000000p-1f},
{-0x1.f19f980000000p-3f,-0x1.f0a7f00000000p-1f}, {-0x1.fdcdc20000000p-3f,-0x1.efe2200000000p-1f}, {-0x1.04fb800000000p-2f,-0x1.ef178a0000000p-1f}, {-0x1.0b0d9c0000000p-2f,-0x1.ee482e0000000p-1f},
{-0x1.111d260000000p-2f,-0x1.ed740e0000000p-1f}, {-0x1.172a0e0000000p-2f,-0x1.ec9b2e0000000p-1f}, {-0x1.1d34440000000p-2f,-0x1.ebbd8c0000000p-1f}, {-0x1.233bba0000000p-2f,-0x1.eadb2e0000000p-1f},
{-0x1.2940620000000p-2f,-0x1.e9f4160000000p-1f}, {-0x1.2f422e0000000p-2f,-0x1.e908440000000p-1f}, {-0x1.35410c0000000p-2f,-0x1.e817ba0000000p-1f}, {-0x1.3b3cf00000000p-2f,-0x1.e7227e0000000p-1f},
{-0x1.4135ca0000000p-2f,-0x1.e6288e0000000p-1f}, {-0x1.472b8a0000000p-2f,-0x1.e529f00000000p-1f}, {-0x1.4d1e240000000p-2f,-0x1.e426a40000000p-1f}, {-0x1.530d880000000p-2f,-0x1.e31eae0000000p-1f},
{-0x1.58f9a80000000p-2f,-0x1.e212100000000p-1f}, {-0x1.5ee2740000000p-2f,-0x1.e100cc0000000p-1f}, {-0x1.64c7de0000000p-2f,-0x1.dfeae60000000p-1f}, {-0x1.6aa9d80000000p-2f,-0x1.ded0600000000p-1f},
{-0x1.7088540000000p-2f,-0x1.ddb13c0000000p-1f}, {-0x1.7663400000000p-2f,-0x1.dc8d7c0000000p-1f}, {-0x1.7c3a940000000p-2f,-0x1.db65260000000p-1f}, {-0x1.820e3c0000000p-2f,-0x1.da383a0000000p-1f},
{-0x1.87de2a0000000p-2f,-0x1.d906bc0000000p-1f}, {-0x1.8daa520000000p-2f,-0x1.d7d0b00000000p-1f}, {-0x1.9372a60000000p-2f,-0x1.d696180000000p-1f}, {-0x1.9937160000000p-2f,-0x1.d556f60000000p-1f},
{-0x1.9ef7940000000p-2f,-0x1.d4134e0000000p-1f}, {-0x1.a4b4120000000p-2f,-0x1.d2cb220000000p-1f}, {-0x1.aa6c820000000p-2f,-0x1.d17e780000000p-1f}, {-0x1.b020d60000000p-2f,-0x1.d02d500000000p-1f},
{-0x1.b5d1000000000p-2f,-0x1.ced7b00000000p-1f}, {-0x1.bb7cf20000000p-2f,-0x1.cd7d980000000p-1f}, {-0x1.c1249e0000000p-2f,-0x1.cc1f100000000p-1f}, {-0x1.c6c7f40000000p-2f,-0x1.cabc160000000p-1f},
{-0x1.cc66ea0000000p-2f,-0x1.c954b20000000p-1f}, {-0x1.d2016e0000000p-2f,-0x1.c7e8e60000000p-1f}, {-0x1.d797760000000p-2f,-0x1.c678b40000000p-1f}, {-0x1.dd28f20000000p-2f,-0x1.c504200000000p-1f},
{-0x1.e2b5d40000000p-2f,-0x1.c38b300000000p-1f}, {-0x1.e83e0e0000000p-2f,-0x1.c20de40000000p-1f}, {-0x1.edc1960000000p-2f,-0x1.c08c420000000p-1f}, {-0x1.f3405a0000000p-2f,-0x1.bf064e0000000p-1f},
{-0x1.f8ba4e0000000p-2f,-0x1.bd7c0a0000000p-1f}, {-0x1.fe2f640000000p-2f,-0x1.bbed7c0000000p-1f}, {-0x1.01cfc80000000p-1f,-0x1.ba5aa60000000p-1f}, {-0x1.0485620000000p-1f,-0x1.b8c38e0000000p-1f},
{-0x1.07387a0000000p-1f,-0x1.b728340000000p-1f}, {-0x1.09e9080000000p-1f,-0x1.b588a00000000p-1f}, {-0x1.0c97040000000p-1f,-0x1.b3e4d40000000p-1f}, {-0x1.0f426c0000000p-1f,-0x1.b23cd40000000p-1f},
{-0x1.11eb360000000p-1f,-0x1.b090a60000000p-1f}, {-0x1.14915a0000000p-1f,-0x1.aee04c0000000p-1f}, {-0x1.1734d60000000p-1f,-0x1.ad2bca0000000p-1f}, {-0x1.19d5a00000000p-1f,-0x1.ab73260000000p-1f},
{-0x1.1c73b40000000p-1f,-0x1.a9b6620000000p-1f}, {-0x1.1f0f080000000p-1f,-0x1.a7f5860000000p-1f}, {-0x1.21a79a0000000p-1f,-0x1.a630920000000p-1f}, {-0x1.243d600000000p-1f,-0x1.a4678c0000000p-1f},
{-0x1.26d0540000000p-1f,-0x1.a29a7a0000000p-1f}, {-0x1.2960720000000p-1f,-0x1.a0c95e0000000p-1f}, {-0x1.2bedb20000000p-1f,-0x1.9ef43e0000000p-1f}, {-0x1.2e780e0000000p-1f,-0x1.9d1b200000000p-1f},
{-0x1.30ff800000000p-1f,-0x1.9b3e040000000p-1f}, {-0x1.3384000000000p-1f,-0x1.995cf20000000p-1f}, {-0x1.36058c0000000p-1f,-0x1.9777f00000000p-1f}, {-0x1.3884180000000p-1f,-0x1.958efe0000000p-1f},
{-0x1.3affa20000000p-1f,-0x1.93a2240000000p-1f}, {-0x1.3d78240000000p-1f,-0x1.91b1660000000p-1f}, {-0x1.3fed960000000p-1f,-0x1.8fbcca0000000p-1f}, {-0x1.425ff20000000p-1f,-0x1.8dc4540000000p-1f},
{-0x1.44cf320000000p-1f,-0x1.8bc8060000000p-1f}, {-0x1.473b520000000p-1f,-0x1.89c7ea0000000p-1f}, {-0x1.49a44a0000000p-1f,-0x1.87c4000000000p-1f}, {-0x1.4c0a140000000p-1f,-0x1.85bc520000000p-1f},
{-0x1.4e6cac0000000p-1f,-0x1.83b0e00000000p-1f}, {-0x1.50cc0a0000000p-1f,-0x1.81a1b40000000p-1f}, {-0x1.53282a0000000p-1f,-0x1.7f8ece0000000p-1f}, {-0x1.5581040000000p-1f,-0x1.7d78360000000p-1f},
{-0x1.57d6940000000p-1f,-0x1.7b5df20000000p-1f}, {-0x1.5a28d20000000p-1f,-0x1.7940060000000p-1f}, {-0x1.5c77bc0000000p-1f,-0x1.771e760000000p-1f}, {-0x1.5ec34a0000000p-1f,-0x1.74f9480000000p-1f},
{-0x1.610b760000000p-1f,-0x1.72d0840000000p-1f}, {-0x1.63503a0000000p-1f,-0x1.70a42c0000000p-1f}, {-0x1.6591920000000p-1f,-0x1.6e74460000000p-1f}, {-0x1.67cf780000000p-1f,-0x1.6c40d80000000p-1f},
{-0x1.6a09e60000000p-1f,-0x1.6a09e60000000p-1f}, {-0x1.6c40d80000000p-1f,-0x1.67cf780000000p-1f}, {-0x1.6e74460000000p-1f,-0x1.6591920000000p-1f}, {-0x1.70a42c0000000p-1f,-0x1.63503a0000000p-1f},
{-0x1.72d0840000000p-1f,-0x1.610b760000000p-1f}, {-0x1.74f9480000000p-1f,-0x1.5ec34a0000000p-1f}, {-0x1.771e760000000p-1f,-0x1.5c77bc0000000p-1f}, {-0x1.7940060000000p-1f,-0x1.5a28d20000000p-1f},
{-0x1.7b5df20000000p-1f,-0x1.57d6940000000p-1f}, {-0x1.7d78360000000p-1f,-0x1.5581040000000p-1f}, {-0x1.7f8ece0000000p-1f,-0x1.53282a0000000p-1f}, {-0x1.81a1b40000000p-1f,-0x1.50cc0a0000000p-1f},
{-0x1.83b0e00000000p-1f,-0x1.4e6cac0000000p-1f}, {-0x1.85bc520000000p-1f,-0x1.4c0a140000000p-1f}, {-0x1.87c4000000000p-1f,-0x1.49a44a0000000p-1f}, {-0x1.89c7ea0000000p-1f,-0x1.473b520000000p-1f},
{-0x1.8bc8060000000p-1f,-0x1.44cf320000000p-1f}, {-0x1.8dc4540000000p-1f,-0x1.425ff20000000p-1f}, {-0x1.8fbcca0000000p-1f,-0x1.3fed960000000p-1f}, {-0x1.91b1660000000p-1f,-0x1.3d78240000000p-1f},
{-0x1.93a2240000000p-1f,-0x1.3affa20000000p-1f}, {-0x1.958efe0000000p-1f,-0x1.3884180000000p-1f}, {-0x1.9777f00000000p-1f,-0x1.36058c0000000p-1f}, {-0x1.995cf20000000p-1f,-0x1.3384000000000p-1f},
{-0x1.9b3e040000000p-1f,-0x1.30ff800000000p-1f}, {-0x1.9d1b200000000p-1f,-0x1.2e780e0000000p-1f}, {-0x1.9ef43e0000000p-1f,-0x1.2bedb20000000p-1f}, {-0x1.a0c95e0000000p-1f,-0x1.2960720000000p-1f},
{-0x1.a29a7a0000000p-1f,-0x1.26d0540000000p-1f}, {-0x1.a4678c0000000p-1f,-0x1.243d600000000p-1f}, {-0x1.a630920000000p-1f,-0x1.21a79a0000000p-1f}, {-0x1.a7f5860000000p-1f,-0x1.1f0f080000000p-1f},
{-0x1.a9b6620000000p-1f,-0x1.1c73b40000000p-1f}, {-0x1.ab73260000000p-1f,-0x1.19d5a00000000p-1f}, {-0x1.ad2bca0000000p-1f,-0x1.1734d60000000p-1f}, {-0x1.aee04c0000000p-1f,-0x1.14915a0000000p-1f},
{-0x1.b090a60000000p-1f,-0x1.11eb360000000p-1f}, {-0x1.b23cd40000000p-1f,-0x1.0f426c0000000p-1f}, {-0x1.b3e4d40000000p-1f,-0x1.0c97040000000p-1f}, {-0x1.b588a00000000p-1f,-0x1.09e9080000000p-1f},
{-0x1.b728340000000p-1f,-0x1.07387a0000000p-1f}, {-0x1.b8c38e0000000p-1f,-0x1.0485620000000p-1f}, {-0x1.ba5aa60000000p-1f,-0x1.01cfc80000000p-1f}, {-0x1.bbed7c0000000p-1f,-0x1.fe2f640000000p-2f},
{-0x1.bd7c0a0000000p-1f,-0x1.f8ba4e0000000p-2f}, {-0x1.bf064e0000000p-1f,-0x1.f3405a0000000p-2f}, {-0x1.c08c420000000p-1f,-0x1.edc1960000000p-2f}, {-0x1.c20de40000000p-1f,-0x1.e83e0e0000000p-2f},
{-0x1.c38b300000000p-1f,-0x1.e2b5d40000000p-2f}, {-0x1.c504200000000p-1f,-0x1.dd28f20000000p-2f}, {-0x1.c678b40000000p-1f,-0x1.d797760000000p-2f}, {-0x1.c7e8e60000000p-1f,-0x1.d2016e0000000p-2f},
{-0x1.c954b20000000p-1f,-0x1.cc66ea0000000p-2f}, {-0x1.cabc160000000p-1f,-0x1.c6c7f40000000p-2f}, {-0x1.cc1f100000000p-1f,-0x1.c1249e0000000p-2f}, {-0x1.cd7d980000000p-1f,-0x1.bb7cf20000000p-2f},
{-0x1.ced7b00000000p-1f,-0x1.b5d1000000000p-2f}, {-0x1.d02d500000000p-1f,-0x1.b020d60000000p-2f}, {-0x1.d17e780000000p-1f,-0x1.aa6c820000000p-2f}, {-0x1.d2cb220000000p-1f,-0x1.a4b4120000000p-2f},
{-0x1.d4134e0000000p-1f,-0x1.9ef7940000000p-2f}, {-0x1.d556f60000000p-1f,-0x1.9937160000000p-2f}, {-0x1.d696180000000p-1f,-0x1.9372a60000000p-2f}, {-0x1.d7d0b00000000p-1f,-0x1.8daa520000000p-2f},
{-0x1.d906bc0000000p-1f,-0x1.87de2a0000000p-2f}, {-0x1.da383a0000000p-1f,-0x1.820e3c0000000p-2f}, {-0x1.db65260000000p-1f,-0x1.7c3a940000000p-2f}, {-0x1.dc8d7c0000000p-1f,-0x1.7663400000000p-2f},
{-0x1.ddb13c0000000p-1f,-0x1.7088540000000p-2f}, {-0x1.ded0600000000p-1f,-0x1.6aa9d80000000p-2f}, {-0x1.dfeae60000000p-1f,-0x1.64c7de0000000p-2f}, {-0x1.e100cc0000000p-1f,-0x1.5ee2740000000p-2f},
{-0x1.e212100000000p-1f,-0x1.58f9a80000000p-2f}, {-0x1.e31eae0000000p-1f,-0x1.530d880000000p-2f}, {-0x1.e426a40000000p-1f,-0x1.4d1e240000000p-2f}, {-0x1.e529f00000000p-1f,-0x1.472b8a0000000p-2f},
{-0x1.e6288e0000000p-1f,-0x1.4135ca0000000p-2f}, {-0x1.e7227e0000000p-1f,-0x1.3b3cf00000000p-2f}, {-0x1.e817ba0000000p-1f,-0x1.35410c0000000p-2f}, {-0x1.e908440000000p-1f,-0x1.2f422e0000000p-2f},
{-0x1.e9f4160000000p-1f,-0x1.2940620000000p-2f}, {-0x1.eadb2e0000000p-1f,-0x1.233bba0000000p-2f}, {-0x1.ebbd8c0000000p-1f,-0x1.1d34440000000p-2f}, {-0x1.ec9b2e0000000p-1f,-0x1.172a0e0000000p-2f},
{-0x1.ed740e0000000p-1f,-0x1.111d260000000p-2f}, {-0x1.ee482e0000000p-1f,-0x1.0b0d9c0000000p-2f}, {-0x1.ef178a0000000p-1f,-0x1.04fb800000000p-2f}, {-0x1.efe2200000000p-1f,-0x1.fdcdc20000000p-3f},
{-0x1.f0a7f00000000p-1f,-0x1.f19f980000000p-3f}, {-0x1.f168f60000000p-1f,-0x1.e56ca20000000p-3f}, {-0x1.f225300000000p-1f,-0x1.d934fe0000000p-3f}, {-0x1.f2dc9c0000000p-1f,-0x1.ccf8cc0000000p-3f},
{-0x1.f38f3a0000000p-1f,-0x1.c0b8260000000p-3f}, {-0x1.f43d080000000p-1f,-0x1.b4732e0000000p-3f}, {-0x1.f4e6040000000p-1f,-0x1.a82a020000000p-3f}, {-0x1.f58a2c0000000p-1f,-0x1.9bdcc00000000p-3f},
{-0x1.f6297c0000000p-1f,-0x1.8f8b840000000p-3f}, {-0x1.f6c3f80000000p-1f,-0x1.83366e0000000p-3f}, {-0x1.f7599a0000000p-1f,-0x1.76dd9e0000000p-3f}, {-0x1.f7ea620000000p-1f,-0x1.6a81300000000p-3f},
{-0x1.f876500000000p-1f,-0x1.5e21440000000p-3f}, {-0x1.f8fd600000000p-1f,-0x1.51bdf80000000p-3f}, {-0x1.f97f920000000p-1f,-0x1.45576c0000000p-3f}, {-0x1.f9fce60000000p-1f,-0x1.38edbc0000000p-3f},
{-0x1.fa75580000000p-1f,-0x1.2c81060000000p-3f}, {-0x1.fae8e80000000p-1f,-0x1.20116e0000000p-3f}, {-0x1.fb57980000000p-1f,-0x1.139f0c0000000p-3f}, {-0x1.fbc1620000000p-1f,-0x1.072a040000000p-3f},
{-0x1.fc26480000000p-1f,-0x1.f564e60000000p-4f}, {-0x1.fc86460000000p-1f,-0x1.dc70ec0000000p-4f}, {-0x1.fce1600000000p-1f,-0x1.c3785c0000000p-4f}, {-0x1.fd37920000000p-1f,-0x1.aa7b720000000p-4f},
{-0x1.fd88da0000000p-1f,-0x1.917a6c0000000p-4f}, {-0x1.fdd53a0000000p-1f,-0x1.7875860000000p-4f}, {-0x1.fe1cb00000000p-1f,-0x1.5f6d000000000p-4f}, {-0x1.fe5f3a0000000p-1f,-0x1.4661180000000p-4f},
{-0x1.fe9cda0000000p-1f,-0x1.2d520a0000000p-4f}, {-0x1.fed58e0000000p-1f,-0x1.1440140000000p-4f}, {-0x1.ff09560000000p-1f,-0x1.f656e80000000p-5f}, {-0x1.ff38300000000p-1f,-0x1.c428d20000000p-5f},
{-0x1.ff621e0000000p-1f,-0x1.91f6600000000p-5f}, {-0x1.ff871e0000000p-1f,-0x1.5fc00e0000000p-5f}, {-0x1.ffa72e0000000p-1f,-0x1.2d86580000000p-5f}, {-0x1.ffc2520000000p-1f,-0x1.f693740000000p-6f},
{-0x1.ffd8860000000p-1f,-0x1.9215600000000p-6f}, {-0x1.ffe9cc0000000p-1f,-0x1.2d936c0000000p-6f}, {-0x1.fff6220000000p-1f,-0x1.921d200000000p-7f}, {-0x1.fffd880000000p-1f,-0x1.921f100000000p-8f},
{0x1.0000000000000p+0f,-0x0.0p+0f}, {0x1.fffa720000000p-1f,-0x1.2d96b00000000p-7f}, {0x1.ffe9cc0000000p-1f,-0x1.2d936c0000000p-6f}, {0x1.ffce0a0000000p-1f,-0x1.c454f40000000p-6f},
{0x1.ffa72e0000000p-1f,-0x1.2d86580000000p-5f}, {0x1.ff753c0000000p-1f,-0x1.78dbaa0000000p-5f}, {0x1.ff38300000000p-1f,-0x1.c428d20000000p-5f}, {0x1.fef0100000000p-1f,-0x1.07b6140000000p-4f},
{0x1.fe9cda0000000p-1f,-0x1.2d520a0000000p-4f}, {0x1.fe3e920000000p-1f,-0x1.52e7740000000p-4f}, {0x1.fdd53a0000000p-1f,-0x1.7875860000000p-4f}, {0x1.fd60d20000000p-1f,-0x1.9dfb6e0000000p-4f},
{0x1.fce1600000000p-1f,-0x1.c3785c0000000p-4f}, {0x1.fc56e40000000p-1f,-0x1.e8eb800000000p-4f}, {0x1.fbc1620000000p-1f,-0x1.072a040000000p-3f}, {0x1.fb20dc0000000p-1f,-0x1.19d8940000000p-3f},
{0x1.fa75580000000p-1f,-0x1.2c81060000000p-3f}, {0x1.f9bed80000000p-1f,-0x1.3f22f60000000p-3f}, {0x1.f8fd600000000p-1f,-0x1.51bdf80000000p-3f}, {0x1.f830f40000000p-1f,-0x1.6451a80000000p-3f},
{0x1.f7599a0000000p-1f,-0x1.76dd9e0000000p-3f}, {0x1.f677560000000p-1f,-0x1.8961720000000p-3f}, {0x1.f58a2c0000000p-1f,-0x1.9bdcc00000000p-3f}, {0x1.f492200000000p-1f,-0x1.ae4f1e0000000p-3f},
{0x1.f38f3a0000000p-1f,-0x1.c0b8260000000p-3f}, {0x1.f281800000000p-1f,-0x1.d317740000000p-3f}, {0x1.f168f60000000p-1f,-0x1.e56ca20000000p-3f}, {0x1.f045a20000000p-1f,-0x1.f7b7480000000p-3f},
{0x1.ef178a0000000p-1f,-0x1.04fb800000000p-2f}, {0x1.eddeb60000000p-1f,-0x1.0e15b40000000p-2f}, {0x1.ec9b2e0000000p-1f,-0x1.172a0e0000000p-2f}, {0x1.eb4cf60000000p-1f,-0x1.2038580000000p-2f},
{0x1.e9f4160000000p-1f,-0x1.2940620000000p-2f}, {0x1.e890960000000p-1f,-0x1.3241fc0000000p-2f}, {0x1.e7227e0000000p-1f,-0x1.3b3cf00000000p-2f}, {0x1.e5a9d60000000p-1f,-0x1.44310e0000000p-2f},
{0x1.e426a40000000p-1f,-0x1.4d1e240000000p-2f}, {0x1.e298f40000000p-1f,-0x1.5604020000000p-2f}, {0x1.e100cc0000000p-1f,-0x1.5ee2740000000p-2f}, {0x1.df5e360000000p-1f,-0x1.67b94a0000000p-2f},
{0x1.ddb13c0000000p-1f,-0x1.7088540000000p-2f}, {0x1.dbf9e40000000p-1f,-0x1.794f5e0000000p-2f}, {0x1.da383a0000000p-1f,-0x1.820e3c0000000p-2f}, {0x1.d86c480000000p-1f,-0x1.8ac4b80000000p-2f},
{0x1.d696180000000p-1f,-0x1.9372a60000000p-2f}, {0x1.d4b5b20000000p-1f,-0x1.9c17d40000000p-2f}, {0x1.d2cb220000000p-1f,-0x1.a4b4120000000p-2f}, {0x1.d0d6720000000p-1f,-0x1.ad47320000000p-2f},
{0x1.ced7b00000000p-1f,-0x1.b5d1000000000p-2f}, {0x1.cccee20000000p-1f,-0x1.be51520000000p-2f}, {0x1.cabc160000000p-1f,-0x1.c6c7f40000000p-2f}, {0x1.c89f580000000p-1f,-0x1.cf34ba0000000p-2f},
{0x1.c678b40000000p-1f,-0x1.d797760000000p-2f}, {0x1.c448340000000p-1f,-0x1.dfeff60000000p-2f}, {0x1.c20de40000000p-1f,-0x1.e83e0e0000000p-2f}, {0x1.bfc9d20000000p-1f,-0x1.f081900000000p-2f},
{0x1.bd7c0a0000000p-1f,-0x1.f8ba4e0000000p-2f}, {0x1.bb249a0000000p-1f,-0x1.00740c0000000p-1f}, {0x1.b8c38e0000000p-1f,-0x1.0485620000000p-1f}, {0x1.b658f20000000p-1f,-0x1.0891120000000p-1f},
{0x1.b3e4d40000000p-1f,-0x1.0c97040000000p-1f}, {0x1.b167420000000p-1f,-0x1.1097240000000p-1f}, {0x1.aee04c0000000p-1f,-0x1.14915a0000000p-1f}, {0x1.ac4ffc0000000p-1f,-0x1.1885920000000p-1f},
{0x1.a9b6620000000p-1f,-0x1.1c73b40000000p-1f}, {0x1.a7138e0000000p-1f,-0x1.205baa0000000p-1f}, {0x1.a4678c0000000p-1f,-0x1.243d600000000p-1f}, {0x1.a1b26e0000000p-1f,-0x1.2818be0000000p-1f},
{0x1.9ef43e0000000p-1f,-0x1.2bedb20000000p-1f}, {0x1.9c2d120000000p-1f,-0x1.2fbc240000000p-1f}, {0x1.995cf20000000p-1f,-0x1.3384000000000p-1f}, {0x1.9683f40000000p-1f,-0x1.3745320000000p-1f},
{0x1.93a2240000000p-1f,-0x1.3affa20000000p-1f}, {0x1.90b7940000000p-1f,-0x1.3eb33e0000000p-1f}, {0x1.8dc4540000000p-1f,-0x1.425ff20000000p-1f}, {0x1.8ac8720000000p-1f,-0x1.4605a60000000p-1f},
{0x1.87c4000000000p-1f,-0x1.49a44a0000000p-1f}, {0x1.84b7120000000p-1f,-0x1.4d3bc60000000p-1f}, {0x1.81a1b40000000p-1f,-0x1.50cc0a0000000p-1f}, {0x1.7e83f80000000p-1f,-0x1.5455000000000p-1f},
{0x1.7b5df20000000p-1f,-0x1.57d6940000000p-1f}, {0x1.782fb20000000p-1f,-0x1.5b50b20000000p-1f}, {0x1.74f9480000000p-1f,-0x1.5ec34a0000000p-1f}, {0x1.71baca0000000p-1f,-0x1.622e440000000p-1f},
{0x1.6e74460000000p-1f,-0x1.6591920000000p-1f}, {0x1.6b25ce0000000p-1f,-0x1.68ed1e0000000p-1f}, {0x1.67cf780000000p-1f,-0x1.6c40d80000000p-1f}, {0x1.6471540000000p-1f,-0x1.6f8caa0000000p-1f},
{0x1.610b760000000p-1f,-0x1.72d0840000000p-1f}, {0x1.5d9dee0000000p-1f,-0x1.760c520000000p-1f}, {0x1.5a28d20000000p-1f,-0x1.7940060000000p-1f}, {0x1.56ac360000000p-1f,-0x1.7c6b8a0000000p-1f},
{0x1.53282a0000000p-1f,-0x1.7f8ece0000000p-1f}, {0x1.4f9cc20000000p-1f,-0x1.82a9c20000000p-1f}, {0x1.4c0a140000000p-1f,-0x1.85bc520000000p-1f}, {0x1.4870340000000p-1f,-0x1.88c66e0000000p-1f},
{0x1.44cf320000000p-1f,-0x1.8bc8060000000p-1f}, {0x1.4127260000000p-1f,-0x1.8ec10a0000000p-1f}, {0x1.3d78240000000p-1f,-0x1.91b1660000000p-1f}, {0x1.39c23e0000000p-1f,-0x1.94990e0000000p-1f},
{0x1.36058c0000000p-1f,-0x1.9777f00000000p-1f}, {0x1.32421e0000000p-1f,-0x1.9a4dfa0000000p-1f}, {0x1.2e780e0000000p-1f,-0x1.9d1b200000000p-1f}, {0x1.2aa76e0000000p-1f,-0x1.9fdf500000000p-1f},
{0x1.26d0540000000p-1f,-0x1.a29a7a0000000p-1f}, {0x1.22f2d60000000p-1f,-0x1.a54c920000000p-1f}, {0x1.1f0f080000000p-1f,-0x1.a7f5860000000p-1f}, {0x1.1b25020000000p-1f,-0x1.aa95480000000p-1f},
{0x1.1734d60000000p-1f,-0x1.ad2bca0000000p-1f}, {0x1.133e9c0000000p-1f,-0x1.afb8fe0000000p-1f}, {0x1.0f426c0000000p-1f,-0x1.b23cd40000000p-1f}, {0x1.0b40580000000p-1f,-0x1.b4b7400000000p-1f},
{0x1.07387a0000000p-1f,-0x1.b728340000000p-1f}, {0x1.032ae60000000p-1f,-0x1.b98fa20000000p-1f}, {0x1.fe2f640000000p-2f,-0x1.bbed7c0000000p-1f}, {0x1.f5fdee0000000p-2f,-0x1.be41b60000000p-1f},
{0x1.edc1960000000p-2f,-0x1.c08c420000000p-1f}, {0x1.e57a860000000p-2f,-0x1.c2cd140000000p-1f}, {0x1.dd28f20000000p-2f,-0x1.c504200000000p-1f}, {0x1.d4cd020000000p-2f,-0x1.c731580000000p-1f},
{0x1.cc66ea0000000p-2f,-0x1.c954b20000000p-1f}, {0x1.c3f6d40000000p-2f,-0x1.cb6e200000000p-1f}, {0x1.bb7cf20000000p-2f,-0x1.cd7d980000000p-1f}, {0x1.b2f9720000000p-2f,-0x1.cf830e0000000p-1f},
{0x1.aa6c820000000p-2f,-0x1.d17e780000000p-1f}, {0x1.a1d6540000000p-2f,-0x1.d36fc80000000p-1f}, {0x1.9937160000000p-2f,-0x1.d556f60000000p-1f}, {0x1.908ef80000000p-2f,-0x1.d733f60000000p-1f},
{0x1.87de2a0000000p-2f,-0x1.d906bc0000000p-1f}, {0x1.7f24de0000000p-2f,-0x1.dacf420000000p-1f}, {0x1.7663400000000p-2f,-0x1.dc8d7c0000000p-1f}, {0x1.6d99860000000p-2f,-0x1.de41600000000p-1f},
{0x1.64c7de0000000p-2f,-0x1.dfeae60000000p-1f}, {0x1.5bee780000000p-2f,-0x1.e18a020000000p-1f}, {0x1.530d880000000p-2f,-0x1.e31eae0000000p-1f}, {0x1.4a253e0000000p-2f,-0x1.e4a8e00000000p-1f},
{0x1.4135ca0000000p-2f,-0x1.e6288e0000000p-1f}, {0x1.383f5e0000000p-2f,-0x1.e79db20000000p-1f}, {0x1.2f422e0000000p-2f,-0x1.e908440000000p-1f}, {0x1.263e6a0000000p-2f,-0x1.ea683a0000000p-1f},
{0x1.1d34440000000p-2f,-0x1.ebbd8c0000000p-1f}, {0x1.1423ee0000000p-2f,-0x1.ed08360000000p-1f}, {0x1.0b0d9c0000000p-2f,-0x1.ee482e0000000p-1f}, {0x1.01f1800000000p-2f,-0x1.ef7d6e0000000p-1f},
{0x1.f19f980000000p-3f,-0x1.f0a7f00000000p-1f}, {0x1.df51640000000p-3f,-0x1.f1c7ac0000000p-1f}, {0x1.ccf8cc0000000p-3f,-0x1.f2dc9c0000000p-1f}, {0x1.ba96340000000p-3f,-0x1.f3e6bc0000000p-1f},
{0x1.a82a020000000p-3f,-0x1.f4e6040000000p-1f}, {0x1.95b49e0000000p-3f,-0x1.f5da6e0000000p-1f}, {0x1.83366e0000000p-3f,-0x1.f6c3f80000000p-1f}, {0x1.70afd80000000p-3f,-0x1.f7a29a0000000p-1f},
{0x1.5e21440000000p-3f,-0x1.f876500000000p-1f}, {0x1.4b8b180000000p-3f,-0x1.f93f140000000p-1f}, {0x1.38edbc0000000p-3f,-0x1.f9fce60000000p-1f}, {0x1.2649940000000p-3f,-0x1.faafbc0000000p-1f},
{0x1.139f0c0000000p-3f,-0x1.fb57980000000p-1f}, {0x1.00ee8a0000000p-3f,-0x1.fbf4700000000p-1f}, {0x1.dc70ec0000000p-4f,-0x1.fc86460000000p-1f}, {0x1.b6fa6e0000000p-4f,-0x1.fd0d160000000p-1f},
{0x1.917a6c0000000p-4f,-0x1.fd88da0000000p-1f}, {0x1.6bf1b40000000p-4f,-0x1.fdf9920000000p-1f}, {0x1.4661180000000p-4f,-0x1.fe5f3a0000000p-1f}, {0x1.20c9680000000p-4f,-0x1.feb9d20000000p-1f},
{0x1.f656e80000000p-5f,-0x1.ff09560000000p-1f}, {0x1.ab101c0000000p-5f,-0x1.ff4dc60000000p-1f}, {0x1.5fc00e0000000p-5f,-0x1.ff871e0000000p-1f}, {0x1.14685e0000000p-5f,-0x1.ffb55e0000000p-1f},
{0x1.9215600000000p-6f,-0x1.ffd8860000000p-1f}, {0x1.f6a2960000000p-7f,-0x1.fff0940000000p-1f}, {0x1.921f100000000p-8f,-0x1.fffd880000000p-1f}, {-0x1.921f8c0000000p-9f,-0x1.ffff620000000p-1f},
{-0x1.921d200000000p-7f,-0x1.fff6220000000p-1f}, {-0x1.5fd4d20000000p-6f,-0x1.ffe1c60000000p-1f}, {-0x1.f693740000000p-6f,-0x1.ffc2520000000p-1f}, {-0x1.46a3960000000p-5f,-0x1.ff97c40000000p-1f},
{-0x1.91f6600000000p-5f,-0x1.ff621e0000000p-1f}, {-0x1.dd40700000000p-5f,-0x1.ff21620000000p-1f}, {-0x1.1440140000000p-4f,-0x1.fed58e0000000p-1f}, {-0x1.39d9f20000000p-4f,-0x1.fe7ea80000000p-1f},
{-0x1.5f6d000000000p-4f,-0x1.fe1cb00000000p-1f}, {-0x1.84f8720000000p-4f,-0x1.fdafa80000000p-1f}, {-0x1.aa7b720000000p-4f,-0x1.fd37920000000p-1f}, {-0x1.cff5340000000p-4f,-0x1.fcb4700000000p-1f},
{-0x1.f564e60000000p-4f,-0x1.fc26480000000p-1f}, {-0x1.0d64dc0000000p-3f,-0x1.fb8d180000000p-1f}, {-0x1.20116e0000000p-3f,-0x1.fae8e80000000p-1f}, {-0x1.32b7c00000000p-3f,-0x1.fa39ba0000000p-1f},
{-0x1.45576c0000000p-3f,-0x1.f97f920000000p-1f}, {-0x1.57f0080000000p-3f,-0x1.f8ba740000000p-1f}, {-0x1.6a81300000000p-3f,-0x1.f7ea620000000p-1f}, {-0x1.7d0a7c0000000p-3f,-0x1.f70f640000000p-1f},
{-0x1.8f8b840000000p-3f,-0x1.f6297c0000000p-1f}, {-0x1.a203e20000000p-3f,-0x1.f538b20000000p-1f}, {-0x1.b4732e0000000p-3f,-0x1.f43d080000000p-1f}, {-0x1.c6d9060000000p-3f,-0x1.f336860000000p-1f},
{-0x1.d934fe0000000p-3f,-0x1.f225300000000p-1f}, {-0x1.eb86b40000000p-3f,-0x1.f1090c0000000p-1f}, {-0x1.fdcdc20000000p-3f,-0x1.efe2200000000p-1f}, {-0x1.0804e00000000p-2f,-0x1.eeb0740000000p-1f},
{-0x1.111d260000000p-2f,-0x1.ed740e0000000p-1f}, {-0x1.1a2f800000000p-2f,-0x1.ec2cf40000000p-1f}, {-0x1.233bba0000000p-2f,-0x1.eadb2e0000000p-1f}, {-0x1.2c41a40000000p-2f,-0x1.e97ec40000000p-1f},
{-0x1.35410c0000000p-2f,-0x1.e817ba0000000p-1f}, {-0x1.3e39be0000000p-2f,-0x1.e6a61c0000000p-1f}, {-0x1.472b8a0000000p-2f,-0x1.e529f00000000p-1f}, {-0x1.50163e0000000p-2f,-0x1.e3a33e0000000p-1f},
{-0x1.58f9a80000000p-2f,-0x1.e212100000000p-1f}, {-0x1.61d5960000000p-2f,-0x1.e0766e0000000p-1f}, {-0x1.6aa9d80000000p-2f,-0x1.ded0600000000p-1f}, {-0x1.73763c0000000p-2f,-0x1.dd1ff00000000p-1f},
{-0x1.7c3a940000000p-2f,-0x1.db65260000000p-1f}, {-0x1.84f6aa0000000p-2f,-0x1.d9a00e0000000p-1f}, {-0x1.8daa520000000p-2f,-0x1.d7d0b00000000p-1f}, {-0x1.96555c0000000p-2f,-0x1.d5f7180000000p-1f},
{-0x1.9ef7940000000p-2f,-0x1.d4134e0000000p-1f}, {-0x1.a790ce0000000p-2f,-0x1.d2255c0000000p-1f}, {-0x1.b020d60000000p-2f,-0x1.d02d500000000p-1f}, {-0x1.b8a7820000000p-2f,-0x1.ce2b320000000p-1f},
{-0x1.c1249e0000000p-2f,-0x1.cc1f100000000p-1f}, {-0x1.c997fc0000000p-2f,-0x1.ca08f20000000p-1f}, {-0x1.d2016e0000000p-2f,-0x1.c7e8e60000000p-1f}, {-0x1.da60c60000000p-2f,-0x1.c5bef60000000p-1f},
{-0x1.e2b5d40000000p-2f,-0x1.c38b300000000p-1f}, {-0x1.eb006a0000000p-2f,-0x1.c14d9e0000000p-1f}, {-0x1.f3405a0000000p-2f,-0x1.bf064e0000000p-1f}, {-0x1.fb75760000000p-2f,-0x1.bcb54c0000000p-1f},
{-0x1.01cfc80000000p-1f,-0x1.ba5aa60000000p-1f}, {-0x1.05df3e0000000p-1f,-0x1.b7f6680000000p-1f}, {-0x1.09e9080000000p-1f,-0x1.b588a00000000p-1f}, {-0x1.0ded0c0000000p-1f,-0x1.b3115a0000000p-1f},
{-0x1.11eb360000000p-1f,-0x1.b090a60000000p-1f}, {-0x1.15e36e0000000p-1f,-0x1.ae06900000000p-1f}, {-0x1.19d5a00000000p-1f,-0x1.ab73260000000p-1f}, {-0x1.1dc1b60000000p-1f,-0x1.a8d6760000000p-1f},
{-0x1.21a79a0000000p-1f,-0x1.a630920000000p-1f}, {-0x1.2587340000000p-1f,-0x1.a381840000000p-1f}, {-0x1.2960720000000p-1f,-0x1.a0c95e0000000p-1f}, {-0x1.2d333e0000000p-1f,-0x1.9e082e0000000p-1f},
{-0x1.30ff800000000p-1f,-0x1.9b3e040000000p-1f}, {-0x1.34c5260000000p-1f,-0x1.986af00000000p-1f}, {-0x1.3884180000000p-1f,-0x1.958efe0000000p-1f}, {-0x1.3c3c440000000p-1f,-0x1.92aa420000000p-1f},
{-0x1.3fed960000000p-1f,-0x1.8fbcca0000000p-1f}, {-0x1.4397f60000000p-1f,-0x1.8cc6a80000000p-1f}, {-0x1.473b520000000p-1f,-0x1.89c7ea0000000p-1f}, {-0x1.4ad7960000000p-1f,-0x1.86c0a20000000p-1f},
{-0x1.4e6cac0000000p-1f,-0x1.83b0e00000000p-1f}, {-0x1.51fa820000000p-1f,-0x1.8098b80000000p-1f}, {-0x1.5581040000000p-1f,-0x1.7d78360000000p-1f}, {-0x1.59001e0000000p-1f,-0x1.7a4f700000000p-1f},
{-0x1.5c77bc0000000p-1f,-0x1.771e760000000p-1f}, {-0x1.5fe7cc0000000p-1f,-0x1.73e5580000000p-1f}, {-0x1.63503a0000000p-1f,-0x1.70a42c0000000p-1f}, {-0x1.66b0f40000000p-1f,-0x1.6d5afe0000000p-1f},
{-0x1.6a09e60000000p-1f,-0x1.6a09e60000000p-1f}, {-0x1.6d5afe0000000p-1f,-0x1.66b0f40000000p-1f}, {-0x1.70a42c0000000p-1f,-0x1.63503a0000000p-1f}, {-0x1.73e5580000000p-1f,-0x1.5fe7cc0000000p-1f},
{-0x1.771e760000000p-1f,-0x1.5c77bc0000000p-1f}, {-0x1.7a4f700000000p-1f,-0x1.59001e0000000p-1f}, {-0x1.7d78360000000p-1f,-0x1.5581040000000p-1f}, {-0x1.8098b80000000p-1f,-0x1.51fa820000000p-1f},
{-0x1.83b0e00000000p-1f,-0x1.4e6cac0000000p-1f}, {-0x1.86c0a20000000p-1f,-0x1.4ad7960000000p-1f}, {-0x1.89c7ea0000000p-1f,-0x1.473b520000000p-1f}, {-0x1.8cc6a80000000p-1f,-0x1.4397f60000000p-1f},
{-0x1.8fbcca0000000p-1f,-0x1.3fed960000000p-1f}, {-0x1.92aa420000000p-1f,-0x1.3c3c440000000p-1f}, {-0x1.958efe0000000p-1f,-0x1.3884180000000p-1f}, {-0x1.986af00000000p-1f,-0x1.34c5260000000p-1f},
{-0x1.9b3e040000000p-1f,-0x1.30ff800000000p-1f}, {-0x1.9e082e0000000p-1f,-0x1.2d333e0000000p-1f}, {-0x1.a0c95e0000000p-1f,-0x1.2960720000000p-1f}, {-0x1.a381840000000p-1f,-0x1.2587340000000p-1f},
{-0x1.a630920000000p-1f,-0x1.21a79a0000000p-1f}, {-0x1.a8d6760000000p-1f,-0x1.1dc1b60000000p-1f}, {-0x1.ab73260000000p-1f,-0x1.19d5a00000000p-1f}, {-0x1.ae06900000000p-1f,-0x1.15e36e0000000p-1f},
{-0x1.b090a60000000p-1f,-0x1.11eb360000000p-1f}, {-0x1.b3115a0000000p-1f,-0x1.0ded0c0000000p-1f}, {-0x1.b588a00000000p-1f,-0x1.09e9080000000p-1f}, {-0x1.b7f6680000000p-1f,-0x1.05df3e0000000p-1f},
{-0x1.ba5aa60000000p-1f,-0x1.01cfc80000000p-1f}, {-0x1.bcb54c0000000p-1f,-0x1.fb75760000000p-2f}, {-0x1.bf064e0000000p-1f,-0x1.f3405a0000000p-2f}, {-0x1.c14d9e0000000p-1f,-0x1.eb006a0000000p-2f},
{-0x1.c38b300000000p-1f,-0x1.e2b5d40000000p-2f}, {-0x1.c5bef60000000p-1f,-0x1.da60c60000000p-2f}, {-0x1.c7e8e60000000p-1f,-0x1.d2016e0000000p-2f}, {-0x1.ca08f20000000p-1f,-0x1.c997fc0000000p-2f},
{-0x1.cc1f100000000p-1f,-0x1.c1249e0000000p-2f}, {-0x1.ce2b320000000p-1f,-0x1.b8a7820000000p-2f}, {-0x1.d02d500000000p-1f,-0x1.b020d60000000p-2f}, {-0x1.d2255c0000000p-1f,-0x1.a790ce0000000p-2f},
{-0x1.d4134e0000000p-1f,-0x1.9ef7940000000p-2f}, {-0x1.d5f7180000000p-1f,-0x1.96555c0000000p-2f}, {-0x1.d7d0b00000000p-1f,-0x1.8daa520000000p-2f}, {-0x1.d9a00e0000000p-1f,-0x1.84f6aa0000000p-2f},
{-0x1.db65260000000p-1f,-0x1.7c3a940000000p-2f}, {-0x1.dd1ff00000000p-1f,-0x1.73763c0000000p-2f}, {-0x1.ded0600000000p-1f,-0x1.6aa9d80000000p-2f}, {-0x1.e0766e0000000p-1f,-0x1.61d5960000000p-2f},
{-0x1.e212100000000p-1f,-0x1.58f9a80000000p-2f}, {-0x1.e3a33e0000000p-1f,-0x1.50163e0000000p-2f}, {-0x1.e529f00000000p-1f,-0x1.472b8a0000000p-2f}, {-0x1.e6a61c0000000p-1f,-0x1.3e39be0000000p-2f},
{-0x1.e817ba0000000p-1f,-0x1.35410c0000000p-2f}, {-0x1.e97ec40000000p-1f,-0x1.2c41a40000000p-2f}, {-0x1.eadb2e0000000p-1f,-0x1.233bba0000000p-2f}, {-0x1.ec2cf40000000p-1f,-0x1.1a2f800000000p-2f},
{-0x1.ed740e0000000p-1f,-0x1.111d260000000p-2f}, {-0x1.eeb0740000000p-1f,-0x1.0804e00000000p-2f}, {-0x1.efe2200000000p-1f,-0x1.fdcdc20000000p-3f}, {-0x1.f1090c0000000p-1f,-0x1.eb86b40000000p-3f},
{-0x1.f225300000000p-1f,-0x1.d934fe0000000p-3f}, {-0x1.f336860000000p-1f,-0x1.c6d9060000000p-3f}, {-0x1.f43d080000000p-1f,-0x1.b4732e0000000p-3f}, {-0x1.f538b20000000p-1f,-0x1.a203e20000000p-3f},
{-0x1.f6297c0000000p-1f,-0x1.8f8b840000000p-3f}, {-0x1.f70f640000000p-1f,-0x1.7d0a7c0000000p-3f}, {-0x1.f7ea620000000p-1f,-0x1.6a81300000000p-3f}, {-0x1.f8ba740000000p-1f,-0x1.57f0080000000p-3f},
{-0x1.f97f920000000p-1f,-0x1.45576c0000000p-3f}, {-0x1.fa39ba0000000p-1f,-0x1.32b7c00000000p-3f}, {-0x1.fae8e80000000p-1f,-0x1.20116e0000000p-3f}, {-0x1.fb8d180000000p-1f,-0x1.0d64dc0000000p-3f},
{-0x1.fc26480000000p-1f,-0x1.f564e60000000p-4f}, {-0x1.fcb4700000000p-1f,-0x1.cff5340000000p-4f}, {-0x1.fd37920000000p-1f,-0x1.aa7b720000000p-4f}, {-0x1.fdafa80000000p-1f,-0x1.84f8720000000p-4f},
{-0x1.fe1cb00000000p-1f,-0x1.5f6d000000000p-4f}, {-0x1.fe7ea80000000p-1f,-0x1.39d9f20000000p-4f}, {-0x1.fed58e0000000p-1f,-0x1.1440140000000p-4f}, {-0x1.ff21620000000p-1f,-0x1.dd40700000000p-5f},
{-0x1.ff621e0000000p-1f,-0x1.91f6600000000p-5f}, {-0x1.ff97c40000000p-1f,-0x1.46a3960000000p-5f}, {-0x1.ffc2520000000p-1f,-0x1.f693740000000p-6f}, {-0x1.ffe1c60000000p-1f,-0x1.5fd4d20000000p-6f},
{-0x1.fff6220000000p-1f,-0x1.921d200000000p-7f}, {-0x1.ffff620000000p-1f,-0x1.921f8c0000000p-9f}, {-0x1.fffd880000000p-1f,0x1.921f100000000p-8f}, {-0x1.fff0940000000p-1f,0x1.f6a2960000000p-7f},
{-0x1.ffd8860000000p-1f,0x1.9215600000000p-6f}, {-0x1.ffb55e0000000p-1f,0x1.14685e0000000p-5f}, {-0x1.ff871e0000000p-1f,0x1.5fc00e0000000p-5f}, {-0x1.ff4dc60000000p-1f,0x1.ab101c0000000p-5f},
{-0x1.ff09560000000p-1f,0x1.f656e80000000p-5f}, {-0x1.feb9d20000000p-1f,0x1.20c9680000000p-4f}, {-0x1.fe5f3a0000000p-1f,0x1.4661180000000p-4f}, {-0x1.fdf9920000000p-1f,0x1.6bf1b40000000p-4f},
{-0x1.fd88da0000000p-1f,0x1.917a6c0000000p-4f}, {-0x1.fd0d160000000p-1f,0x1.b6fa6e0000000p-4f}, {-0x1.fc86460000000p-1f,0x1.dc70ec0000000p-4f}, {-0x1.fbf4700000000p-1f,0x1.00ee8a0000000p-3f},
{-0x1.fb57980000000p-1f,0x1.139f0c0000000p-3f}, {-0x1.faafbc0000000p-1f,0x1.2649940000000p-3f}, {-0x1.f9fce60000000p-1f,0x1.38edbc0000000p-3f}, {-0x1.f93f140000000p-1f,0x1.4b8b180000000p-3f},
{-0x1.f876500000000p-1f,0x1.5e21440000000p-3f}, {-0x1.f7a29a0000000p-1f,0x1.70afd80000000p-3f}, {-0x1.f6c3f80000000p-1f,0x1.83366e0000000p-3f}, {-0x1.f5da6e0000000p-1f,0x1.95b49e0000000p-3f},
{-0x1.f4e6040000000p-1f,0x1.a82a020000000p-3f}, {-0x1.f3e6bc0000000p-1f,0x1.ba96340000000p-3f}, {-0x1.f2dc9c0000000p-1f,0x1.ccf8cc0000000p-3f}, {-0x1.f1c7ac0000000p-1f,0x1.df51640000000p-3f},
{-0x1.f0a7f00000000p-1f,0x1.f19f980000000p-3f}, {-0x1.ef7d6e0000000p-1f,0x1.01f1800000000p-2f}, {-0x1.ee482e0000000p-1f,0x1.0b0d9c0000000p-2f}, {-0x1.ed08360000000p-1f,0x1.1423ee0000000p-2f},
{-0x1.ebbd8c0000000p-1f,0x1.1d34440000000p-2f}, {-0x1.ea683a0000000p-1f,0x1.263e6a0000000p-2f}, {-0x1.e908440000000p-1f,0x1.2f422e0000000p-2f}, {-0x1.e79db20000000p-1f,0x1.383f5e0000000p-2f},
{-0x1.e6288e0000000p-1f,0x1.4135ca0000000p-2f}, {-0x1.e4a8e00000000p-1f,0x1.4a253e0000000p-2f}, {-0x1.e31eae0000000p-1f,0x1.530d880000000p-2f}, {-0x1.e18a020000000p-1f,0x1.5bee780000000p-2f},
{-0x1.dfeae60000000p-1f,0x1.64c7de0000000p-2f}, {-0x1.de41600000000p-1f,0x1.6d99860000000p-2f}, {-0x1.dc8d7c0000000p-1f,0x1.7663400000000p-2f}, {-0x1.dacf420000000p-1f,0x1.7f24de0000000p-2f},
{-0x1.d906bc0000000p-1f,0x1.87de2a0000000p-2f}, {-0x1.d733f60000000p-1f,0x1.908ef80000000p-2f}, {-0x1.d556f60000000p-1f,0x1.9937160000000p-2f}, {-0x1.d36fc80000000p-1f,0x1.a1d6540000000p-2f},
{-0x1.d17e780000000p-1f,0x1.aa6c820000000p-2f}, {-0x1.cf830e0000000p-1f,0x1.b2f9720000000p-2f}, {-0x1.cd7d980000000p-1f,0x1.bb7cf20000000p-2f}, {-0x1.cb6e200000000p-1f,0x1.c3f6d40000000p-2f},
{-0x1.c954b20000000p-1f,0x1.cc66ea0000000p-2f}, {-0x1.c731580000000p-1f,0x1.d4cd020000000p-2f}, {-0x1.c504200000000p-1f,0x1.dd28f20000000p-2f}, {-0x1.c2cd140000000p-1f,0x1.e57a860000000p-2f},
{-0x1.c08c420000000p-1f,0x1.edc1960000000p-2f}, {-0x1.be41b60000000p-1f,0x1.f5fdee0000000p-2f}, {-0x1.bbed7c0000000p-1f,0x1.fe2f640000000p-2f}, {-0x1.b98fa20000000p-1f,0x1.032ae60000000p-1f},
{-0x1.b728340000000p-1f,0x1.07387a0000000p-1f}, {-0x1.b4b7400000000p-1f,0x1.0b40580000000p-1f}, {-0x1.b23cd40000000p-1f,0x1.0f426c0000000p-1f}, {-0x1.afb8fe0000000p-1f,0x1.133e9c0000000p-1f},
{-0x1.ad2bca0000000p-1f,0x1.1734d60000000p-1f}, {-0x1.aa95480000000p-1f,0x1.1b25020000000p-1f}, {-0x1.a7f5860000000p-1f,0x1.1f0f080000000p-1f}, {-0x1.a54c920000000p-1f,0x1.22f2d60000000p-1f},
{-0x1.a29a7a0000000p-1f,0x1.26d0540000000p-1f}, {-0x1.9fdf500000000p-1f,0x1.2aa76e0000000p-1f}, {-0x1.9d1b200000000p-1f,0x1.2e780e0000000p-1f}, {-0x1.9a4dfa0000000p-1f,0x1.32421e0000000p-1f},
{-0x1.9777f00000000p-1f,0x1.36058c0000000p-1f}, {-0x1.94990e0000000p-1f,0x1.39c23e0000000p-1f}, {-0x1.91b1660000000p-1f,0x1.3d78240000000p-1f}, {-0x1.8ec10a0000000p-1f,0x1.4127260000000p-1f},
{-0x1.8bc8060000000p-1f,0x1.44cf320000000p-1f}, {-0x1.88c66e0000000p-1f,0x1.4870340000000p-1f}, {-0x1.85bc520000000p-1f,0x1.4c0a140000000p-1f}, {-0x1.82a9c20000000p-1f,0x1.4f9cc20000000p-1f},
{-0x1.7f8ece0000000p-1f,0x1.53282a0000000p-1f}, {-0x1.7c6b8a0000000p-1f,0x1.56ac360000000p-1f}, {-0x1.7940060000000p-1f,0x1.5a28d20000000p-1f}, {-0x1.760c520000000p-1f,0x1.5d9dee0000000p-1f},
{-0x1.72d0840000000p-1f,0x1.610b760000000p-1f}, {-0x1.6f8caa0000000p-1f,0x1.6471540000000p-1f}, {-0x1.6c40d80000000p-1f,0x1.67cf780000000p-1f}, {-0x1.68ed1e0000000p-1f,0x1.6b25ce0000000p-1f},
{-0x1.6591920000000p-1f,0x1.6e74460000000p-1f}, {-0x1.622e440000000p-1f,0x1.71baca0000000p-1f}, {-0x1.5ec34a0000000p-1f,0x1.74f9480000000p-1f}, {-0x1.5b50b20000000p-1f,0x1.782fb20000000p-1f},
{-0x1.57d6940000000p-1f,0x1.7b5df20000000p-1f}, {-0x1.5455000000000p-1f,0x1.7e83f80000000p-1f}, {-0x1.50cc0a0000000p-1f,0x1.81a1b40000000p-1f}, {-0x1.4d3bc60000000p-1f,0x1.84b7120000000p-1f},
{-0x1.49a44a0000000p-1f,0x1.87c4000000000p-1f}, {-0x1.4605a60000000p-1f,0x1.8ac8720000000p-1f}, {-0x1.425ff20000000p-1f,0x1.8dc4540000000p-1f}, {-0x1.3eb33e0000000p-1f,0x1.90b7940000000p-1f},
{-0x1.3affa20000000p-1f,0x1.93a2240000000p-1f}, {-0x1.3745320000000p-1f,0x1.9683f40000000p-1f}, {-0x1.3384000000000p-1f,0x1.995cf20000000p-1f}, {-0x1.2fbc240000000p-1f,0x1.9c2d120000000p-1f},
{-0x1.2bedb20000000p-1f,0x1.9ef43e0000000p-1f}, {-0x1.2818be0000000p-1f,0x1.a1b26e0000000p-1f}, {-0x1.243d600000000p-1f,0x1.a4678c0000000p-1f}, {-0x1.205baa0000000p-1f,0x1.a7138e0000000p-1f},
{-0x1.1c73b40000000p-1f,0x1.a9b6620000000p-1f}, {-0x1.1885920000000p-1f,0x1.ac4ffc0000000p-1f}, {-0x1.14915a0000000p-1f,0x1.aee04c0000000p-1f}, {-0x1.1097240000000p-1f,0x1.b167420000000p-1f},
{-0x1.0c97040000000p-1f,0x1.b3e4d40000000p-1f}, {-0x1.0891120000000p-1f,0x1.b658f20000000p-1f}, {-0x1.0485620000000p-1f,0x1.b8c38e0000000p-1f}, {-0x1.00740c0000000p-1f,0x1.bb249a0000000p-1f},
{-0x1.f8ba4e0000000p-2f,0x1.bd7c0a0000000p-1f}, {-0x1.f081900000000p-2f,0x1.bfc9d20000000p-1f}, {-0x1.e83e0e0000000p-2f,0x1.c20de40000000p-1f}, {-0x1.dfeff60000000p-2f,0x1.c448340000000p-1f},
{-0x1.d797760000000p-2f,0x1.c678b40000000p-1f}, {-0x1.cf34ba0000000p-2f,0x1.c89f580000000p-1f}, {-0x1.c6c7f40000000p-2f,0x1.cabc160000000p-1f}, {-0x1.be51520000000p-2f,0x1.cccee20000000p-1f},
{-0x1.b5d1000000000p-2f,0x1.ced7b00000000p-1f}, {-0x1.ad47320000000p-2f,0x1.d0d6720000000p-1f}, {-0x1.a4b4120000000p-2f,0x1.d2cb220000000p-1f}, {-0x1.9c17d40000000p-2f,0x1.d4b5b20000000p-1f},
{-0x1.9372a60000000p-2f,0x1.d696180000000p-1f}, {-0x1.8ac4b80000000p-2f,0x1.d86c480000000p-1f}, {-0x1.820e3c0000000p-2f,0x1.da383a0000000p-1f}, {-0x1.794f5e0000000p-2f,0x1.dbf9e40000000p-1f},
{-0x1.7088540000000p-2f,0x1.ddb13c0000000p-1f}, {-0x1.67b94a0000000p-2f,0x1.df5e360000000p-1f}, {-0x1.5ee2740000000p-2f,0x1.e100cc0000000p-1f}, {-0x1.5604020000000p-2f,0x1.e298f40000000p-1f},
{-0x1.4d1e240000000p-2f,0x1.e426a40000000p-1f}, {-0x1.44310e0000000p-2f,0x1.e5a9d60000000p-1f}, {-0x1.3b3cf00000000p-2f,0x1.e7227e0000000p-1f}, {-0x1.3241fc0000000p-2f,0x1.e890960000000p-1f},
{-0x1.2940620000000p-2f,0x1.e9f4160000000p-1f}, {-0x1.2038580000000p-2f,0x1.eb4cf60000000p-1f}, {-0x1.172a0e0000000p-2f,0x1.ec9b2e0000000p-1f}, {-0x1.0e15b40000000p-2f,0x1.eddeb60000000p-1f},
{-0x1.04fb800000000p-2f,0x1.ef178a0000000p-1f}, {-0x1.f7b7480000000p-3f,0x1.f045a20000000p-1f}, {-0x1.e56ca20000000p-3f,0x1.f168f60000000p-1f}, {-0x1.d317740000000p-3f,0x1.f281800000000p-1f},
{-0x1.c0b8260000000p-3f,0x1.f38f3a0000000p-1f}, {-0x1.ae4f1e0000000p-3f,0x1.f492200000000p-1f}, {-0x1.9bdcc00000000p-3f,0x1.f58a2c0000000p-1f}, {-0x1.8961720000000p-3f,0x1.f677560000000p-1f},
{-0x1.76dd9e0000000p-3f,0x1.f7599a0000000p-1f}, {-0x1.6451a80000000p-3f,0x1.f830f40000000p-1f}, {-0x1.51bdf80000000p-3f,0x1.f8fd600000000p-1f}, {-0x1.3f22f60000000p-3f,0x1.f9bed80000000p-1f},
{-0x1.2c81060000000p-3f,0x1.fa75580000000p-1f}, {-0x1.19d8940000000p-3f,0x1.fb20dc0000000p-1f}, {-0x1.072a040000000p-3f,0x1.fbc1620000000p-1f}, {-0x1.e8eb800000000p-4f,0x1.fc56e40000000p-1f},
{-0x1.c3785c0000000p-4f,0x1.fce1600000000p-1f}, {-0x1.9dfb6e0000000p-4f,0x1.fd60d20000000p-1f}, {-0x1.7875860000000p-4f,0x1.fdd53a0000000p-1f}, {-0x1.52e7740000000p-4f,0x1.fe3e920000000p-1f},
{-0x1.2d520a0000000p-4f,0x1.fe9cda0000000p-1f}, {-0x1.07b6140000000p-4f,0x1.fef0100000000p-1f}, {-0x1.c428d20000000p-5f,0x1.ff38300000000p-1f}, {-0x1.78dbaa0000000p-5f,0x1.ff753c0000000p-1f},
{-0x1.2d86580000000p-5f,0x1.ffa72e0000000p-1f}, {-0x1.c454f40000000p-6f,0x1.ffce0a0000000p-1f}, {-0x1.2d936c0000000p-6f,0x1.ffe9cc0000000p-1f}, {-0x1.2d96b00000000p-7f,0x1.fffa720000000p-1f},
};
typedef __attribute__((ext_vector_type(4))) unsigned u32x4;
typedef __attribute__((ext_vector_type(2))) unsigned u32x2;
#define WT_STORES 0
__device__ __forceinline__ void st16(void* p, uint4 v) {
  *reinterpret_cast<uint4*>(p) = v;
}
__device__ __forceinline__ void st8(void* p, uint2 v) {
  *reinterpret_cast<uint2*>(p) = v;
}
__device__ __forceinline__ void st16f(void* p, float4 v) {
  st16(p, make_uint4(__float_as_uint(v.x), __float_as_uint(v.y), __float_as_uint(v.z), __float_as_uint(v.w)));
}
__device__ __forceinline__ void st8f(void* p, float2 v) { st8(p, make_uint2(__float_as_uint(v.x), __float_as_uint(v.y))); }

__device__ __forceinline__ float bf2f(u16 v) { return __uint_as_float(((unsigned)v) << 16); }
__device__ __forceinline__ u16 f2bf(float f) {
  __hip_bfloat16 b = __float2bfloat16(f);
  return *reinterpret_cast<u16*>(&b);
}
__device__ __forceinline__ float bfr(float f) { return bf2f(f2bf(f)); }
__device__ __forceinline__ int row_of_pos(int p) { return 4 * (p & 511) + (p >> 9); }

__device__ __forceinline__ float2 cadd(float2 a, float2 b) { return make_float2(a.x + b.x, a.y + b.y); }
__device__ __forceinline__ float2 csub(float2 a, float2 b) { return make_float2(a.x - b.x, a.y - b.y); }
__device__ __forceinline__ float2 cmul(float2 a, float2 b) {
  return make_float2(a.x * b.x - a.y * b.y, a.x * b.y + a.y * b.x);
}
template <bool INV>
__device__ __forceinline__ float2 twmul(float2 a, float2 w) {
  if (INV) return make_float2(a.x * w.x + a.y * w.y, a.y * w.x - a.x * w.y);
  else return make_float2(a.x * w.x - a.y * w.y, a.x * w.y + a.y * w.x);
}
template <bool INV>
__device__ __forceinline__ float2 mul_mi(float2 a) {
  if (INV) return make_float2(-a.y, a.x);
  else return make_float2(a.y, -a.x);
}
template <bool INV>
__device__ __forceinline__ void fft4(float2& v0, float2& v1, float2& v2, float2& v3) {
  float2 a0 = cadd(v0, v2), a1 = csub(v0, v2);
  float2 b0 = cadd(v1, v3), b1 = mul_mi<INV>(csub(v1, v3));
  v0 = cadd(a0, b0);
  v2 = csub(a0, b0);
  v1 = cadd(a1, b1);
  v3 = csub(a1, b1);
}
template <bool INV>
__device__ __forceinline__ void fft8(float2 (&v)[8]) {
  const float r = 0.70710678118654752440f;
  float2 e0 = cadd(v[0], v[4]), o0 = csub(v[0], v[4]);
  float2 e1 = cadd(v[1], v[5]), o1 = csub(v[1], v[5]);
  float2 e2 = cadd(v[2], v[6]), o2 = csub(v[2], v[6]);
  float2 e3 = cadd(v[3], v[7]), o3 = csub(v[3], v[7]);
  if (INV) {
    o1 = make_float2((o1.x - o1.y) * r, (o1.x + o1.y) * r);
    o3 = make_float2((-o3.x - o3.y) * r, (o3.x - o3.y) * r);
  } else {
    o1 = make_float2((o1.x + o1.y) * r, (o1.y - o1.x) * r);
    o3 = make_float2((o3.y - o3.x) * r, (-o3.x - o3.y) * r);
  }
  o2 = mul_mi<INV>(o2);
  fft4<INV>(e0, e1, e2, e3);
  fft4<INV>(o0, o1, o2, o3);
  v[0] = e0; v[2] = e1; v[4] = e2; v[6] = e3;
  v[1] = o0; v[3] = o1; v[5] = o2; v[7] = o3;
}
__device__ __forceinline__ int padi(int i) { return i + (i >> 4); }

template <int CTRL>
__device__ __forceinline__ float dpp_mov(float v) {
  return __int_as_float(__builtin_amdgcn_update_dpp(0, __float_as_int(v), CTRL, 0xF, 0xF, true));
}
__device__ __forceinline__ float wave_sum(float v) {
  v += dpp_mov<0xB1>(v);
  v += dpp_mov<0x4E>(v);
  v += dpp_mov<0x141>(v);
  v += dpp_mov<0x140>(v);
  const float a = __int_as_float(__builtin_amdgcn_readlane(__float_as_int(v), 0));
  const float b = __int_as_float(__builtin_amdgcn_readlane(__float_as_int(v), 16));
  const float c = __int_as_float(__builtin_amdgcn_readlane(__float_as_int(v), 32));
  const float d = __int_as_float(__builtin_amdgcn_readlane(__float_as_int(v), 48));
  return (a + b) + (c + d);
}

__device__ __forceinline__ void ln_rows2(float (&xa)[8], float (&xb)[8]) {
  float sa = 0.f, sb = 0.f;
#pragma unroll
  for (int m = 0; m < 8; ++m) { sa += xa[m]; sb += xb[m]; }
  sa = wave_sum(sa);
  sb = wave_sum(sb);
  const float ma = sa * (1.0f / 512.0f), mbn = sb * (1.0f / 512.0f);
  float qa = 0.f, qb = 0.f;
#pragma unroll
  for (int m = 0; m < 8; ++m) {
    float da = xa[m] - ma, db = xb[m] - mbn;
    qa += da * da; qb += db * db;
  }
  qa = wave_sum(qa);
  qb = wave_sum(qb);
  const float mab = bfr(ma), mbb = bfr(mbn);
  const float dena = bfr(sqrtf(bfr(bfr(qa * (1.0f / 512.0f)) + 1e-5f)));
  const float denb = bfr(sqrtf(bfr(bfr(qb * (1.0f / 512.0f)) + 1e-5f)));
  const float ra = __builtin_amdgcn_rcpf(dena), rb = __builtin_amdgcn_rcpf(denb);
#pragma unroll
  for (int m = 0; m < 8; ++m) {
    const float na = bfr(xa[m] - mab), nb = bfr(xb[m] - mbb);
    float q0 = na * ra, q1 = nb * rb;
    q0 = fmaf(fmaf(-q0, dena, na), ra, q0);
    q1 = fmaf(fmaf(-q1, denb, nb), rb, q1);
    xa[m] = bfr(q0);
    xb[m] = bfr(q1);
  }
}

template <bool INV>
__device__ __forceinline__ void wave_fft512(float2 (&v)[8], float2* ex, int j, const float2 (&tw1)[7],
                                            const float2 (&tw2)[7]) {
  fft8<INV>(v);
#pragma unroll
  for (int m = 0; m < 8; ++m) ex[padi(j * 8 + m)] = v[m];
  __builtin_amdgcn_wave_barrier();
#pragma unroll
  for (int m = 0; m < 8; ++m) v[m] = ex[padi(j + 64 * m)];
  __builtin_amdgcn_wave_barrier();
#pragma unroll
  for (int m = 1; m < 8; ++m) v[m] = twmul<INV>(v[m], tw1[m - 1]);
  fft8<INV>(v);
#pragma unroll
  for (int m = 0; m < 8; ++m) ex[padi((j >> 3) * 64 + (j & 7) + 8 * m)] = v[m];
  __builtin_amdgcn_wave_barrier();
#pragma unroll
  for (int m = 0; m < 8; ++m) v[m] = ex[padi(j + 64 * m)];
  __builtin_amdgcn_wave_barrier();
#pragma unroll
  for (int m = 1; m < 8; ++m) v[m] = twmul<INV>(v[m], tw2[m - 1]);
  fft8<INV>(v);
}

#define CNT_STRIDE_ 32
#define EXW 544
#define TSTR 10

__device__ __forceinline__ void transpose8x8_store(const uint4 (&r)[8], uint4* __restrict__ dst) {
#pragma unroll
  for (int c = 0; c < 8; ++c) {
    unsigned o[4];
#pragma unroll
    for (int d = 0; d < 4; ++d) {
      const unsigned* a = reinterpret_cast<const unsigned*>(&r[2 * d]);
      const unsigned* b = reinterpret_cast<const unsigned*>(&r[2 * d + 1]);
      o[d] = __builtin_amdgcn_perm(b[c >> 1], a[c >> 1], (c & 1) ? 0x07060302u : 0x05040100u);
    }
    st16(dst + c, make_uint4(o[0], o[1], o[2], o[3]));
  }
}
#define NG_GU (172 * 64 * 2)
#define NG_D (64 * 2 * NT16)
#define SIDE_W ((NG_GU + NG_D + 255) / 256)

__global__ __launch_bounds__(256) void k1_ln_rfft(const u16* __restrict__ x, const u16* __restrict__ wr,
                                                  const u16* __restrict__ wi, float2* __restrict__ spec,
                                                  unsigned* __restrict__ wt, unsigned* __restrict__ cnt) {
  __shared__ __attribute__((aligned(16))) float2 sm[4 * EXW + 256 * TSTR];
  const int t = threadIdx.x;
  if (blockIdx.x >= 256) {
    const int m0 = (blockIdx.x - 256) * 8;
    if (blockIdx.x == 256 && t < 64) cnt[t * CNT_STRIDE_] = 0u;
    unsigned p[8];
#pragma unroll
    for (int r = 0; r < 8; ++r)
      p[r] = (unsigned)wr[(m0 + r) * DF + t] | ((unsigned)wi[(m0 + r) * DF + t] << 16);
    uint4* dst = reinterpret_cast<uint4*>(wt + t * S_LEN + m0);
    st16(dst, make_uint4(p[0], p[1], p[2], p[3]));
    st16(dst + 1, make_uint4(p[4], p[5], p[6], p[7]));
    if (t < 8) {
      const int f = m0 + t, fn = (S_LEN - f) & (S_LEN - 1);
      const float g0x = 0.5f * (bf2f(wr[f * DF]) + bf2f(wr[fn * DF])), g0y = 0.5f * (bf2f(wi[f * DF]) - bf2f(wi[fn * DF]));
      const float g1x = 0.5f * (bf2f(wr[f * DF + 256]) + bf2f(wr[fn * DF + 256]));
      const float g1y = 0.5f * (bf2f(wi[f * DF + 256]) - bf2f(wi[fn * DF + 256]));
      reinterpret_cast<float4*>(wt + 256 * S_LEN)[f] =
          make_float4(0.5f * (g0x + g1x), 0.5f * (g0y + g1y), 0.5f * (g0x - g1x), 0.5f * (g0y - g1y));
    }
    return;
  }
  const int w = t >> 6, j = t & 63;
  const int p0 = blockIdx.x * 8;
  const int ra = row_of_pos(p0 + 2 * w), rb = row_of_pos(p0 + 2 * w + 1);
  float2* ex = sm + w * EXW;
  float2* tile = sm + 4 * EXW;

  float xa[8], xb[8];
#pragma unroll
  for (int m = 0; m < 8; ++m) {
    xa[m] = bf2f(x[ra * D_DIM + j + 64 * m]);
    xb[m] = bf2f(x[rb * D_DIM + j + 64 * m]);
  }
  float2 tw1[7], tw2[7];
#pragma unroll
  for (int m = 1; m < 8; ++m) {
    tw1[m - 1] = g_twl[(m - 1) * 64 + j];
    tw2[m - 1] = g_twl[(m + 6) * 64 + j];
  }
  ln_rows2(xa, xb);
  float2 v[8];
#pragma unroll
  for (int m = 0; m < 8; ++m) v[m] = make_float2(xa[m], xb[m]);
  wave_fft512<false>(v, ex, j, tw1, tw2);
  __builtin_amdgcn_wave_barrier();
#pragma unroll
  for (int m = 4; m < 8; ++m) ex[padi(j + 64 * m)] = v[m];
  if (j == 0) ex[padi(0)] = v[0];
  __builtin_amdgcn_wave_barrier();
#pragma unroll
  for (int m = 0; m < 4; ++m) {
    const int k = j + 64 * m;
    const float2 zc = ex[padi((512 - k) & 511)];
    float4 o;
    if (k == 0) {
      const float2 zn = ex[padi(256)];
      o = make_float4(v[0].x, zn.x, v[0].y, zn.y);
    } else {
      const float2 z = v[m];
      o.x = 0.5f * (z.x + zc.x);
      o.y = 0.5f * (z.y - zc.y);
      o.z = 0.5f * (z.y + zc.y);
      o.w = 0.5f * (zc.x - z.x);
    }
    *reinterpret_cast<float4*>(tile + k * TSTR + 2 * w) = o;
  }
  __syncthreads();
#pragma unroll
  for (int i = 0; i < 4; ++i) {
    const int idx = t + 256 * i;
    const int k = idx >> 2, part = idx & 3;
    const float4 o = *reinterpret_cast<const float4*>(tile + k * TSTR + 2 * part);
    st16f(spec + k * S_LEN + p0 + 2 * part, o);
  }
}

__device__ __forceinline__ float2 unpack_w(unsigned p) {
  return make_float2(__uint_as_float(p << 16), __uint_as_float(p & 0xffff0000u));
}

__global__ __launch_bounds__(256) void k2_colfft(float2* __restrict__ spec, const unsigned* __restrict__ wt,
                                                 const u16* __restrict__ wg, const u16* __restrict__ wu,
                                                 const u16* __restrict__ wd, uint4* __restrict__ wgu_t,
                                                 uint4* __restrict__ wd_t) {
  __shared__ __attribute__((aligned(16))) float2 sm[4 * EXW + 2 * 2048];
  const int t = threadIdx.x;
  if (blockIdx.x >= 256) {
    const int g = (blockIdx.x - 256) * 256 + t;
    uint4 r[8];
    if (g < NG_GU) {
      const int n8 = g % 172, kb = (g / 172) & 63, which = g / (172 * 64);
      const u16* src = (which ? wu : wg) + (8 * kb) * F_DIM + 8 * n8;
#pragma unroll
      for (int i = 0; i < 8; ++i) r[i] = *reinterpret_cast<const uint4*>(src + i * F_DIM);
      transpose8x8_store(r, wgu_t + ((n8 >> 1) * 32 + (kb >> 1)) * 64 + 32 * (kb & 1) + 16 * which + 8 * (n8 & 1));
    } else if (g < NG_GU + NG_D) {
      const int g2 = g - NG_GU;
      const int n8 = g2 & 63, hh = (g2 >> 6) & 1, u = g2 >> 7;
      const u16* src = wd + (16 * u + 4 * hh) * D_DIM + 8 * n8;
#pragma unroll
      for (int i = 0; i < 8; ++i) r[i] = *reinterpret_cast<const uint4*>(src + ((i & 3) + 8 * (i >> 2)) * D_DIM);
      transpose8x8_store(r, wd_t + ((n8 >> 2) * NT16 + u) * 64 + 32 * hh + 8 * (n8 & 3));
    }
    return;
  }
  const int k = blockIdx.x;
  const int w = t >> 6, j = t & 63;
  float2* ex = sm + w * EXW;
  float2* xb1 = sm + 4 * EXW;
  float2* xb2 = xb1 + 2048;
  float2* col = spec + k * S_LEN;
  float2 v[8];
#pragma unroll
  for (int m = 0; m < 8; ++m) v[m] = col[w * 512 + j + 64 * m];
  const int kA = (k == 0) ? t : (j + 128 * w);
  const int kB = (k == 0) ? (t ? 512 - t : 256) : (kA + 64);
  float2 tw1[7], tw2[7], twa[3], twb[3];
#pragma unroll
  for (int m = 1; m < 8; ++m) {
    tw1[m - 1] = g_twl[(m - 1) * 64 + j];
    tw2[m - 1] = g_twl[(m + 6) * 64 + j];
  }
#pragma unroll
  for (int ww = 1; ww < 4; ++ww) {
    twa[ww - 1] = g_twx[(ww - 1) * 512 + kA];
    twb[ww - 1] = g_twx[(ww - 1) * 512 + kB];
  }
  unsigned wp[8];
  float4 pq[8];
  if (k != 0) {
#pragma unroll
    for (int q = 0; q < 4; ++q) {
      wp[q] = wt[k * S_LEN + kA + 512 * q];
      wp[4 + q] = wt[k * S_LEN + kB + 512 * q];
    }
  } else {
#pragma unroll
    for (int q = 0; q < 4; ++q) {
      pq[q] = reinterpret_cast<const float4*>(wt + 256 * S_LEN)[kA + 512 * q];
      pq[4 + q] = reinterpret_cast<const float4*>(wt + 256 * S_LEN)[kB + 512 * q];
    }
  }
  wave_fft512<false>(v, ex, j, tw1, tw2);
#pragma unroll
  for (int m = 0; m < 8; ++m) xb1[w * 512 + m * 64 + j] = v[m];
  __syncthreads();
  float2 a[4], b[4];
#pragma unroll
  for (int ww = 0; ww < 4; ++ww) {
    a[ww] = xb1[ww * 512 + kA];
    b[ww] = xb1[ww * 512 + kB];
  }
#pragma unroll
  for (int ww = 1; ww < 4; ++ww) {
    a[ww] = twmul<false>(a[ww], twa[ww - 1]);
    b[ww] = twmul<false>(b[ww], twb[ww - 1]);
  }
  fft4<false>(a[0], a[1], a[2], a[3]);
  fft4<false>(b[0], b[1], b[2], b[3]);
  if (k != 0) {
#pragma unroll
    for (int q = 0; q < 4; ++q) {
      a[q] = cmul(a[q], unpack_w(wp[q]));
      b[q] = cmul(b[q], unpack_w(wp[4 + q]));
    }
  } else {
    float2 na[4], nb[4];
#pragma unroll
    for (int q = 0; q < 4; ++q) {
      const float2 za = (t == 0) ? a[(4 - q) & 3] : b[3 - q];
      const float2 zb = (t == 0) ? b[3 - q] : a[3 - q];
      na[q] = cadd(cmul(a[q], make_float2(pq[q].x, pq[q].y)), cmul(make_float2(za.x, -za.y), make_float2(pq[q].z, pq[q].w)));
      nb[q] = cadd(cmul(b[q], make_float2(pq[4 + q].x, pq[4 + q].y)), cmul(make_float2(zb.x, -zb.y), make_float2(pq[4 + q].z, pq[4 + q].w)));
    }
#pragma unroll
    for (int q = 0; q < 4; ++q) { a[q] = na[q]; b[q] = nb[q]; }
  }
  fft4<true>(a[0], a[1], a[2], a[3]);
  fft4<true>(b[0], b[1], b[2], b[3]);
#pragma unroll
  for (int ww = 1; ww < 4; ++ww) {
    a[ww] = twmul<true>(a[ww], twa[ww - 1]);
    b[ww] = twmul<true>(b[ww], twb[ww - 1]);
  }
#pragma unroll
  for (int ww = 0; ww < 4; ++ww) {
    xb2[ww * 512 + kA] = a[ww];
    xb2[ww * 512 + kB] = b[ww];
  }
  __syncthreads();
#pragma unroll
  for (int m = 0; m < 8; ++m) v[m] = xb2[w * 512 + m * 64 + j];
  wave_fft512<true>(v, ex, j, tw1, tw2);
#pragma unroll
  for (int m = 0; m < 8; ++m) st8f(col + w * 512 + j + 64 * m, v[m]);
}

#define GAS __attribute__((address_space(1)))
typedef GAS unsigned gu32;
#define SPIN_CAP (1u << 19)
#define CNT_STRIDE 32

__device__ __forceinline__ unsigned geglu2(float g0, float g1, float u0, float u1) {
  g0 = bfr(g0); g1 = bfr(g1); u0 = bfr(u0); u1 = bfr(u1);
  const float c1 = -2.0f * 0.7978845608028654f * 1.4426950408889634f;
  const float c2 = c1 * 0.044715f;
  const float z0 = g0 * fmaf(g0 * g0, c2, c1), z1 = g1 * fmaf(g1 * g1, c2, c1);
  const float e0 = __builtin_amdgcn_exp2f(z0), e1 = __builtin_amdgcn_exp2f(z1);
  const float a0 = bfr(g0 * __builtin_amdgcn_rcpf(1.0f + e0));
  const float a1 = bfr(g1 * __builtin_amdgcn_rcpf(1.0f + e1));
  return (unsigned)f2bf(a0 * u0) | ((unsigned)f2bf(a1 * u1) << 16);
}
__device__ __forceinline__ void publish_add(unsigned* cnt, int t) {
  asm volatile("s_waitcnt vmcnt(0)" ::: "memory");
  __syncthreads();
  if (t == 0) __hip_atomic_fetch_add((gu32*)cnt, 1u, __ATOMIC_RELAXED, __HIP_MEMORY_SCOPE_AGENT);
}
__device__ __forceinline__ void wait_count(unsigned* cnt, unsigned want, int w) {
  if (w == 0) {
    unsigned spins = 0;
    while ((unsigned)__builtin_amdgcn_readfirstlane(__hip_atomic_load((gu32*)cnt, __ATOMIC_RELAXED, __HIP_MEMORY_SCOPE_AGENT)) < want) {
      __builtin_amdgcn_s_sleep(1);
      if (++spins > SPIN_CAP) break;
    }
  }
  __builtin_amdgcn_fence(__ATOMIC_ACQUIRE, "wavefront");
  __syncthreads();
}
__device__ __forceinline__ uint4 ld_sc1(__amdgpu_buffer_rsrc_t rs, unsigned off) {
  u32x4 v = __builtin_amdgcn_raw_buffer_load_b128(rs, off, 0, 16);
  return make_uint4(v.x, v.y, v.z, v.w);
}
__device__ __forceinline__ void st_sc1(__amdgpu_buffer_rsrc_t rs, unsigned off, uint4 v) {
  u32x4 x = {v.x, v.y, v.z, v.w};
  __builtin_amdgcn_raw_buffer_store_b128(x, rs, off, 0, 16);
}
__device__ __forceinline__ void st_sc1_8(__amdgpu_buffer_rsrc_t rs, unsigned off, uint2 v) {
  u32x2 x = {v.x, v.y};
  __builtin_amdgcn_raw_buffer_store_b64(x, rs, off, 0, 16);
}

#define MAPV 1
#define GSLEEP 0
#define F_PRE 0
#define WD_MODE 0
#define PSTR 68
typedef __attribute__((ext_vector_type(16))) float f32x16;
#define MFMA32(a, b, c) __builtin_amdgcn_mfma_f32_32x32x16_bf16(*reinterpret_cast<const bf16x8*>(&(a)), *reinterpret_cast<const bf16x8*>(&(b)), (c), 0, 0, 0)
#define SB() __builtin_amdgcn_sched_barrier(0)
#define F_PFA 16
#define F_PFB 14

template <int NU, int PF>
__device__ __forceinline__ void k345_tail(char* smem, uint4 (&bw)[32][NU > 0 ? NU : 1], const uint4* __restrict__ bp,
                                          const uint4* __restrict__ wd_t, __amdgpu_buffer_rsrc_t rsH,
                                          __amdgpu_buffer_rsrc_t rsX, __amdgpu_buffer_rsrc_t rsA,
                                          u16* __restrict__ out, unsigned* cnt, int mt64, int ng, int u0, int t,
                                          int w, int j) {
  uint4* As = reinterpret_cast<uint4*>(smem);
  wait_count(cnt + mt64 * CNT_STRIDE, 8u, w ^ 7);
  {
    uint4 at[8];
#pragma unroll
    for (int i = 0; i < 8; ++i) at[i] = ld_sc1(rsX, (unsigned)(mt64 * 65536 + (i * 512 + t) * 16));
#pragma unroll
    for (int i = 0; i < 8; ++i) As[i * 512 + t] = at[i];
  }
  __syncthreads();
  const int ub = 11 * w - (w > 6 ? w - 6 : 0);
  const bool full = (w < 6);
  const uint4* dp = wd_t + ((size_t)(ng * 2) * NT16 + ub) * 64 + j;
  uint4 df[11][2];
  f32x16 acc1[NU > 0 ? NU : 1][2];
  {
#pragma unroll
    for (int n = 0; n < NU; ++n)
#pragma unroll
      for (int mi = 0; mi < 2; ++mi)
#pragma unroll
        for (int i = 0; i < 16; ++i) acc1[n][mi][i] = 0.f;
    __builtin_amdgcn_s_setprio(3);
    uint4 a[32][2];
#pragma unroll
    for (int s = 0; s < 2; ++s)
#pragma unroll
      for (int mi = 0; mi < 2; ++mi) a[s][mi] = As[(mi * 32 + s) * 64 + j];
#pragma unroll
    for (int s = 0; s < 32; ++s) {
      if (s + 2 < 32) {
#pragma unroll
        for (int mi = 0; mi < 2; ++mi) a[s + 2][mi] = As[(mi * 32 + s + 2) * 64 + j];
      }
      if (s + PF < 32) {
#pragma unroll
        for (int n = 0; n < NU; ++n) bw[s + PF][n] = bp[(n * 32 + s + PF) * 64];
      } else if (WD_MODE == 0 && s >= 21) {
        const int d = s - 21;
        if (d < 10 || full) {
#pragma unroll
          for (int i = 0; i < 2; ++i) df[d][i] = dp[(i * NT16 + d) * 64];
        }
      }
      SB();
#pragma unroll
      for (int n = 0; n < NU; ++n)
#pragma unroll
        for (int mi = 0; mi < 2; ++mi) acc1[n][mi] = MFMA32(bw[s][n], a[s][mi], acc1[n][mi]);
      SB();
    }
    __builtin_amdgcn_s_setprio(0);
  }
  if (NU > 0) {
#pragma unroll
    for (int n = 0; n < NU; ++n) {
#pragma unroll
      for (int mi = 0; mi < 2; ++mi) {
        unsigned o[4];
#pragma unroll
        for (int i = 0; i < 4; ++i)
          o[i] = geglu2(acc1[n][mi][2 * i], acc1[n][mi][2 * i + 1], acc1[n][mi][8 + 2 * i], acc1[n][mi][9 + 2 * i]);
        st_sc1(rsA, (unsigned)((((mt64 * 2 + mi) * NT16 + u0 + n) * 64 + j) * 16), make_uint4(o[0], o[1], o[2], o[3]));
      }
    }
  }
  if (WD_MODE == 1) {
#pragma unroll
    for (int d = 0; d < 11; ++d) {
      if (d < 10 || full) {
#pragma unroll
        for (int i = 0; i < 2; ++i) df[d][i] = dp[(i * NT16 + d) * 64];
      }
      SB();
    }
  }
  publish_add(cnt + (32 + mt64) * CNT_STRIDE, t);
  if (WD_MODE == 2) {
#pragma unroll
    for (int d = 0; d < 11; ++d) {
      if (d < 10 || full) {
#pragma unroll
        for (int i = 0; i < 2; ++i) df[d][i] = dp[(i * NT16 + d) * 64];
      }
      SB();
    }
  }
  wait_count(cnt + (32 + mt64) * CNT_STRIDE, 8u, w);
  f32x16 acc[2][2];
#pragma unroll
  for (int a = 0; a < 2; ++a)
#pragma unroll
    for (int b = 0; b < 2; ++b)
#pragma unroll
      for (int i = 0; i < 16; ++i) acc[a][b][i] = 0.f;
  uint4 hv;
  {
    uint4 af[11][2];
#pragma unroll
    for (int s = 0; s < 11; ++s) {
      if (s < 10 || full) {
#pragma unroll
        for (int i = 0; i < 2; ++i)
          af[s][i] = ld_sc1(rsA, (unsigned)((((mt64 * 2 + i) * NT16 + ub + s) * 64 + j) * 16));
      }
      SB();
    }
    hv = ld_sc1(rsH, (unsigned)(((mt64 * 64 + (t >> 3)) * D_DIM + ng * 64 + (t & 7) * 8) * 2));
#pragma unroll
    for (int s = 0; s < 11; ++s) {
      if (s < 10 || full) {
#pragma unroll
        for (int mi = 0; mi < 2; ++mi)
#pragma unroll
          for (int ni = 0; ni < 2; ++ni) acc[mi][ni] = MFMA32(df[s][ni], af[s][mi], acc[mi][ni]);
      }
    }
  }
  float* part = reinterpret_cast<float*>(smem);
  float* pw = part + w * 64 * PSTR;
#pragma unroll
  for (int mi = 0; mi < 2; ++mi)
#pragma unroll
    for (int ni = 0; ni < 2; ++ni)
#pragma unroll
      for (int g = 0; g < 4; ++g) {
        f32x4 v4 = {acc[mi][ni][4 * g], acc[mi][ni][4 * g + 1], acc[mi][ni][4 * g + 2], acc[mi][ni][4 * g + 3]};
        *reinterpret_cast<f32x4*>(pw + (32 * mi + (j & 31)) * PSTR + 32 * ni + 8 * g + 4 * (j >> 5)) = v4;
      }
  __syncthreads();
  {
    const int er = t >> 3, ec = (t & 7) * 8;
    f32x4 s0 = {0.f, 0.f, 0.f, 0.f}, s1 = {0.f, 0.f, 0.f, 0.f};
#pragma unroll
    for (int ww = 0; ww < 8; ++ww) {
      s0 += *reinterpret_cast<const f32x4*>(part + ww * 64 * PSTR + er * PSTR + ec);
      s1 += *reinterpret_cast<const f32x4*>(part + ww * 64 * PSTR + er * PSTR + ec + 4);
    }
    const unsigned hw[4] = {hv.x, hv.y, hv.z, hv.w};
    unsigned ow[4];
#pragma unroll
    for (int i = 0; i < 4; ++i) {
      const float d0 = bfr(i < 2 ? s0[2 * i] : s1[2 * i - 4]);
      const float d1 = bfr(i < 2 ? s0[2 * i + 1] : s1[2 * i - 3]);
      const float h0 = __uint_as_float(hw[i] << 16), h1 = __uint_as_float(hw[i] & 0xffff0000u);
      ow[i] = (unsigned)f2bf(h0 + d0) | ((unsigned)f2bf(h1 + d1) << 16);
    }
    *reinterpret_cast<uint4*>(out + (size_t)row_of_pos(mt64 * 64 + er) * D_DIM + ng * 64 + ec) = make_uint4(ow[0], ow[1], ow[2], ow[3]);
  }
}

#define K345_LDS (8 * 64 * PSTR * 4)
__global__ __launch_bounds__(512, 2) void k345(const float2* __restrict__ spec, const u16* __restrict__ x,
                                               const uint4* __restrict__ wgu_t, const uint4* __restrict__ wd_t,
                                               u16* h, u16* xn_t, uint4* act_t, u16* __restrict__ out,
                                               unsigned* cnt) {
  __shared__ __attribute__((aligned(16))) char smem[K345_LDS];
  const int t = threadIdx.x, w = __builtin_amdgcn_readfirstlane(t >> 6), j = t & 63;
  const int bid = blockIdx.x;
  const int xcd = bid & 7, idx = bid >> 3;
  const int mt64 = (xcd >> 1) * 8 + (idx & 7);
  const int ng = (xcd & 1) * 4 + (idx >> 3);
  int start, cnt_t;
  if (ng < 6) { start = 11 * ng; cnt_t = 11; } else { start = 66 + 10 * (ng - 6); cnt_t = 10; }
  const __amdgpu_buffer_rsrc_t rsH = __builtin_amdgcn_make_buffer_rsrc((void*)h, 0, S_LEN * D_DIM * 2, 0x00020000);
  const __amdgpu_buffer_rsrc_t rsX = __builtin_amdgcn_make_buffer_rsrc((void*)xn_t, 0, S_LEN * D_DIM * 2, 0x00020000);
  const __amdgpu_buffer_rsrc_t rsA = __builtin_amdgcn_make_buffer_rsrc((void*)act_t, 0, 64 * NT16 * 1024, 0x00020000);
  if (w < 4) {
    float2* ex = reinterpret_cast<float2*>(smem + 65536) + w * EXW;
    float2* tile = reinterpret_cast<float2*>(smem + 65536) + 4 * EXW;
    const int p0 = mt64 * 64 + ng * 8;
    const int ra = row_of_pos(p0 + 2 * w), rb = row_of_pos(p0 + 2 * w + 1);
    const int u0 = start + w;
    const uint4* bp = wgu_t + (size_t)u0 * 32 * 64 + j;
    uint4 bw[32][1];
    float4 ld[4];
#pragma unroll
    for (int i = 0; i < 4; ++i) {
      const int id = t + 256 * i;
      ld[i] = *reinterpret_cast<const float4*>(spec + (id >> 2) * S_LEN + p0 + 2 * (id & 3));
    }
    u16 xra[8], xrb[8];
#pragma unroll
    for (int m = 0; m < 8; ++m) {
      xra[m] = x[ra * D_DIM + j + 64 * m];
      xrb[m] = x[rb * D_DIM + j + 64 * m];
    }
    float2 tw1[7], tw2[7];
#pragma unroll
    for (int m = 1; m < 8; ++m) {
      tw1[m - 1] = g_twl[(m - 1) * 64 + j];
      tw2[m - 1] = g_twl[(m + 6) * 64 + j];
    }
#pragma unroll
    for (int i = 0; i < 4; ++i) {
      const int id = t + 256 * i;
      *reinterpret_cast<float4*>(tile + (id >> 2) * TSTR + 2 * (id & 3)) = ld[i];
    }
    __syncthreads();
    float xa[8], xb[8];
    {
      float2 v[8];
#pragma unroll
      for (int m = 0; m < 8; ++m) {
        const int n = j + 64 * m;
        const int k = (m < 4) ? n : (512 - n);
        const float4 y = *reinterpret_cast<const float4*>(tile + (k & 255) * TSTR + 2 * w);
        if (n == 0) v[m] = make_float2(y.x, y.z);
        else if (n == 256) v[m] = make_float2(y.y, y.w);
        else if (m < 4) v[m] = make_float2(y.x - y.w, y.y + y.z);
        else v[m] = make_float2(y.x + y.w, y.z - y.y);
      }
      wave_fft512<true>(v, ex, j, tw1, tw2);
      const float sc = 1.0f / (2048.0f * 512.0f);
#pragma unroll
      for (int m = 0; m < 8; ++m) {
        xa[m] = bfr(bf2f(xra[m]) + bfr(v[m].x * sc));
        xb[m] = bfr(bf2f(xrb[m]) + bfr(v[m].y * sc));
      }
    }
    {
      u16* st_h = reinterpret_cast<u16*>(ex);
      u16* st_x = st_h + 2 * 512;
      __builtin_amdgcn_wave_barrier();
#pragma unroll
      for (int m = 0; m < 8; ++m) {
        st_h[j + 64 * m] = f2bf(xa[m]);
        st_h[512 + j + 64 * m] = f2bf(xb[m]);
      }
      ln_rows2(xa, xb);
#pragma unroll
      for (int m = 0; m < 8; ++m) {
        st_x[j + 64 * m] = f2bf(xa[m]);
        st_x[512 + j + 64 * m] = f2bf(xb[m]);
      }
      __builtin_amdgcn_wave_barrier();
      const int pr = p0 + 2 * w;
      const unsigned xo = (unsigned)((((pr >> 5) * 32 + (j >> 1)) * 64 + 32 * (j & 1) + (pr & 31)) * 16);
      st_sc1(rsX, xo, reinterpret_cast<const uint4*>(st_x)[j]);
      st_sc1(rsX, xo + 16, reinterpret_cast<const uint4*>(st_x)[64 + j]);
    }
    publish_add(cnt + mt64 * CNT_STRIDE, t);
    {
      const uint4* st_h = reinterpret_cast<const uint4*>(ex);
      const int pr = p0 + 2 * w;
      st_sc1(rsH, (unsigned)(pr * D_DIM * 2 + j * 16), st_h[j]);
      st_sc1(rsH, (unsigned)(pr * D_DIM * 2 + 1024 + j * 16), st_h[64 + j]);
    }
    SB();
#pragma unroll
    for (int s = 0; s < F_PFA; ++s) bw[s][0] = bp[s * 64];
    SB();
    k345_tail<1, F_PFA>(smem, bw, bp, wd_t, rsH, rsX, rsA, out, cnt, mt64, ng, u0, t, w, j);
  } else {
    const int nu = (cnt_t == 11) ? ((w < 7) ? 2 : 1) : ((w < 6) ? 2 : 1);
    const int u0 = start + 4 + ((cnt_t == 11 || w < 7) ? 2 * (w - 4) : 5);
    const uint4* bp = wgu_t + (size_t)u0 * 32 * 64 + j;
    if (nu == 2) {
      uint4 bw[32][2];
#pragma unroll
      for (int s = 0; s < F_PRE; ++s) {
        bw[s][0] = bp[s * 64];
        bw[s][1] = bp[(32 + s) * 64];
        SB();
      }
      __syncthreads();
#pragma unroll
      for (int s = F_PRE; s < F_PFB; ++s) {
        bw[s][0] = bp[s * 64];
        bw[s][1] = bp[(32 + s) * 64];
        SB();
      }
      __syncthreads();
      k345_tail<2, F_PFB>(smem, bw, bp, wd_t, rsH, rsX, rsA, out, cnt, mt64, ng, u0, t, w, j);
    } else {
      uint4 bw[32][1];
#pragma unroll
      for (int s = 0; s < F_PRE; ++s) { bw[s][0] = bp[s * 64]; SB(); }
      __syncthreads();
#pragma unroll
      for (int s = F_PRE; s < F_PFA; ++s) { bw[s][0] = bp[s * 64]; SB(); }
      __syncthreads();
      k345_tail<1, F_PFA>(smem, bw, bp, wd_t, rsH, rsX, rsA, out, cnt, mt64, ng, u0, t, w, j);
    }
  }
}

extern "C" void kernel_launch(void* const* d_in, const int* in_sizes, int n_in, void* d_out, int out_size,
                              void* d_ws, size_t ws_size, hipStream_t stream) {
  const u16* x = (const u16*)d_in[0];
  const u16* wr = (const u16*)d_in[1];
  const u16* wi = (const u16*)d_in[2];
  const u16* wg = (const u16*)d_in[4];
  const u16* wu = (const u16*)d_in[5];
  const u16* wd = (const u16*)d_in[6];
  u16* out = (u16*)d_out;
  char* ws = (char*)d_ws;
  size_t off = 0;
  float2* spec = (float2*)(ws + off); off += (size_t)256 * S_LEN * 8;
  unsigned* wt = (unsigned*)(ws + off); off += (size_t)260 * S_LEN * 4;
  u16* h = (u16*)(ws + off); off += (size_t)S_LEN * D_DIM * 2;
  u16* xn_t = (u16*)(ws + off); off += (size_t)S_LEN * D_DIM * 2;
  uint4* wgu_t = (uint4*)(ws + off); off += (size_t)NT16 * 16 * 2 * 1024;
  uint4* wd_t = (uint4*)(ws + off); off += (size_t)32 * KS2 * 1024;
  uint4* act_t = (uint4*)(ws + off); off += (size_t)128 * KS2 * 1024;

  unsigned* cnt = (unsigned*)(ws + off); off += 64 * CNT_STRIDE_ * 4;
  k1_ln_rfft<<<512, 256, 0, stream>>>(x, wr, wi, spec, wt, cnt);
  k2_colfft<<<256 + SIDE_W, 256, 0, stream>>>(spec, wt, wg, wu, wd, wgu_t, wd_t);
  k345<<<256, 512, 0, stream>>>(spec, x, wgu_t, wd_t, h, xn_t, act_t, out, cnt);
}
```
